```python
import torch

def _softmax(x, axis=-1):
    return torch.softmax(x, dim=axis)
def _gelu(x):
    return torch.nn.functional.gelu(x, approximate="tanh")
def _silu(x):
    return torch.nn.functional.silu(x)
def _sigmoid(x):
    return torch.sigmoid(x)
def _layernorm(x, axis=-1, eps=1e-5):
    m = x.mean(dim=axis, keepdim=True)
    v = x.var(dim=axis, keepdim=True, unbiased=False)
    return (x - m) / torch.sqrt(v + eps)
def _scatter_add(tgt, idx, src, axis=0):
    return tgt.clone().index_add_(axis, idx, src)
def _segment_sum(src, seg, n):
    out = torch.zeros((n,) + src.shape[1:], dtype=src.dtype)
    return out.index_add_(0, seg, src)
def _pad(x, pw):
    flat = tuple(v for pair in reversed(pw) for v in pair)
    return torch.nn.functional.pad(x, flat)

kernel_name = 'gated_mix__spectral_fnet__dense_activation_relu__prompt'

def setup_inputs():

    _w = (lambda a: (lambda t: t.to(torch.bfloat16) if t.is_floating_point() else t)(torch.as_tensor(a)))
    def _A_setup():
        B, S, D = 1, 2048, 576
        ns = torch.arange(S)[:, None] * torch.arange(S)[None, :]
        nd = torch.arange(D)[:, None] * torch.arange(D)[None, :]
        Ws_re = torch.cos(-2.0 * torch.pi * ns / S).to(float)
        Ws_im = torch.sin(-2.0 * torch.pi * ns / S).to(float)
        Wd_re = torch.cos(-2.0 * torch.pi * nd / D).to(float)
        Wd_im = torch.sin(-2.0 * torch.pi * nd / D).to(float)
        return {
            "x": _w((lambda *s: torch.randn(*s, dtype=torch.bfloat16) * 0.05)(B, S, D)),
            "dft_seq_re": _w(Ws_re),
            "dft_seq_im": _w(Ws_im),
            "dft_hid_re": _w(Wd_re),
            "dft_hid_im": _w(Wd_im),
        }
    _A_in = _A_setup()

    def _B_setup():
        B, S, D, Dff = 1, 2048, 576, 4608
        return {
            "x": _w((lambda *s: torch.randn(*s, dtype=torch.bfloat16) * 0.05)(B, S, D)),
            "w1": _w((lambda *s: torch.randn(*s, dtype=torch.bfloat16) * 0.4)(D, Dff)),
            "w2": _w((lambda *s: torch.randn(*s, dtype=torch.bfloat16) * 0.4)(Dff, D)),
        }
    _B_in = _B_setup()

    return {
        "A_x": _A_in["x"],
        "A_dft_seq_re": _A_in["dft_seq_re"],
        "A_dft_seq_im": _A_in["dft_seq_im"],
        "A_dft_hid_re": _A_in["dft_hid_re"],
        "A_dft_hid_im": _A_in["dft_hid_im"],
        "B_x": _B_in["x"],
        "B_w1": _B_in["w1"],
        "B_w2": _B_in["w2"],
        "gate_w": _w((lambda *s: torch.randn(*s, dtype=torch.bfloat16) * 0.05)(576) * 16.0),
    }

def reference(A_x, A_dft_seq_re, A_dft_seq_im, A_dft_hid_re, A_dft_hid_im, B_x, B_w1, B_w2, gate_w):
    def _A_ref(x, dft_seq_re, dft_seq_im, dft_hid_re, dft_hid_im):
        a = torch.einsum("bsd,ed->bse", x, dft_hid_re)
        b = torch.einsum("bsd,ed->bse", x, dft_hid_im)
        y = torch.einsum("ts,bse->bte", dft_seq_re, a) - torch.einsum("ts,bse->bte", dft_seq_im, b)
        return y

    def _B_ref(x, w1, w2):
        h = torch.einsum("bsd,df->bsf", x, w1)
        zero = torch.zeros_like(h)
        h = torch.maximum(h, zero)
        return torch.einsum("bsf,fd->bsd", h, w2)

    x = A_x
    g = _sigmoid((x * gate_w).sum(-1, keepdims=True))
    a = _A_ref(x, A_dft_seq_re, A_dft_seq_im, A_dft_hid_re, A_dft_hid_im)
    b = _B_ref(x, B_w1, B_w2)
    return g * a + (1 - g) * b
```

```cpp
#include <hip/hip_runtime.h>
constexpr int LDS_BYTES = 148032;

__global__ __launch_bounds__(512, 2) void fused_main(const void* x, const void* sre, const void* sim, const void* hre, const void* him,
                                                     const void* w1, const void* w2, const void* gw, void* out, unsigned* ws) {
  const unsigned wid = __builtin_amdgcn_readfirstlane(threadIdx.x >> 6);
  const unsigned bid = blockIdx.x;
  auto ka = __builtin_amdgcn_kernarg_segment_ptr();
  asm volatile(
    "KBEG_%=:\n\t"
    "s_load_dwordx16 s[68:83], %[ka], 0x0\n\t"
    "s_load_dwordx4 s[84:87], %[ka], 0x40\n\t"
    "s_mov_b32 s64, %[wid]\n\t"
    "v_mbcnt_lo_u32_b32 v11, -1, 0\n\t"
    "v_mbcnt_hi_u32_b32 v11, -1, v11\n\t"
    "v_lshlrev_b32 v0, 4, v11\n\t"
    "s_and_b32 s65, %[bid], 7\n\t"
    "s_bfe_u32 s66, %[bid], 0x10003\n\t"
    "s_lshl_b32 s65, s65, 1\n\t"
    "s_or_b32 s49, s65, s66\n\t"
    "s_lshr_b32 s48, %[bid], 4\n\t"
    "s_cmp_eq_u32 s64, 7\n\t"
    "s_cbranch_scc0 NOIPF_%=\n\t"
    "s_getpc_b64 s[92:93]\n\t"
    "IPC_%=:\n\t"
    "s_sub_u32 s92, s92, IPC_%=-KBEG_%=\n\t"
    "s_subb_u32 s93, s93, 0\n\t"
    "s_mov_b32 s94, END_%=-KBEG_%=\n\t"
    "s_mov_b32 s95, 0x20000\n\t"
    "s_lshl_b32 s66, s48, 1\n\t"
    "s_and_b32 s67, s49, 1\n\t"
    "s_add_u32 s66, s66, s67\n\t"
    "s_lshl_b32 s66, s66, 10\n\t"
    "s_add_u32 s67, s66, 32768\n\t"
    "buffer_load_dwordx4 v[64:67], v0, s[92:95], s66 offen\n\t"
    "buffer_load_dwordx4 v[68:71], v0, s[92:95], s67 offen\n\t"
    "NOIPF_%=:\n\t"
    "s_and_b32 s50, s49, 7\n\t"
    "s_lshr_b32 s88, s49, 3\n\t"
    "s_lshl_b32 s53, s88, 31\n\t"
    "s_or_b32 s53, s53, 1.0\n\t"
    "s_add_u32 s65, s48, s49\n\t"
    "s_mul_i32 s66, s65, 57\n\t"
    "s_lshr_b32 s66, s66, 9\n\t"
    "s_mul_i32 s66, s66, 9\n\t"
    "s_sub_u32 s51, s65, s66\n\t"
    "s_cmp_ge_u32 s48, 9\n\t"
    "s_cselect_b32 s66, 9, 0\n\t"
    "s_sub_u32 s52, s48, s66\n\t"
    "s_lshr_b32 s55, s66, 3\n\t"
    "s_mov_b32 s55, 0\n\t"
    "s_mul_i32 s89, s48, 147456\n\t"
    "s_mul_i32 s90, s50, 82944\n\t"
    "s_cmp_eq_u32 s88, 0\n\t"
    "s_waitcnt lgkmcnt(0)\n\t"
    "s_cselect_b64 s[70:71], s[70:71], s[72:73]\n\t"
    "s_cselect_b64 s[74:75], s[74:75], s[76:77]\n\t"
    "s_add_u32 s8, s68, s89\n\t"
    "s_addc_u32 s9, s69, 0\n\t"
    "s_mov_b32 s10, 147456\n\t"
    "s_mov_b32 s11, 0x20000\n\t"
    "s_add_u32 s20, s74, s90\n\t"
    "s_addc_u32 s21, s75, 0\n\t"
    "s_mov_b32 s22, 82944\n\t"
    "s_mov_b32 s23, 0x20000\n\t"
    "s_mov_b32 s24, s82\n\t"
    "s_mov_b32 s25, s83\n\t"
    "s_mov_b32 s26, 1152\n\t"
    "s_mov_b32 s27, 0x20000\n\t"
    "s_add_u32 s44, s84, s89\n\t"
    "s_addc_u32 s45, s85, 0\n\t"
    "s_mov_b32 s46, 147456\n\t"
    "s_mov_b32 s47, 0x20000\n\t"
    "s_mov_b32 s40, s86\n\t"
    "s_mov_b32 s41, s87\n\t"
    "s_mov_b32 s42, 131072\n\t"
    "s_mov_b32 s43, 0x20000\n\t"
    "s_mul_i32 s65, s49, 576\n\t"
    "s_add_u32 s12, s78, s65\n\t"
    "s_addc_u32 s13, s79, 0\n\t"
    "s_sub_u32 s14, 5308416, s65\n\t"
    "s_mov_b32 s15, 0x20000\n\t"
    "s_mul_i32 s65, s49, 331776\n\t"
    "s_add_u32 s16, s80, s65\n\t"
    "s_addc_u32 s17, s81, 0\n\t"
    "s_mov_b32 s18, 331776\n\t"
    "s_mov_b32 s19, 0x20000\n\t"
    "s_lshl_b32 s65, s48, 19\n\t"
    "s_lshl_b32 s66, s50, 9\n\t"
    "s_add_u32 s65, s65, s66\n\t"
    "s_add_u32 s28, s70, s65\n\t"
    "s_addc_u32 s29, s71, 0\n\t"
    "s_sub_u32 s30, 524288, s66\n\t"
    "s_mov_b32 s31, 0x20000\n\t"
    "s_mul_i32 s65, s88, 2359296\n\t"
    "s_add_u32 s65, s65, 131072\n\t"
    "s_add_u32 s56, s86, s65\n\t"
    "s_addc_u32 s57, s87, 0\n\t"
    "s_mov_b32 s58, 2359296\n\t"
    "s_mov_b32 s59, 0x20000\n\t"
    "s_mul_i32 s65, s48, 2359296\n\t"
    "s_add_u32 s65, s65, 4849664\n\t"
    "s_add_u32 s36, s86, s65\n\t"
    "s_addc_u32 s37, s87, 0\n\t"
    "s_mov_b32 s38, 2359296\n\t"
    "s_mov_b32 s39, 0x20000\n\t"
    "s_cmp_lt_u32 s64, 4\n\t"
    "s_cbranch_scc0 HELP_%=\n\t"
    "v_mbcnt_lo_u32_b32 v11, -1, 0\n\t"
    "v_mbcnt_hi_u32_b32 v11, -1, v11\n\t"
    "v_and_b32 v12, 15, v11\n\t"
    "v_lshrrev_b32 v13, 4, v11\n\t"
    "v_lshrrev_b32 v14, 1, v12\n\t"
    "v_xor_b32 v14, v14, v13\n\t"
    "v_lshlrev_b32 v14, 4, v14\n\t"
    "s_lshl_b32 s65, s55, 6\n\t"
    "v_xor_b32 v14, s65, v14\n\t"
    "v_lshl_add_u32 v3, v12, 7, v14\n\t"
    "v_xor_b32 v4, 64, v3\n\t"
    "v_lshrrev_b32 v15, 2, v12\n\t"
    "v_lshlrev_b32 v15, 3, v15\n\t"
    "v_and_b32 v1, 3, v12\n\t"
    "v_add_u32 v15, v15, v1\n\t"
    "s_lshl_b32 s65, s64, 12\n\t"
    "v_lshl_add_u32 v1, v15, 7, v14\n\t"
    "v_add_u32 v1, s65, v1\n\t"
    "v_xor_b32 v2, 64, v1\n\t"
    "v_mov_b32 v152, 0\n\t"
    "v_mov_b32 v153, 0\n\t"
    "v_mov_b32 v154, 0\n\t"
    "v_mov_b32 v155, 0\n\t"
    "v_mov_b32 v156, 0\n\t"
    "v_mov_b32 v157, 0\n\t"
    "v_mov_b32 v158, 0\n\t"
    "v_mov_b32 v159, 0\n\t"
    "v_mov_b32 v160, 0\n\t"
    "v_mov_b32 v161, 0\n\t"
    "v_mov_b32 v162, 0\n\t"
    "v_mov_b32 v163, 0\n\t"
    "v_mov_b32 v164, 0\n\t"
    "v_mov_b32 v165, 0\n\t"
    "v_mov_b32 v166, 0\n\t"
    "v_mov_b32 v167, 0\n\t"
    "v_mov_b32 v168, 0\n\t"
    "v_mov_b32 v169, 0\n\t"
    "v_mov_b32 v170, 0\n\t"
    "v_mov_b32 v171, 0\n\t"
    "v_mov_b32 v172, 0\n\t"
    "v_mov_b32 v173, 0\n\t"
    "v_mov_b32 v174, 0\n\t"
    "v_mov_b32 v175, 0\n\t"
    "v_mov_b32 v176, 0\n\t"
    "v_mov_b32 v177, 0\n\t"
    "v_mov_b32 v178, 0\n\t"
    "v_mov_b32 v179, 0\n\t"
    "v_mov_b32 v180, 0\n\t"
    "v_mov_b32 v181, 0\n\t"
    "v_mov_b32 v182, 0\n\t"
    "v_mov_b32 v183, 0\n\t"
    "v_mov_b32 v184, 0\n\t"
    "v_mov_b32 v185, 0\n\t"
    "v_mov_b32 v186, 0\n\t"
    "v_mov_b32 v187, 0\n\t"
    "v_mov_b32 v188, 0\n\t"
    "v_mov_b32 v189, 0\n\t"
    "v_mov_b32 v190, 0\n\t"
    "v_mov_b32 v191, 0\n\t"
    "s_setprio 2\n\t"
    "v_mov_b32 v9, v1\n\t"
    "s_mov_b32 s66, 0\n\t"
    "s_mov_b32 s67, 0\n\t"
    "s_mov_b32 s69, -110592\n\t"
    "s_barrier\n\t"
    "ds_read_b128 v[32:35], v1 offset:0\n\t"
    "ds_read_b128 v[36:39], v1 offset:512\n\t"
    "ds_read_b128 v[88:91], v3 offset:16384\n\t"
    "ds_read_b128 v[92:95], v3 offset:18432\n\t"
    "ds_read_b128 v[96:99], v3 offset:20480\n\t"
    "ds_read_b128 v[100:103], v3 offset:22528\n\t"
    "s_waitcnt lgkmcnt(3)\n\t"
    "v_mfma_f32_16x16x32_bf16 v[152:155], v[32:35], v[88:91], v[152:155]\n\t"
    "ds_read_b128 v[104:107], v3 offset:24576\n\t"
    "ds_read_b128 v[40:43], v2 offset:0\n\t"
    "v_mfma_f32_16x16x32_bf16 v[156:159], v[36:39], v[88:91], v[156:159]\n\t"
    "ds_read_b128 v[44:47], v2 offset:512\n\t"
    "ds_read_b128 v[108:111], v4 offset:16384\n\t"
    "s_waitcnt lgkmcnt(6)\n\t"
    "v_mfma_f32_16x16x32_bf16 v[164:167], v[36:39], v[92:95], v[164:167]\n\t"
    "ds_read_b128 v[112:115], v4 offset:18432\n\t"
    "v_mfma_f32_16x16x32_bf16 v[160:163], v[32:35], v[92:95], v[160:163]\n\t"
    "ds_read_b128 v[116:119], v4 offset:20480\n\t"
    "s_waitcnt lgkmcnt(7)\n\t"
    "v_mfma_f32_16x16x32_bf16 v[168:171], v[32:35], v[96:99], v[168:171]\n\t"
    "ds_read_b128 v[120:123], v4 offset:22528\n\t"
    "v_mfma_f32_16x16x32_bf16 v[172:175], v[36:39], v[96:99], v[172:175]\n\t"
    "ds_read_b128 v[124:127], v4 offset:24576\n\t"
    "s_waitcnt lgkmcnt(8)\n\t"
    "v_mfma_f32_16x16x32_bf16 v[180:183], v[36:39], v[100:103], v[180:183]\n\t"
    "v_mfma_f32_16x16x32_bf16 v[176:179], v[32:35], v[100:103], v[176:179]\n\t"
    "s_waitcnt lgkmcnt(7)\n\t"
    "v_mfma_f32_16x16x32_bf16 v[184:187], v[32:35], v[104:107], v[184:187]\n\t"
    "v_mfma_f32_16x16x32_bf16 v[188:191], v[36:39], v[104:107], v[188:191]\n\t"
    "s_waitcnt lgkmcnt(4)\n\t"
    "v_mfma_f32_16x16x32_bf16 v[156:159], v[44:47], v[108:111], v[156:159]\n\t"
    "v_mfma_f32_16x16x32_bf16 v[152:155], v[40:43], v[108:111], v[152:155]\n\t"
    "s_waitcnt lgkmcnt(0)\n\t"
    "s_mov_b32 s68, 36864\n\t"
    "s_barrier\n\t"
    "v_add_u32 v1, s68, v1\n\t"
    "v_add_u32 v2, s68, v2\n\t"
    "v_add_u32 v3, s68, v3\n\t"
    "v_add_u32 v4, s68, v4\n\t"
    "v_mfma_f32_16x16x32_bf16 v[160:163], v[40:43], v[112:115], v[160:163]\n\t"
    "ds_read_b128 v[32:35], v1 offset:0\n\t"
    "ds_read_b128 v[36:39], v1 offset:512\n\t"
    "v_mfma_f32_16x16x32_bf16 v[164:167], v[44:47], v[112:115], v[164:167]\n\t"
    "ds_read_b128 v[88:91], v3 offset:16384\n\t"
    "v_mfma_f32_16x16x32_bf16 v[172:175], v[44:47], v[116:119], v[172:175]\n\t"
    "ds_read_b128 v[92:95], v3 offset:18432\n\t"
    "v_mfma_f32_16x16x32_bf16 v[168:171], v[40:43], v[116:119], v[168:171]\n\t"
    "v_mfma_f32_16x16x32_bf16 v[176:179], v[40:43], v[120:123], v[176:179]\n\t"
    "ds_read_b128 v[96:99], v3 offset:20480\n\t"
    "v_mfma_f32_16x16x32_bf16 v[180:183], v[44:47], v[120:123], v[180:183]\n\t"
    "v_mfma_f32_16x16x32_bf16 v[188:191], v[44:47], v[124:127], v[188:191]\n\t"
    "ds_read_b128 v[100:103], v3 offset:22528\n\t"
    "v_mfma_f32_16x16x32_bf16 v[184:187], v[40:43], v[124:127], v[184:187]\n\t"
    "s_waitcnt lgkmcnt(3)\n\t"
    "v_mfma_f32_16x16x32_bf16 v[152:155], v[32:35], v[88:91], v[152:155]\n\t"
    "ds_read_b128 v[104:107], v3 offset:24576\n\t"
    "ds_read_b128 v[40:43], v2 offset:0\n\t"
    "v_mfma_f32_16x16x32_bf16 v[156:159], v[36:39], v[88:91], v[156:159]\n\t"
    "ds_read_b128 v[44:47], v2 offset:512\n\t"
    "ds_read_b128 v[108:111], v4 offset:16384\n\t"
    "s_waitcnt lgkmcnt(6)\n\t"
    "v_mfma_f32_16x16x32_bf16 v[164:167], v[36:39], v[92:95], v[164:167]\n\t"
    "ds_read_b128 v[112:115], v4 offset:18432\n\t"
    "v_mfma_f32_16x16x32_bf16 v[160:163], v[32:35], v[92:95], v[160:163]\n\t"
    "ds_read_b128 v[116:119], v4 offset:20480\n\t"
    "s_waitcnt lgkmcnt(7)\n\t"
    "v_mfma_f32_16x16x32_bf16 v[168:171], v[32:35], v[96:99], v[168:171]\n\t"
    "ds_read_b128 v[120:123], v4 offset:22528\n\t"
    "v_mfma_f32_16x16x32_bf16 v[172:175], v[36:39], v[96:99], v[172:175]\n\t"
    "ds_read_b128 v[124:127], v4 offset:24576\n\t"
    "s_waitcnt lgkmcnt(8)\n\t"
    "v_mfma_f32_16x16x32_bf16 v[180:183], v[36:39], v[100:103], v[180:183]\n\t"
    "v_mfma_f32_16x16x32_bf16 v[176:179], v[32:35], v[100:103], v[176:179]\n\t"
    "s_waitcnt lgkmcnt(7)\n\t"
    "v_mfma_f32_16x16x32_bf16 v[184:187], v[32:35], v[104:107], v[184:187]\n\t"
    "v_mfma_f32_16x16x32_bf16 v[188:191], v[36:39], v[104:107], v[188:191]\n\t"
    "s_waitcnt lgkmcnt(4)\n\t"
    "v_mfma_f32_16x16x32_bf16 v[156:159], v[44:47], v[108:111], v[156:159]\n\t"
    "v_mfma_f32_16x16x32_bf16 v[152:155], v[40:43], v[108:111], v[152:155]\n\t"
    "s_waitcnt lgkmcnt(0)\n\t"
    "s_mov_b32 s68, 36864\n\t"
    "s_barrier\n\t"
    "v_add_u32 v1, s68, v1\n\t"
    "v_add_u32 v2, s68, v2\n\t"
    "v_add_u32 v3, s68, v3\n\t"
    "v_add_u32 v4, s68, v4\n\t"
    "v_mfma_f32_16x16x32_bf16 v[160:163], v[40:43], v[112:115], v[160:163]\n\t"
    "ds_read_b128 v[32:35], v1 offset:0\n\t"
    "ds_read_b128 v[36:39], v1 offset:512\n\t"
    "v_mfma_f32_16x16x32_bf16 v[164:167], v[44:47], v[112:115], v[164:167]\n\t"
    "ds_read_b128 v[88:91], v3 offset:16384\n\t"
    "v_mfma_f32_16x16x32_bf16 v[172:175], v[44:47], v[116:119], v[172:175]\n\t"
    "ds_read_b128 v[92:95], v3 offset:18432\n\t"
    "v_mfma_f32_16x16x32_bf16 v[168:171], v[40:43], v[116:119], v[168:171]\n\t"
    "v_mfma_f32_16x16x32_bf16 v[176:179], v[40:43], v[120:123], v[176:179]\n\t"
    "ds_read_b128 v[96:99], v3 offset:20480\n\t"
    "v_mfma_f32_16x16x32_bf16 v[180:183], v[44:47], v[120:123], v[180:183]\n\t"
    "v_mfma_f32_16x16x32_bf16 v[188:191], v[44:47], v[124:127], v[188:191]\n\t"
    "ds_read_b128 v[100:103], v3 offset:22528\n\t"
    "v_mfma_f32_16x16x32_bf16 v[184:187], v[40:43], v[124:127], v[184:187]\n\t"
    "s_waitcnt lgkmcnt(3)\n\t"
    "v_mfma_f32_16x16x32_bf16 v[152:155], v[32:35], v[88:91], v[152:155]\n\t"
    "ds_read_b128 v[104:107], v3 offset:24576\n\t"
    "ds_read_b128 v[40:43], v2 offset:0\n\t"
    "v_mfma_f32_16x16x32_bf16 v[156:159], v[36:39], v[88:91], v[156:159]\n\t"
    "ds_read_b128 v[44:47], v2 offset:512\n\t"
    "ds_read_b128 v[108:111], v4 offset:16384\n\t"
    "s_waitcnt lgkmcnt(6)\n\t"
    "v_mfma_f32_16x16x32_bf16 v[164:167], v[36:39], v[92:95], v[164:167]\n\t"
    "ds_read_b128 v[112:115], v4 offset:18432\n\t"
    "v_mfma_f32_16x16x32_bf16 v[160:163], v[32:35], v[92:95], v[160:163]\n\t"
    "ds_read_b128 v[116:119], v4 offset:20480\n\t"
    "s_waitcnt lgkmcnt(7)\n\t"
    "v_mfma_f32_16x16x32_bf16 v[168:171], v[32:35], v[96:99], v[168:171]\n\t"
    "ds_read_b128 v[120:123], v4 offset:22528\n\t"
    "v_mfma_f32_16x16x32_bf16 v[172:175], v[36:39], v[96:99], v[172:175]\n\t"
    "ds_read_b128 v[124:127], v4 offset:24576\n\t"
    "s_waitcnt lgkmcnt(8)\n\t"
    "v_mfma_f32_16x16x32_bf16 v[180:183], v[36:39], v[100:103], v[180:183]\n\t"
    "v_mfma_f32_16x16x32_bf16 v[176:179], v[32:35], v[100:103], v[176:179]\n\t"
    "s_waitcnt lgkmcnt(7)\n\t"
    "v_mfma_f32_16x16x32_bf16 v[184:187], v[32:35], v[104:107], v[184:187]\n\t"
    "v_mfma_f32_16x16x32_bf16 v[188:191], v[36:39], v[104:107], v[188:191]\n\t"
    "s_waitcnt lgkmcnt(4)\n\t"
    "v_mfma_f32_16x16x32_bf16 v[156:159], v[44:47], v[108:111], v[156:159]\n\t"
    "v_mfma_f32_16x16x32_bf16 v[152:155], v[40:43], v[108:111], v[152:155]\n\t"
    "s_waitcnt lgkmcnt(0)\n\t"
    "s_mov_b32 s68, 36864\n\t"
    "s_barrier\n\t"
    "v_add_u32 v1, s68, v1\n\t"
    "v_add_u32 v2, s68, v2\n\t"
    "v_add_u32 v3, s68, v3\n\t"
    "v_add_u32 v4, s68, v4\n\t"
    "v_mfma_f32_16x16x32_bf16 v[160:163], v[40:43], v[112:115], v[160:163]\n\t"
    "ds_read_b128 v[32:35], v1 offset:0\n\t"
    "ds_read_b128 v[36:39], v1 offset:512\n\t"
    "v_mfma_f32_16x16x32_bf16 v[164:167], v[44:47], v[112:115], v[164:167]\n\t"
    "ds_read_b128 v[88:91], v3 offset:16384\n\t"
    "v_mfma_f32_16x16x32_bf16 v[172:175], v[44:47], v[116:119], v[172:175]\n\t"
    "ds_read_b128 v[92:95], v3 offset:18432\n\t"
    "v_mfma_f32_16x16x32_bf16 v[168:171], v[40:43], v[116:119], v[168:171]\n\t"
    "v_mfma_f32_16x16x32_bf16 v[176:179], v[40:43], v[120:123], v[176:179]\n\t"
    "ds_read_b128 v[96:99], v3 offset:20480\n\t"
    "v_mfma_f32_16x16x32_bf16 v[180:183], v[44:47], v[120:123], v[180:183]\n\t"
    "v_mfma_f32_16x16x32_bf16 v[188:191], v[44:47], v[124:127], v[188:191]\n\t"
    "ds_read_b128 v[100:103], v3 offset:22528\n\t"
    "v_mfma_f32_16x16x32_bf16 v[184:187], v[40:43], v[124:127], v[184:187]\n\t"
    "s_waitcnt lgkmcnt(3)\n\t"
    "v_mfma_f32_16x16x32_bf16 v[152:155], v[32:35], v[88:91], v[152:155]\n\t"
    "ds_read_b128 v[104:107], v3 offset:24576\n\t"
    "ds_read_b128 v[40:43], v2 offset:0\n\t"
    "v_mfma_f32_16x16x32_bf16 v[156:159], v[36:39], v[88:91], v[156:159]\n\t"
    "ds_read_b128 v[44:47], v2 offset:512\n\t"
    "ds_read_b128 v[108:111], v4 offset:16384\n\t"
    "s_waitcnt lgkmcnt(6)\n\t"
    "v_mfma_f32_16x16x32_bf16 v[164:167], v[36:39], v[92:95], v[164:167]\n\t"
    "ds_read_b128 v[112:115], v4 offset:18432\n\t"
    "v_mfma_f32_16x16x32_bf16 v[160:163], v[32:35], v[92:95], v[160:163]\n\t"
    "ds_read_b128 v[116:119], v4 offset:20480\n\t"
    "s_waitcnt lgkmcnt(7)\n\t"
    "v_mfma_f32_16x16x32_bf16 v[168:171], v[32:35], v[96:99], v[168:171]\n\t"
    "ds_read_b128 v[120:123], v4 offset:22528\n\t"
    "v_mfma_f32_16x16x32_bf16 v[172:175], v[36:39], v[96:99], v[172:175]\n\t"
    "ds_read_b128 v[124:127], v4 offset:24576\n\t"
    "s_waitcnt lgkmcnt(8)\n\t"
    "v_mfma_f32_16x16x32_bf16 v[180:183], v[36:39], v[100:103], v[180:183]\n\t"
    "v_mfma_f32_16x16x32_bf16 v[176:179], v[32:35], v[100:103], v[176:179]\n\t"
    "s_waitcnt lgkmcnt(7)\n\t"
    "v_mfma_f32_16x16x32_bf16 v[184:187], v[32:35], v[104:107], v[184:187]\n\t"
    "v_mfma_f32_16x16x32_bf16 v[188:191], v[36:39], v[104:107], v[188:191]\n\t"
    "s_waitcnt lgkmcnt(4)\n\t"
    "v_mfma_f32_16x16x32_bf16 v[156:159], v[44:47], v[108:111], v[156:159]\n\t"
    "v_mfma_f32_16x16x32_bf16 v[152:155], v[40:43], v[108:111], v[152:155]\n\t"
    "s_waitcnt lgkmcnt(0)\n\t"
    "s_mov_b32 s68, -110592\n\t"
    "s_barrier\n\t"
    "v_add_u32 v1, s68, v1\n\t"
    "v_add_u32 v2, s68, v2\n\t"
    "v_add_u32 v3, s68, v3\n\t"
    "v_add_u32 v4, s68, v4\n\t"
    "v_mfma_f32_16x16x32_bf16 v[160:163], v[40:43], v[112:115], v[160:163]\n\t"
    "ds_read_b128 v[240:243], v1 offset:0\n\t"
    "ds_read_b128 v[244:247], v1 offset:512\n\t"
    "v_mfma_f32_16x16x32_bf16 v[164:167], v[44:47], v[112:115], v[164:167]\n\t"
    "ds_read_b128 v[88:91], v3 offset:16384\n\t"
    "v_mfma_f32_16x16x32_bf16 v[172:175], v[44:47], v[116:119], v[172:175]\n\t"
    "ds_read_b128 v[92:95], v3 offset:18432\n\t"
    "v_mfma_f32_16x16x32_bf16 v[168:171], v[40:43], v[116:119], v[168:171]\n\t"
    "v_mfma_f32_16x16x32_bf16 v[176:179], v[40:43], v[120:123], v[176:179]\n\t"
    "ds_read_b128 v[96:99], v3 offset:20480\n\t"
    "v_mfma_f32_16x16x32_bf16 v[180:183], v[44:47], v[120:123], v[180:183]\n\t"
    "v_mfma_f32_16x16x32_bf16 v[188:191], v[44:47], v[124:127], v[188:191]\n\t"
    "ds_read_b128 v[100:103], v3 offset:22528\n\t"
    "v_mfma_f32_16x16x32_bf16 v[184:187], v[40:43], v[124:127], v[184:187]\n\t"
    "s_waitcnt lgkmcnt(3)\n\t"
    "v_mfma_f32_16x16x32_bf16 v[152:155], v[240:243], v[88:91], v[152:155]\n\t"
    "ds_read_b128 v[104:107], v3 offset:24576\n\t"
    "ds_read_b128 v[248:251], v2 offset:0\n\t"
    "v_mfma_f32_16x16x32_bf16 v[156:159], v[244:247], v[88:91], v[156:159]\n\t"
    "ds_read_b128 v[252:255], v2 offset:512\n\t"
    "ds_read_b128 v[108:111], v4 offset:16384\n\t"
    "s_waitcnt lgkmcnt(6)\n\t"
    "v_mfma_f32_16x16x32_bf16 v[164:167], v[244:247], v[92:95], v[164:167]\n\t"
    "ds_read_b128 v[112:115], v4 offset:18432\n\t"
    "v_mfma_f32_16x16x32_bf16 v[160:163], v[240:243], v[92:95], v[160:163]\n\t"
    "ds_read_b128 v[116:119], v4 offset:20480\n\t"
    "s_waitcnt lgkmcnt(7)\n\t"
    "v_mfma_f32_16x16x32_bf16 v[168:171], v[240:243], v[96:99], v[168:171]\n\t"
    "ds_read_b128 v[120:123], v4 offset:22528\n\t"
    "v_mfma_f32_16x16x32_bf16 v[172:175], v[244:247], v[96:99], v[172:175]\n\t"
    "ds_read_b128 v[124:127], v4 offset:24576\n\t"
    "s_waitcnt lgkmcnt(8)\n\t"
    "v_mfma_f32_16x16x32_bf16 v[180:183], v[244:247], v[100:103], v[180:183]\n\t"
    "v_mfma_f32_16x16x32_bf16 v[176:179], v[240:243], v[100:103], v[176:179]\n\t"
    "s_waitcnt lgkmcnt(7)\n\t"
    "v_mfma_f32_16x16x32_bf16 v[184:187], v[240:243], v[104:107], v[184:187]\n\t"
    "v_mfma_f32_16x16x32_bf16 v[188:191], v[244:247], v[104:107], v[188:191]\n\t"
    "s_waitcnt lgkmcnt(4)\n\t"
    "v_mfma_f32_16x16x32_bf16 v[156:159], v[252:255], v[108:111], v[156:159]\n\t"
    "v_mfma_f32_16x16x32_bf16 v[152:155], v[248:251], v[108:111], v[152:155]\n\t"
    "s_waitcnt lgkmcnt(0)\n\t"
    "s_mov_b32 s68, 36864\n\t"
    "s_barrier\n\t"
    "v_add_u32 v1, s68, v1\n\t"
    "v_add_u32 v2, s68, v2\n\t"
    "v_add_u32 v3, s68, v3\n\t"
    "v_add_u32 v4, s68, v4\n\t"
    "v_mfma_f32_16x16x32_bf16 v[160:163], v[248:251], v[112:115], v[160:163]\n\t"
    "ds_read_b128 v[224:227], v1 offset:0\n\t"
    "ds_read_b128 v[228:231], v1 offset:512\n\t"
    "v_mfma_f32_16x16x32_bf16 v[164:167], v[252:255], v[112:115], v[164:167]\n\t"
    "ds_read_b128 v[88:91], v3 offset:16384\n\t"
    "v_mfma_f32_16x16x32_bf16 v[172:175], v[252:255], v[116:119], v[172:175]\n\t"
    "ds_read_b128 v[92:95], v3 offset:18432\n\t"
    "v_mfma_f32_16x16x32_bf16 v[168:171], v[248:251], v[116:119], v[168:171]\n\t"
    "v_mfma_f32_16x16x32_bf16 v[176:179], v[248:251], v[120:123], v[176:179]\n\t"
    "ds_read_b128 v[96:99], v3 offset:20480\n\t"
    "v_mfma_f32_16x16x32_bf16 v[180:183], v[252:255], v[120:123], v[180:183]\n\t"
    "v_mfma_f32_16x16x32_bf16 v[188:191], v[252:255], v[124:127], v[188:191]\n\t"
    "ds_read_b128 v[100:103], v3 offset:22528\n\t"
    "v_mfma_f32_16x16x32_bf16 v[184:187], v[248:251], v[124:127], v[184:187]\n\t"
    "s_waitcnt lgkmcnt(3)\n\t"
    "v_mfma_f32_16x16x32_bf16 v[152:155], v[224:227], v[88:91], v[152:155]\n\t"
    "ds_read_b128 v[104:107], v3 offset:24576\n\t"
    "ds_read_b128 v[232:235], v2 offset:0\n\t"
    "v_mfma_f32_16x16x32_bf16 v[156:159], v[228:231], v[88:91], v[156:159]\n\t"
    "ds_read_b128 v[236:239], v2 offset:512\n\t"
    "ds_read_b128 v[108:111], v4 offset:16384\n\t"
    "s_waitcnt lgkmcnt(6)\n\t"
    "v_mfma_f32_16x16x32_bf16 v[164:167], v[228:231], v[92:95], v[164:167]\n\t"
    "ds_read_b128 v[112:115], v4 offset:18432\n\t"
    "v_mfma_f32_16x16x32_bf16 v[160:163], v[224:227], v[92:95], v[160:163]\n\t"
    "ds_read_b128 v[116:119], v4 offset:20480\n\t"
    "s_waitcnt lgkmcnt(7)\n\t"
    "v_mfma_f32_16x16x32_bf16 v[168:171], v[224:227], v[96:99], v[168:171]\n\t"
    "ds_read_b128 v[120:123], v4 offset:22528\n\t"
    "v_mfma_f32_16x16x32_bf16 v[172:175], v[228:231], v[96:99], v[172:175]\n\t"
    "ds_read_b128 v[124:127], v4 offset:24576\n\t"
    "s_waitcnt lgkmcnt(8)\n\t"
    "v_mfma_f32_16x16x32_bf16 v[180:183], v[228:231], v[100:103], v[180:183]\n\t"
    "v_mfma_f32_16x16x32_bf16 v[176:179], v[224:227], v[100:103], v[176:179]\n\t"
    "s_waitcnt lgkmcnt(7)\n\t"
    "v_mfma_f32_16x16x32_bf16 v[184:187], v[224:227], v[104:107], v[184:187]\n\t"
    "v_mfma_f32_16x16x32_bf16 v[188:191], v[228:231], v[104:107], v[188:191]\n\t"
    "s_waitcnt lgkmcnt(4)\n\t"
    "v_mfma_f32_16x16x32_bf16 v[156:159], v[236:239], v[108:111], v[156:159]\n\t"
    "v_mfma_f32_16x16x32_bf16 v[152:155], v[232:235], v[108:111], v[152:155]\n\t"
    "s_waitcnt lgkmcnt(0)\n\t"
    "s_mov_b32 s68, 36864\n\t"
    "s_barrier\n\t"
    "v_add_u32 v1, s68, v1\n\t"
    "v_add_u32 v2, s68, v2\n\t"
    "v_add_u32 v3, s68, v3\n\t"
    "v_add_u32 v4, s68, v4\n\t"
    "v_mfma_f32_16x16x32_bf16 v[160:163], v[232:235], v[112:115], v[160:163]\n\t"
    "ds_read_b128 v[208:211], v1 offset:0\n\t"
    "ds_read_b128 v[212:215], v1 offset:512\n\t"
    "v_mfma_f32_16x16x32_bf16 v[164:167], v[236:239], v[112:115], v[164:167]\n\t"
    "ds_read_b128 v[88:91], v3 offset:16384\n\t"
    "v_mfma_f32_16x16x32_bf16 v[172:175], v[236:239], v[116:119], v[172:175]\n\t"
    "ds_read_b128 v[92:95], v3 offset:18432\n\t"
    "v_mfma_f32_16x16x32_bf16 v[168:171], v[232:235], v[116:119], v[168:171]\n\t"
    "v_mfma_f32_16x16x32_bf16 v[176:179], v[232:235], v[120:123], v[176:179]\n\t"
    "ds_read_b128 v[96:99], v3 offset:20480\n\t"
    "v_mfma_f32_16x16x32_bf16 v[180:183], v[236:239], v[120:123], v[180:183]\n\t"
    "v_mfma_f32_16x16x32_bf16 v[188:191], v[236:239], v[124:127], v[188:191]\n\t"
    "ds_read_b128 v[100:103], v3 offset:22528\n\t"
    "v_mfma_f32_16x16x32_bf16 v[184:187], v[232:235], v[124:127], v[184:187]\n\t"
    "s_waitcnt lgkmcnt(3)\n\t"
    "v_mfma_f32_16x16x32_bf16 v[152:155], v[208:211], v[88:91], v[152:155]\n\t"
    "ds_read_b128 v[104:107], v3 offset:24576\n\t"
    "ds_read_b128 v[216:219], v2 offset:0\n\t"
    "v_mfma_f32_16x16x32_bf16 v[156:159], v[212:215], v[88:91], v[156:159]\n\t"
    "ds_read_b128 v[220:223], v2 offset:512\n\t"
    "ds_read_b128 v[108:111], v4 offset:16384\n\t"
    "s_waitcnt lgkmcnt(6)\n\t"
    "v_mfma_f32_16x16x32_bf16 v[164:167], v[212:215], v[92:95], v[164:167]\n\t"
    "ds_read_b128 v[112:115], v4 offset:18432\n\t"
    "v_mfma_f32_16x16x32_bf16 v[160:163], v[208:211], v[92:95], v[160:163]\n\t"
    "ds_read_b128 v[116:119], v4 offset:20480\n\t"
    "s_waitcnt lgkmcnt(7)\n\t"
    "v_mfma_f32_16x16x32_bf16 v[168:171], v[208:211], v[96:99], v[168:171]\n\t"
    "ds_read_b128 v[120:123], v4 offset:22528\n\t"
    "v_mfma_f32_16x16x32_bf16 v[172:175], v[212:215], v[96:99], v[172:175]\n\t"
    "ds_read_b128 v[124:127], v4 offset:24576\n\t"
    "s_waitcnt lgkmcnt(8)\n\t"
    "v_mfma_f32_16x16x32_bf16 v[180:183], v[212:215], v[100:103], v[180:183]\n\t"
    "v_mfma_f32_16x16x32_bf16 v[176:179], v[208:211], v[100:103], v[176:179]\n\t"
    "s_waitcnt lgkmcnt(7)\n\t"
    "v_mfma_f32_16x16x32_bf16 v[184:187], v[208:211], v[104:107], v[184:187]\n\t"
    "v_mfma_f32_16x16x32_bf16 v[188:191], v[212:215], v[104:107], v[188:191]\n\t"
    "s_waitcnt lgkmcnt(4)\n\t"
    "v_mfma_f32_16x16x32_bf16 v[156:159], v[220:223], v[108:111], v[156:159]\n\t"
    "v_mfma_f32_16x16x32_bf16 v[152:155], v[216:219], v[108:111], v[152:155]\n\t"
    "s_waitcnt lgkmcnt(0)\n\t"
    "s_mov_b32 s68, 36864\n\t"
    "s_barrier\n\t"
    "v_add_u32 v1, s68, v1\n\t"
    "v_add_u32 v2, s68, v2\n\t"
    "v_add_u32 v3, s68, v3\n\t"
    "v_add_u32 v4, s68, v4\n\t"
    "v_mfma_f32_16x16x32_bf16 v[160:163], v[216:219], v[112:115], v[160:163]\n\t"
    "ds_read_b128 v[192:195], v1 offset:0\n\t"
    "ds_read_b128 v[196:199], v1 offset:512\n\t"
    "v_mfma_f32_16x16x32_bf16 v[164:167], v[220:223], v[112:115], v[164:167]\n\t"
    "ds_read_b128 v[88:91], v3 offset:16384\n\t"
    "v_mfma_f32_16x16x32_bf16 v[172:175], v[220:223], v[116:119], v[172:175]\n\t"
    "ds_read_b128 v[92:95], v3 offset:18432\n\t"
    "v_mfma_f32_16x16x32_bf16 v[168:171], v[216:219], v[116:119], v[168:171]\n\t"
    "v_mfma_f32_16x16x32_bf16 v[176:179], v[216:219], v[120:123], v[176:179]\n\t"
    "ds_read_b128 v[96:99], v3 offset:20480\n\t"
    "v_mfma_f32_16x16x32_bf16 v[180:183], v[220:223], v[120:123], v[180:183]\n\t"
    "v_mfma_f32_16x16x32_bf16 v[188:191], v[220:223], v[124:127], v[188:191]\n\t"
    "ds_read_b128 v[100:103], v3 offset:22528\n\t"
    "v_mfma_f32_16x16x32_bf16 v[184:187], v[216:219], v[124:127], v[184:187]\n\t"
    "s_waitcnt lgkmcnt(3)\n\t"
    "v_mfma_f32_16x16x32_bf16 v[152:155], v[192:195], v[88:91], v[152:155]\n\t"
    "ds_read_b128 v[104:107], v3 offset:24576\n\t"
    "ds_read_b128 v[200:203], v2 offset:0\n\t"
    "v_mfma_f32_16x16x32_bf16 v[156:159], v[196:199], v[88:91], v[156:159]\n\t"
    "ds_read_b128 v[204:207], v2 offset:512\n\t"
    "ds_read_b128 v[108:111], v4 offset:16384\n\t"
    "s_waitcnt lgkmcnt(6)\n\t"
    "v_mfma_f32_16x16x32_bf16 v[164:167], v[196:199], v[92:95], v[164:167]\n\t"
    "ds_read_b128 v[112:115], v4 offset:18432\n\t"
    "v_mfma_f32_16x16x32_bf16 v[160:163], v[192:195], v[92:95], v[160:163]\n\t"
    "ds_read_b128 v[116:119], v4 offset:20480\n\t"
    "s_waitcnt lgkmcnt(7)\n\t"
    "v_mfma_f32_16x16x32_bf16 v[168:171], v[192:195], v[96:99], v[168:171]\n\t"
    "ds_read_b128 v[120:123], v4 offset:22528\n\t"
    "v_mfma_f32_16x16x32_bf16 v[172:175], v[196:199], v[96:99], v[172:175]\n\t"
    "ds_read_b128 v[124:127], v4 offset:24576\n\t"
    "s_waitcnt lgkmcnt(8)\n\t"
    "v_mfma_f32_16x16x32_bf16 v[180:183], v[196:199], v[100:103], v[180:183]\n\t"
    "v_mfma_f32_16x16x32_bf16 v[176:179], v[192:195], v[100:103], v[176:179]\n\t"
    "s_waitcnt lgkmcnt(7)\n\t"
    "v_mfma_f32_16x16x32_bf16 v[184:187], v[192:195], v[104:107], v[184:187]\n\t"
    "v_mfma_f32_16x16x32_bf16 v[188:191], v[196:199], v[104:107], v[188:191]\n\t"
    "s_waitcnt lgkmcnt(4)\n\t"
    "v_mfma_f32_16x16x32_bf16 v[156:159], v[204:207], v[108:111], v[156:159]\n\t"
    "v_mfma_f32_16x16x32_bf16 v[152:155], v[200:203], v[108:111], v[152:155]\n\t"
    "s_waitcnt lgkmcnt(0)\n\t"
    "s_mov_b32 s68, -110592\n\t"
    "s_barrier\n\t"
    "v_add_u32 v1, s68, v1\n\t"
    "v_add_u32 v2, s68, v2\n\t"
    "v_add_u32 v3, s68, v3\n\t"
    "v_add_u32 v4, s68, v4\n\t"
    "v_mfma_f32_16x16x32_bf16 v[160:163], v[200:203], v[112:115], v[160:163]\n\t"
    "ds_read_b128 v[32:35], v1 offset:0\n\t"
    "ds_read_b128 v[36:39], v1 offset:512\n\t"
    "v_mfma_f32_16x16x32_bf16 v[164:167], v[204:207], v[112:115], v[164:167]\n\t"
    "ds_read_b128 v[88:91], v3 offset:16384\n\t"
    "v_mfma_f32_16x16x32_bf16 v[172:175], v[204:207], v[116:119], v[172:175]\n\t"
    "ds_read_b128 v[92:95], v3 offset:18432\n\t"
    "v_mfma_f32_16x16x32_bf16 v[168:171], v[200:203], v[116:119], v[168:171]\n\t"
    "v_mfma_f32_16x16x32_bf16 v[176:179], v[200:203], v[120:123], v[176:179]\n\t"
    "ds_read_b128 v[96:99], v3 offset:20480\n\t"
    "v_mfma_f32_16x16x32_bf16 v[180:183], v[204:207], v[120:123], v[180:183]\n\t"
    "v_mfma_f32_16x16x32_bf16 v[188:191], v[204:207], v[124:127], v[188:191]\n\t"
    "ds_read_b128 v[100:103], v3 offset:22528\n\t"
    "v_mfma_f32_16x16x32_bf16 v[184:187], v[200:203], v[124:127], v[184:187]\n\t"
    "s_waitcnt lgkmcnt(3)\n\t"
    "v_mfma_f32_16x16x32_bf16 v[152:155], v[32:35], v[88:91], v[152:155]\n\t"
    "ds_read_b128 v[104:107], v3 offset:24576\n\t"
    "ds_read_b128 v[40:43], v2 offset:0\n\t"
    "v_mfma_f32_16x16x32_bf16 v[156:159], v[36:39], v[88:91], v[156:159]\n\t"
    "ds_read_b128 v[44:47], v2 offset:512\n\t"
    "ds_read_b128 v[108:111], v4 offset:16384\n\t"
    "s_waitcnt lgkmcnt(6)\n\t"
    "v_mfma_f32_16x16x32_bf16 v[164:167], v[36:39], v[92:95], v[164:167]\n\t"
    "ds_read_b128 v[112:115], v4 offset:18432\n\t"
    "v_mfma_f32_16x16x32_bf16 v[160:163], v[32:35], v[92:95], v[160:163]\n\t"
    "ds_read_b128 v[116:119], v4 offset:20480\n\t"
    "s_waitcnt lgkmcnt(7)\n\t"
    "v_mfma_f32_16x16x32_bf16 v[168:171], v[32:35], v[96:99], v[168:171]\n\t"
    "ds_read_b128 v[120:123], v4 offset:22528\n\t"
    "v_mfma_f32_16x16x32_bf16 v[172:175], v[36:39], v[96:99], v[172:175]\n\t"
    "ds_read_b128 v[124:127], v4 offset:24576\n\t"
    "s_waitcnt lgkmcnt(8)\n\t"
    "v_mfma_f32_16x16x32_bf16 v[180:183], v[36:39], v[100:103], v[180:183]\n\t"
    "v_mfma_f32_16x16x32_bf16 v[176:179], v[32:35], v[100:103], v[176:179]\n\t"
    "s_waitcnt lgkmcnt(7)\n\t"
    "v_mfma_f32_16x16x32_bf16 v[184:187], v[32:35], v[104:107], v[184:187]\n\t"
    "v_mfma_f32_16x16x32_bf16 v[188:191], v[36:39], v[104:107], v[188:191]\n\t"
    "s_waitcnt lgkmcnt(4)\n\t"
    "v_mfma_f32_16x16x32_bf16 v[156:159], v[44:47], v[108:111], v[156:159]\n\t"
    "v_mfma_f32_16x16x32_bf16 v[152:155], v[40:43], v[108:111], v[152:155]\n\t"
    "s_waitcnt lgkmcnt(0)\n\t"
    "s_mov_b32 s68, 36864\n\t"
    "v_mfma_f32_16x16x32_bf16 v[160:163], v[40:43], v[112:115], v[160:163]\n\t"
    "v_mfma_f32_16x16x32_bf16 v[164:167], v[44:47], v[112:115], v[164:167]\n\t"
    "v_mfma_f32_16x16x32_bf16 v[172:175], v[44:47], v[116:119], v[172:175]\n\t"
    "v_mfma_f32_16x16x32_bf16 v[168:171], v[40:43], v[116:119], v[168:171]\n\t"
    "v_mfma_f32_16x16x32_bf16 v[176:179], v[40:43], v[120:123], v[176:179]\n\t"
    "v_mfma_f32_16x16x32_bf16 v[180:183], v[44:47], v[120:123], v[180:183]\n\t"
    "v_mfma_f32_16x16x32_bf16 v[188:191], v[44:47], v[124:127], v[188:191]\n\t"
    "v_mfma_f32_16x16x32_bf16 v[184:187], v[40:43], v[124:127], v[184:187]\n\t"
    "v_mov_b32 v5, v9\n\t"
    "v_xor_b32 v6, 64, v5\n\t"
    "v_lshrrev_b32 v14, 2, v12\n\t"
    "v_lshl_add_u32 v14, v13, 3, v14\n\t"
    "v_mul_u32_u24 v14, 576, v14\n\t"
    "v_and_b32 v15, 3, v12\n\t"
    "v_lshl_add_u32 v14, v15, 3, v14\n\t"
    "v_and_b32 v15, 1, v13\n\t"
    "v_lshlrev_b32 v15, 5, v15\n\t"
    "v_add_u32 v1, v14, v15\n\t"
    "v_sub_u32 v2, v14, v15\n\t"
    "v_add_u32 v3, 73728, v1\n\t"
    "v_add_u32 v4, 73728, v2\n\t"
    "v_add_u32 v1, 36864, v1\n\t"
    "v_add_u32 v2, 36864, v2\n\t"
    "s_barrier\n\t"
    "ds_read_b64_tr_b16 v[16:17], v1 offset:0\n\t"
    "ds_read_b64_tr_b16 v[18:19], v1 offset:2304\n\t"
    "ds_read_b64_tr_b16 v[20:21], v2 offset:32\n\t"
    "ds_read_b64_tr_b16 v[22:23], v2 offset:2336\n\t"
    "s_lshl_b32 s68, s64, 7\n\t"
    "s_add_u32 s68, s68, 147456\n\t"
    "v_lshlrev_b32 v14, 5, v13\n\t"
    "v_add_u32 v14, s68, v14\n\t"
    "v_cmp_eq_u32 vcc, 8, v12\n\t"
    "s_nop 1\n\t"
    "s_and_saveexec_b64 s[70:71], vcc\n\t"
    "ds_write_b128 v14, v[184:187]\n\t"
    "ds_write_b128 v14, v[188:191] offset:16\n\t"
    "s_mov_b64 exec, s[70:71]\n\t"
    "v_lshrrev_b32 v14, 2, v12\n\t"
    "v_and_b32 v15, 3, v12\n\t"
    "v_lshl_add_u32 v14, v14, 3, v15\n\t"
    "v_lshl_add_u32 v14, v14, 2, s68\n\t"
    "s_waitcnt lgkmcnt(0)\n\t"
    "ds_read_b32 v7, v14\n\t"
    "ds_read_b32 v8, v14 offset:16\n\t"
    "s_mov_b32 s68, 73728\n\t"
    "s_mov_b32 s69, -73728\n\t"
    "s_mov_b32 s66, 0\n\t"
    "s_waitcnt lgkmcnt(2)\n\t"
    "v_mfma_f32_16x16x32_bf16 v[48:51], v[16:19], v[32:35], 0\n\t"
    "ds_read_b64_tr_b16 v[24:25], v1 offset:64\n\t"
    "ds_read_b64_tr_b16 v[26:27], v1 offset:2368\n\t"
    "s_mul_i32 s74, s50, 72\n\t"
    "v_add_u32 v14, s74, v12\n\t"
    "v_lshrrev_b32 v15, 4, v14\n\t"
    "v_lshlrev_b32 v15, 10, v15\n\t"
    "v_and_b32 v14, 15, v14\n\t"
    "v_lshl_add_u32 v14, v13, 4, v14\n\t"
    "v_mfma_f32_16x16x32_bf16 v[56:59], v[16:19], v[36:39], 0\n\t"
    "ds_read_b64_tr_b16 v[28:29], v2 offset:96\n\t"
    "ds_read_b64_tr_b16 v[30:31], v2 offset:2400\n\t"
    "s_waitcnt lgkmcnt(4)\n\t"
    "v_mfma_f32_16x16x32_bf16 v[60:63], v[20:23], v[36:39], 0\n\t"
    "ds_read_b64_tr_b16 v[16:17], v1 offset:128\n\t"
    "v_lshl_add_u32 v14, v14, 4, v15\n\t"
    "v_mov_b32 v15, 0x40000000\n\t"
    "v_cmp_gt_u32 vcc, 8, v12\n\t"
    "s_nop 1\n\t"
    "v_cndmask_b32 v15, v15, v14, vcc\n\t"
    "v_mfma_f32_16x16x32_bf16 v[52:55], v[20:23], v[32:35], 0\n\t"
    "ds_read_b64_tr_b16 v[18:19], v1 offset:2432\n\t"
    "s_waitcnt lgkmcnt(4)\n\t"
    "v_mfma_f32_16x16x32_bf16 v[64:67], v[24:27], v[32:35], 0\n\t"
    "ds_read_b64_tr_b16 v[20:21], v2 offset:160\n\t"
    "s_lshl_b32 s74, s48, 2\n\t"
    "s_add_u32 s74, s74, s64\n\t"
    "s_mul_i32 s74, s74, 36864\n\t"
    "s_add_u32 s75, s74, 4096\n\t"
    "v_cvt_pk_bf16_f32 v152, v152, v153\n\t"
    "v_mfma_f32_16x16x32_bf16 v[72:75], v[24:27], v[36:39], 0\n\t"
    "ds_read_b64_tr_b16 v[22:23], v2 offset:2464\n\t"
    "s_waitcnt lgkmcnt(4)\n\t"
    "v_mfma_f32_16x16x32_bf16 v[76:79], v[28:31], v[36:39], 0\n\t"
    "ds_read_b64_tr_b16 v[24:25], v1 offset:192\n\t"
    "v_cvt_pk_bf16_f32 v153, v154, v155\n\t"
    "v_cvt_pk_bf16_f32 v154, v156, v157\n\t"
    "v_cvt_pk_bf16_f32 v155, v158, v159\n\t"
    "buffer_store_dwordx4 v[152:155], v14, s[56:59], s74 offen offset:0 sc1\n\t"
    "v_cvt_pk_bf16_f32 v160, v160, v161\n\t"
    "v_mfma_f32_16x16x32_bf16 v[68:71], v[28:31], v[32:35], 0\n\t"
    "ds_read_b64_tr_b16 v[26:27], v1 offset:2496\n\t"
    "s_waitcnt lgkmcnt(4)\n\t"
    "v_mfma_f32_16x16x32_bf16 v[80:83], v[16:19], v[32:35], 0\n\t"
    "ds_read_b64_tr_b16 v[28:29], v2 offset:224\n\t"
    "v_cvt_pk_bf16_f32 v161, v162, v163\n\t"
    "v_cvt_pk_bf16_f32 v162, v164, v165\n\t"
    "v_cvt_pk_bf16_f32 v163, v166, v167\n\t"
    "buffer_store_dwordx4 v[160:163], v14, s[56:59], s74 offen offset:1024 sc1\n\t"
    "v_cvt_pk_bf16_f32 v168, v168, v169\n\t"
    "v_mfma_f32_16x16x32_bf16 v[88:91], v[16:19], v[36:39], 0\n\t"
    "ds_read_b64_tr_b16 v[30:31], v2 offset:2528\n\t"
    "s_waitcnt lgkmcnt(4)\n\t"
    "v_mfma_f32_16x16x32_bf16 v[92:95], v[20:23], v[36:39], 0\n\t"
    "ds_read_b64_tr_b16 v[16:17], v1 offset:256\n\t"
    "v_cvt_pk_bf16_f32 v169, v170, v171\n\t"
    "v_cvt_pk_bf16_f32 v170, v172, v173\n\t"
    "v_cvt_pk_bf16_f32 v171, v174, v175\n\t"
    "buffer_store_dwordx4 v[168:171], v14, s[56:59], s74 offen offset:2048 sc1\n\t"
    "v_cvt_pk_bf16_f32 v176, v176, v177\n\t"
    "v_mfma_f32_16x16x32_bf16 v[84:87], v[20:23], v[32:35], 0\n\t"
    "ds_read_b64_tr_b16 v[18:19], v1 offset:2560\n\t"
    "s_waitcnt lgkmcnt(4)\n\t"
    "v_mfma_f32_16x16x32_bf16 v[96:99], v[24:27], v[32:35], 0\n\t"
    "ds_read_b64_tr_b16 v[20:21], v2 offset:288\n\t"
    "v_cvt_pk_bf16_f32 v177, v178, v179\n\t"
    "v_cvt_pk_bf16_f32 v178, v180, v181\n\t"
    "v_cvt_pk_bf16_f32 v179, v182, v183\n\t"
    "buffer_store_dwordx4 v[176:179], v14, s[56:59], s74 offen offset:3072 sc1\n\t"
    "v_cvt_pk_bf16_f32 v184, v184, v185\n\t"
    "v_mfma_f32_16x16x32_bf16 v[104:107], v[24:27], v[36:39], 0\n\t"
    "ds_read_b64_tr_b16 v[22:23], v2 offset:2592\n\t"
    "s_waitcnt lgkmcnt(4)\n\t"
    "v_mfma_f32_16x16x32_bf16 v[108:111], v[28:31], v[36:39], 0\n\t"
    "ds_read_b64_tr_b16 v[24:25], v1 offset:320\n\t"
    "v_cvt_pk_bf16_f32 v185, v186, v187\n\t"
    "v_cvt_pk_bf16_f32 v186, v188, v189\n\t"
    "v_cvt_pk_bf16_f32 v187, v190, v191\n\t"
    "buffer_store_dwordx4 v[184:187], v15, s[56:59], s75 offen sc1\n\t"
    "v_mul_f32 v7, 0xbfb8aa3b, v7\n\t"
    "v_mfma_f32_16x16x32_bf16 v[100:103], v[28:31], v[32:35], 0\n\t"
    "ds_read_b64_tr_b16 v[26:27], v1 offset:2624\n\t"
    "s_waitcnt lgkmcnt(4)\n\t"
    "v_mfma_f32_16x16x32_bf16 v[112:115], v[16:19], v[32:35], 0\n\t"
    "ds_read_b64_tr_b16 v[28:29], v2 offset:352\n\t"
    "v_exp_f32 v7, v7\n\t"
    "s_nop 0\n\t"
    "v_add_f32 v7, 1.0, v7\n\t"
    "v_rcp_f32 v7, v7\n\t"
    "s_nop 0\n\t"
    "v_mfma_f32_16x16x32_bf16 v[120:123], v[16:19], v[36:39], 0\n\t"
    "ds_read_b64_tr_b16 v[30:31], v2 offset:2656\n\t"
    "s_waitcnt lgkmcnt(4)\n\t"
    "v_mfma_f32_16x16x32_bf16 v[124:127], v[20:23], v[36:39], 0\n\t"
    "ds_read_b64_tr_b16 v[16:17], v1 offset:384\n\t"
    "v_sub_f32 v9, 1.0, v7\n\t"
    "v_mul_f32 v7, s53, v7\n\t"
    "v_mul_f32 v8, 0xbfb8aa3b, v8\n\t"
    "v_exp_f32 v8, v8\n\t"
    "s_nop 0\n\t"
    "v_mfma_f32_16x16x32_bf16 v[116:119], v[20:23], v[32:35], 0\n\t"
    "ds_read_b64_tr_b16 v[18:19], v1 offset:2688\n\t"
    "s_waitcnt lgkmcnt(4)\n\t"
    "v_mfma_f32_16x16x32_bf16 v[128:131], v[24:27], v[32:35], 0\n\t"
    "ds_read_b64_tr_b16 v[20:21], v2 offset:416\n\t"
    "v_add_f32 v8, 1.0, v8\n\t"
    "v_rcp_f32 v8, v8\n\t"
    "s_nop 0\n\t"
    "v_sub_f32 v10, 1.0, v8\n\t"
    "v_mul_f32 v8, s53, v8\n\t"
    "v_mfma_f32_16x16x32_bf16 v[136:139], v[24:27], v[36:39], 0\n\t"
    "ds_read_b64_tr_b16 v[22:23], v2 offset:2720\n\t"
    "s_waitcnt lgkmcnt(4)\n\t"
    "v_mfma_f32_16x16x32_bf16 v[140:143], v[28:31], v[36:39], 0\n\t"
    "ds_read_b64_tr_b16 v[24:25], v1 offset:448\n\t"
    "v_mfma_f32_16x16x32_bf16 v[132:135], v[28:31], v[32:35], 0\n\t"
    "ds_read_b64_tr_b16 v[26:27], v1 offset:2752\n\t"
    "s_waitcnt lgkmcnt(4)\n\t"
    "v_mfma_f32_16x16x32_bf16 v[144:147], v[16:19], v[32:35], 0\n\t"
    "ds_read_b64_tr_b16 v[28:29], v2 offset:480\n\t"
    "v_mfma_f32_16x16x32_bf16 v[152:155], v[16:19], v[36:39], 0\n\t"
    "ds_read_b64_tr_b16 v[30:31], v2 offset:2784\n\t"
    "s_waitcnt lgkmcnt(4)\n\t"
    "v_mfma_f32_16x16x32_bf16 v[156:159], v[20:23], v[36:39], 0\n\t"
    "ds_read_b64_tr_b16 v[16:17], v1 offset:512\n\t"
    "v_mfma_f32_16x16x32_bf16 v[148:151], v[20:23], v[32:35], 0\n\t"
    "ds_read_b64_tr_b16 v[18:19], v1 offset:2816\n\t"
    "s_waitcnt lgkmcnt(4)\n\t"
    "v_mfma_f32_16x16x32_bf16 v[160:163], v[24:27], v[32:35], 0\n\t"
    "ds_read_b64_tr_b16 v[20:21], v2 offset:544\n\t"
    "v_mfma_f32_16x16x32_bf16 v[168:171], v[24:27], v[36:39], 0\n\t"
    "ds_read_b64_tr_b16 v[22:23], v2 offset:2848\n\t"
    "s_waitcnt lgkmcnt(4)\n\t"
    "v_mfma_f32_16x16x32_bf16 v[172:175], v[28:31], v[36:39], 0\n\t"
    "v_mfma_f32_16x16x32_bf16 v[164:167], v[28:31], v[32:35], 0\n\t"
    "s_waitcnt lgkmcnt(0)\n\t"
    "s_barrier\n\t"
    "v_add_u32 v1, s68, v1\n\t"
    "v_add_u32 v2, s68, v2\n\t"
    "v_add_u32 v5, s68, v5\n\t"
    "v_add_u32 v6, s68, v6\n\t"
    "v_mfma_f32_16x16x32_bf16 v[176:179], v[16:19], v[32:35], 0\n\t"
    "ds_read_b64_tr_b16 v[24:25], v3 offset:0\n\t"
    "ds_read_b64_tr_b16 v[26:27], v3 offset:2304\n\t"
    "v_mfma_f32_16x16x32_bf16 v[184:187], v[16:19], v[36:39], 0\n\t"
    "ds_read_b64_tr_b16 v[28:29], v4 offset:32\n\t"
    "ds_read_b64_tr_b16 v[30:31], v4 offset:2336\n\t"
    "v_mfma_f32_16x16x32_bf16 v[188:191], v[20:23], v[36:39], 0\n\t"
    "v_mfma_f32_16x16x32_bf16 v[180:183], v[20:23], v[32:35], 0\n\t"
    "s_waitcnt lgkmcnt(2)\n\t"
    "v_mfma_f32_16x16x32_bf16 v[48:51], v[24:27], v[40:43], v[48:51]\n\t"
    "ds_read_b64_tr_b16 v[16:17], v3 offset:64\n\t"
    "ds_read_b64_tr_b16 v[18:19], v3 offset:2368\n\t"
    "v_mfma_f32_16x16x32_bf16 v[56:59], v[24:27], v[44:47], v[56:59]\n\t"
    "ds_read_b64_tr_b16 v[20:21], v4 offset:96\n\t"
    "ds_read_b64_tr_b16 v[22:23], v4 offset:2400\n\t"
    "s_waitcnt lgkmcnt(4)\n\t"
    "v_mfma_f32_16x16x32_bf16 v[60:63], v[28:31], v[44:47], v[60:63]\n\t"
    "ds_read_b64_tr_b16 v[24:25], v3 offset:128\n\t"
    "v_mfma_f32_16x16x32_bf16 v[52:55], v[28:31], v[40:43], v[52:55]\n\t"
    "ds_read_b64_tr_b16 v[26:27], v3 offset:2432\n\t"
    "s_waitcnt lgkmcnt(4)\n\t"
    "v_mfma_f32_16x16x32_bf16 v[64:67], v[16:19], v[40:43], v[64:67]\n\t"
    "ds_read_b64_tr_b16 v[28:29], v4 offset:160\n\t"
    "v_mfma_f32_16x16x32_bf16 v[72:75], v[16:19], v[44:47], v[72:75]\n\t"
    "ds_read_b64_tr_b16 v[30:31], v4 offset:2464\n\t"
    "s_waitcnt lgkmcnt(4)\n\t"
    "v_mfma_f32_16x16x32_bf16 v[76:79], v[20:23], v[44:47], v[76:79]\n\t"
    "ds_read_b64_tr_b16 v[16:17], v3 offset:192\n\t"
    "v_mfma_f32_16x16x32_bf16 v[68:71], v[20:23], v[40:43], v[68:71]\n\t"
    "ds_read_b64_tr_b16 v[18:19], v3 offset:2496\n\t"
    "s_waitcnt lgkmcnt(4)\n\t"
    "v_mfma_f32_16x16x32_bf16 v[80:83], v[24:27], v[40:43], v[80:83]\n\t"
    "ds_read_b64_tr_b16 v[20:21], v4 offset:224\n\t"
    "v_mfma_f32_16x16x32_bf16 v[88:91], v[24:27], v[44:47], v[88:91]\n\t"
    "ds_read_b64_tr_b16 v[22:23], v4 offset:2528\n\t"
    "s_waitcnt lgkmcnt(4)\n\t"
    "v_mfma_f32_16x16x32_bf16 v[92:95], v[28:31], v[44:47], v[92:95]\n\t"
    "ds_read_b64_tr_b16 v[24:25], v3 offset:256\n\t"
    "v_mfma_f32_16x16x32_bf16 v[84:87], v[28:31], v[40:43], v[84:87]\n\t"
    "ds_read_b64_tr_b16 v[26:27], v3 offset:2560\n\t"
    "s_waitcnt lgkmcnt(4)\n\t"
    "v_mfma_f32_16x16x32_bf16 v[96:99], v[16:19], v[40:43], v[96:99]\n\t"
    "ds_read_b64_tr_b16 v[28:29], v4 offset:288\n\t"
    "v_mfma_f32_16x16x32_bf16 v[104:107], v[16:19], v[44:47], v[104:107]\n\t"
    "ds_read_b64_tr_b16 v[30:31], v4 offset:2592\n\t"
    "s_waitcnt lgkmcnt(4)\n\t"
    "v_mfma_f32_16x16x32_bf16 v[108:111], v[20:23], v[44:47], v[108:111]\n\t"
    "ds_read_b64_tr_b16 v[16:17], v3 offset:320\n\t"
    "v_mfma_f32_16x16x32_bf16 v[100:103], v[20:23], v[40:43], v[100:103]\n\t"
    "ds_read_b64_tr_b16 v[18:19], v3 offset:2624\n\t"
    "s_waitcnt lgkmcnt(4)\n\t"
    "v_mfma_f32_16x16x32_bf16 v[112:115], v[24:27], v[40:43], v[112:115]\n\t"
    "ds_read_b64_tr_b16 v[20:21], v4 offset:352\n\t"
    "v_mfma_f32_16x16x32_bf16 v[120:123], v[24:27], v[44:47], v[120:123]\n\t"
    "ds_read_b64_tr_b16 v[22:23], v4 offset:2656\n\t"
    "s_waitcnt lgkmcnt(4)\n\t"
    "v_mfma_f32_16x16x32_bf16 v[124:127], v[28:31], v[44:47], v[124:127]\n\t"
    "ds_read_b64_tr_b16 v[24:25], v3 offset:384\n\t"
    "v_mfma_f32_16x16x32_bf16 v[116:119], v[28:31], v[40:43], v[116:119]\n\t"
    "ds_read_b64_tr_b16 v[26:27], v3 offset:2688\n\t"
    "s_waitcnt lgkmcnt(4)\n\t"
    "v_mfma_f32_16x16x32_bf16 v[128:131], v[16:19], v[40:43], v[128:131]\n\t"
    "ds_read_b64_tr_b16 v[28:29], v4 offset:416\n\t"
    "v_mfma_f32_16x16x32_bf16 v[136:139], v[16:19], v[44:47], v[136:139]\n\t"
    "ds_read_b64_tr_b16 v[30:31], v4 offset:2720\n\t"
    "s_waitcnt lgkmcnt(4)\n\t"
    "v_mfma_f32_16x16x32_bf16 v[140:143], v[20:23], v[44:47], v[140:143]\n\t"
    "ds_read_b64_tr_b16 v[16:17], v3 offset:448\n\t"
    "v_mfma_f32_16x16x32_bf16 v[132:135], v[20:23], v[40:43], v[132:135]\n\t"
    "ds_read_b64_tr_b16 v[18:19], v3 offset:2752\n\t"
    "s_waitcnt lgkmcnt(4)\n\t"
    "v_mfma_f32_16x16x32_bf16 v[144:147], v[24:27], v[40:43], v[144:147]\n\t"
    "ds_read_b64_tr_b16 v[20:21], v4 offset:480\n\t"
    "v_mfma_f32_16x16x32_bf16 v[152:155], v[24:27], v[44:47], v[152:155]\n\t"
    "ds_read_b64_tr_b16 v[22:23], v4 offset:2784\n\t"
    "s_waitcnt lgkmcnt(4)\n\t"
    "v_mfma_f32_16x16x32_bf16 v[156:159], v[28:31], v[44:47], v[156:159]\n\t"
    "ds_read_b64_tr_b16 v[24:25], v3 offset:512\n\t"
    "v_mfma_f32_16x16x32_bf16 v[148:151], v[28:31], v[40:43], v[148:151]\n\t"
    "ds_read_b64_tr_b16 v[26:27], v3 offset:2816\n\t"
    "s_waitcnt lgkmcnt(4)\n\t"
    "v_mfma_f32_16x16x32_bf16 v[160:163], v[16:19], v[40:43], v[160:163]\n\t"
    "ds_read_b64_tr_b16 v[28:29], v4 offset:544\n\t"
    "v_mfma_f32_16x16x32_bf16 v[168:171], v[16:19], v[44:47], v[168:171]\n\t"
    "ds_read_b64_tr_b16 v[30:31], v4 offset:2848\n\t"
    "s_waitcnt lgkmcnt(4)\n\t"
    "v_mfma_f32_16x16x32_bf16 v[172:175], v[20:23], v[44:47], v[172:175]\n\t"
    "v_mfma_f32_16x16x32_bf16 v[164:167], v[20:23], v[40:43], v[164:167]\n\t"
    "s_waitcnt lgkmcnt(0)\n\t"
    "s_barrier\n\t"
    "v_add_u32 v3, s69, v3\n\t"
    "v_add_u32 v4, s69, v4\n\t"
    "s_sub_u32 s68, 0, s68\n\t"
    "s_sub_u32 s69, 0, s69\n\t"
    "v_mfma_f32_16x16x32_bf16 v[176:179], v[24:27], v[40:43], v[176:179]\n\t"
    "ds_read_b64_tr_b16 v[16:17], v1 offset:0\n\t"
    "ds_read_b64_tr_b16 v[18:19], v1 offset:2304\n\t"
    "v_mfma_f32_16x16x32_bf16 v[184:187], v[24:27], v[44:47], v[184:187]\n\t"
    "ds_read_b64_tr_b16 v[20:21], v2 offset:32\n\t"
    "ds_read_b64_tr_b16 v[22:23], v2 offset:2336\n\t"
    "v_mfma_f32_16x16x32_bf16 v[188:191], v[28:31], v[44:47], v[188:191]\n\t"
    "v_mfma_f32_16x16x32_bf16 v[180:183], v[28:31], v[40:43], v[180:183]\n\t"
    "s_waitcnt lgkmcnt(2)\n\t"
    "v_mfma_f32_16x16x32_bf16 v[48:51], v[16:19], v[192:195], v[48:51]\n\t"
    "ds_read_b64_tr_b16 v[24:25], v1 offset:64\n\t"
    "ds_read_b64_tr_b16 v[26:27], v1 offset:2368\n\t"
    "v_mfma_f32_16x16x32_bf16 v[56:59], v[16:19], v[196:199], v[56:59]\n\t"
    "ds_read_b64_tr_b16 v[28:29], v2 offset:96\n\t"
    "ds_read_b64_tr_b16 v[30:31], v2 offset:2400\n\t"
    "s_waitcnt lgkmcnt(4)\n\t"
    "v_mfma_f32_16x16x32_bf16 v[60:63], v[20:23], v[196:199], v[60:63]\n\t"
    "ds_read_b64_tr_b16 v[16:17], v1 offset:128\n\t"
    "v_mfma_f32_16x16x32_bf16 v[52:55], v[20:23], v[192:195], v[52:55]\n\t"
    "ds_read_b64_tr_b16 v[18:19], v1 offset:2432\n\t"
    "s_waitcnt lgkmcnt(4)\n\t"
    "v_mfma_f32_16x16x32_bf16 v[64:67], v[24:27], v[192:195], v[64:67]\n\t"
    "ds_read_b64_tr_b16 v[20:21], v2 offset:160\n\t"
    "v_mfma_f32_16x16x32_bf16 v[72:75], v[24:27], v[196:199], v[72:75]\n\t"
    "ds_read_b64_tr_b16 v[22:23], v2 offset:2464\n\t"
    "s_waitcnt lgkmcnt(4)\n\t"
    "v_mfma_f32_16x16x32_bf16 v[76:79], v[28:31], v[196:199], v[76:79]\n\t"
    "ds_read_b64_tr_b16 v[24:25], v1 offset:192\n\t"
    "v_mfma_f32_16x16x32_bf16 v[68:71], v[28:31], v[192:195], v[68:71]\n\t"
    "ds_read_b64_tr_b16 v[26:27], v1 offset:2496\n\t"
    "s_waitcnt lgkmcnt(4)\n\t"
    "v_mfma_f32_16x16x32_bf16 v[80:83], v[16:19], v[192:195], v[80:83]\n\t"
    "ds_read_b64_tr_b16 v[28:29], v2 offset:224\n\t"
    "v_mfma_f32_16x16x32_bf16 v[88:91], v[16:19], v[196:199], v[88:91]\n\t"
    "ds_read_b64_tr_b16 v[30:31], v2 offset:2528\n\t"
    "s_waitcnt lgkmcnt(4)\n\t"
    "v_mfma_f32_16x16x32_bf16 v[92:95], v[20:23], v[196:199], v[92:95]\n\t"
    "ds_read_b64_tr_b16 v[16:17], v1 offset:256\n\t"
    "v_mfma_f32_16x16x32_bf16 v[84:87], v[20:23], v[192:195], v[84:87]\n\t"
    "ds_read_b64_tr_b16 v[18:19], v1 offset:2560\n\t"
    "s_waitcnt lgkmcnt(4)\n\t"
    "v_mfma_f32_16x16x32_bf16 v[96:99], v[24:27], v[192:195], v[96:99]\n\t"
    "ds_read_b64_tr_b16 v[20:21], v2 offset:288\n\t"
    "v_mfma_f32_16x16x32_bf16 v[104:107], v[24:27], v[196:199], v[104:107]\n\t"
    "ds_read_b64_tr_b16 v[22:23], v2 offset:2592\n\t"
    "s_waitcnt lgkmcnt(4)\n\t"
    "v_mfma_f32_16x16x32_bf16 v[108:111], v[28:31], v[196:199], v[108:111]\n\t"
    "ds_read_b64_tr_b16 v[24:25], v1 offset:320\n\t"
    "v_mfma_f32_16x16x32_bf16 v[100:103], v[28:31], v[192:195], v[100:103]\n\t"
    "ds_read_b64_tr_b16 v[26:27], v1 offset:2624\n\t"
    "s_waitcnt lgkmcnt(4)\n\t"
    "v_mfma_f32_16x16x32_bf16 v[112:115], v[16:19], v[192:195], v[112:115]\n\t"
    "ds_read_b64_tr_b16 v[28:29], v2 offset:352\n\t"
    "v_mfma_f32_16x16x32_bf16 v[120:123], v[16:19], v[196:199], v[120:123]\n\t"
    "ds_read_b64_tr_b16 v[30:31], v2 offset:2656\n\t"
    "s_waitcnt lgkmcnt(4)\n\t"
    "v_mfma_f32_16x16x32_bf16 v[124:127], v[20:23], v[196:199], v[124:127]\n\t"
    "ds_read_b64_tr_b16 v[16:17], v1 offset:384\n\t"
    "v_mfma_f32_16x16x32_bf16 v[116:119], v[20:23], v[192:195], v[116:119]\n\t"
    "ds_read_b64_tr_b16 v[18:19], v1 offset:2688\n\t"
    "s_waitcnt lgkmcnt(4)\n\t"
    "v_mfma_f32_16x16x32_bf16 v[128:131], v[24:27], v[192:195], v[128:131]\n\t"
    "ds_read_b64_tr_b16 v[20:21], v2 offset:416\n\t"
    "v_mfma_f32_16x16x32_bf16 v[136:139], v[24:27], v[196:199], v[136:139]\n\t"
    "ds_read_b64_tr_b16 v[22:23], v2 offset:2720\n\t"
    "s_waitcnt lgkmcnt(4)\n\t"
    "v_mfma_f32_16x16x32_bf16 v[140:143], v[28:31], v[196:199], v[140:143]\n\t"
    "ds_read_b64_tr_b16 v[24:25], v1 offset:448\n\t"
    "v_mfma_f32_16x16x32_bf16 v[132:135], v[28:31], v[192:195], v[132:135]\n\t"
    "ds_read_b64_tr_b16 v[26:27], v1 offset:2752\n\t"
    "s_waitcnt lgkmcnt(4)\n\t"
    "v_mfma_f32_16x16x32_bf16 v[144:147], v[16:19], v[192:195], v[144:147]\n\t"
    "ds_read_b64_tr_b16 v[28:29], v2 offset:480\n\t"
    "v_mfma_f32_16x16x32_bf16 v[152:155], v[16:19], v[196:199], v[152:155]\n\t"
    "ds_read_b64_tr_b16 v[30:31], v2 offset:2784\n\t"
    "s_waitcnt lgkmcnt(4)\n\t"
    "v_mfma_f32_16x16x32_bf16 v[156:159], v[20:23], v[196:199], v[156:159]\n\t"
    "ds_read_b64_tr_b16 v[16:17], v1 offset:512\n\t"
    "v_mfma_f32_16x16x32_bf16 v[148:151], v[20:23], v[192:195], v[148:151]\n\t"
    "ds_read_b64_tr_b16 v[18:19], v1 offset:2816\n\t"
    "s_waitcnt lgkmcnt(4)\n\t"
    "v_mfma_f32_16x16x32_bf16 v[160:163], v[24:27], v[192:195], v[160:163]\n\t"
    "ds_read_b64_tr_b16 v[20:21], v2 offset:544\n\t"
    "v_mfma_f32_16x16x32_bf16 v[168:171], v[24:27], v[196:199], v[168:171]\n\t"
    "ds_read_b64_tr_b16 v[22:23], v2 offset:2848\n\t"
    "s_waitcnt lgkmcnt(4)\n\t"
    "v_mfma_f32_16x16x32_bf16 v[172:175], v[28:31], v[196:199], v[172:175]\n\t"
    "v_mfma_f32_16x16x32_bf16 v[164:167], v[28:31], v[192:195], v[164:167]\n\t"
    "s_waitcnt lgkmcnt(0)\n\t"
    "s_waitcnt vmcnt(0)\n\t"
    "s_barrier\n\t"
    "v_add_u32 v1, s68, v1\n\t"
    "v_add_u32 v2, s68, v2\n\t"
    "v_add_u32 v5, s68, v5\n\t"
    "v_add_u32 v6, s68, v6\n\t"
    "v_mfma_f32_16x16x32_bf16 v[176:179], v[16:19], v[192:195], v[176:179]\n\t"
    "ds_read_b64_tr_b16 v[24:25], v3 offset:0\n\t"
    "ds_read_b64_tr_b16 v[26:27], v3 offset:2304\n\t"
    "v_mfma_f32_16x16x32_bf16 v[184:187], v[16:19], v[196:199], v[184:187]\n\t"
    "ds_read_b64_tr_b16 v[28:29], v4 offset:32\n\t"
    "ds_read_b64_tr_b16 v[30:31], v4 offset:2336\n\t"
    "v_mfma_f32_16x16x32_bf16 v[188:191], v[20:23], v[196:199], v[188:191]\n\t"
    "v_mfma_f32_16x16x32_bf16 v[180:183], v[20:23], v[192:195], v[180:183]\n\t"
    "s_waitcnt lgkmcnt(2)\n\t"
    "v_mfma_f32_16x16x32_bf16 v[48:51], v[24:27], v[200:203], v[48:51]\n\t"
    "ds_read_b64_tr_b16 v[16:17], v3 offset:64\n\t"
    "ds_read_b64_tr_b16 v[18:19], v3 offset:2368\n\t"
    "v_mfma_f32_16x16x32_bf16 v[56:59], v[24:27], v[204:207], v[56:59]\n\t"
    "ds_read_b64_tr_b16 v[20:21], v4 offset:96\n\t"
    "ds_read_b64_tr_b16 v[22:23], v4 offset:2400\n\t"
    "s_waitcnt lgkmcnt(4)\n\t"
    "v_mfma_f32_16x16x32_bf16 v[60:63], v[28:31], v[204:207], v[60:63]\n\t"
    "ds_read_b64_tr_b16 v[24:25], v3 offset:128\n\t"
    "v_mfma_f32_16x16x32_bf16 v[52:55], v[28:31], v[200:203], v[52:55]\n\t"
    "ds_read_b64_tr_b16 v[26:27], v3 offset:2432\n\t"
    "s_waitcnt lgkmcnt(4)\n\t"
    "v_mfma_f32_16x16x32_bf16 v[64:67], v[16:19], v[200:203], v[64:67]\n\t"
    "ds_read_b64_tr_b16 v[28:29], v4 offset:160\n\t"
    "v_mfma_f32_16x16x32_bf16 v[72:75], v[16:19], v[204:207], v[72:75]\n\t"
    "ds_read_b64_tr_b16 v[30:31], v4 offset:2464\n\t"
    "s_waitcnt lgkmcnt(4)\n\t"
    "v_mfma_f32_16x16x32_bf16 v[76:79], v[20:23], v[204:207], v[76:79]\n\t"
    "ds_read_b64_tr_b16 v[16:17], v3 offset:192\n\t"
    "v_mfma_f32_16x16x32_bf16 v[68:71], v[20:23], v[200:203], v[68:71]\n\t"
    "ds_read_b64_tr_b16 v[18:19], v3 offset:2496\n\t"
    "s_waitcnt lgkmcnt(4)\n\t"
    "v_mfma_f32_16x16x32_bf16 v[80:83], v[24:27], v[200:203], v[80:83]\n\t"
    "ds_read_b64_tr_b16 v[20:21], v4 offset:224\n\t"
    "v_mfma_f32_16x16x32_bf16 v[88:91], v[24:27], v[204:207], v[88:91]\n\t"
    "ds_read_b64_tr_b16 v[22:23], v4 offset:2528\n\t"
    "s_waitcnt lgkmcnt(4)\n\t"
    "v_mfma_f32_16x16x32_bf16 v[92:95], v[28:31], v[204:207], v[92:95]\n\t"
    "ds_read_b64_tr_b16 v[24:25], v3 offset:256\n\t"
    "v_mfma_f32_16x16x32_bf16 v[84:87], v[28:31], v[200:203], v[84:87]\n\t"
    "ds_read_b64_tr_b16 v[26:27], v3 offset:2560\n\t"
    "s_waitcnt lgkmcnt(4)\n\t"
    "v_mfma_f32_16x16x32_bf16 v[96:99], v[16:19], v[200:203], v[96:99]\n\t"
    "ds_read_b64_tr_b16 v[28:29], v4 offset:288\n\t"
    "v_mfma_f32_16x16x32_bf16 v[104:107], v[16:19], v[204:207], v[104:107]\n\t"
    "ds_read_b64_tr_b16 v[30:31], v4 offset:2592\n\t"
    "s_waitcnt lgkmcnt(4)\n\t"
    "v_mfma_f32_16x16x32_bf16 v[108:111], v[20:23], v[204:207], v[108:111]\n\t"
    "ds_read_b64_tr_b16 v[16:17], v3 offset:320\n\t"
    "v_mfma_f32_16x16x32_bf16 v[100:103], v[20:23], v[200:203], v[100:103]\n\t"
    "ds_read_b64_tr_b16 v[18:19], v3 offset:2624\n\t"
    "s_waitcnt lgkmcnt(4)\n\t"
    "v_mfma_f32_16x16x32_bf16 v[112:115], v[24:27], v[200:203], v[112:115]\n\t"
    "ds_read_b64_tr_b16 v[20:21], v4 offset:352\n\t"
    "v_mfma_f32_16x16x32_bf16 v[120:123], v[24:27], v[204:207], v[120:123]\n\t"
    "ds_read_b64_tr_b16 v[22:23], v4 offset:2656\n\t"
    "s_waitcnt lgkmcnt(4)\n\t"
    "v_mfma_f32_16x16x32_bf16 v[124:127], v[28:31], v[204:207], v[124:127]\n\t"
    "ds_read_b64_tr_b16 v[24:25], v3 offset:384\n\t"
    "v_mfma_f32_16x16x32_bf16 v[116:119], v[28:31], v[200:203], v[116:119]\n\t"
    "ds_read_b64_tr_b16 v[26:27], v3 offset:2688\n\t"
    "s_waitcnt lgkmcnt(4)\n\t"
    "v_mfma_f32_16x16x32_bf16 v[128:131], v[16:19], v[200:203], v[128:131]\n\t"
    "ds_read_b64_tr_b16 v[28:29], v4 offset:416\n\t"
    "v_mfma_f32_16x16x32_bf16 v[136:139], v[16:19], v[204:207], v[136:139]\n\t"
    "ds_read_b64_tr_b16 v[30:31], v4 offset:2720\n\t"
    "s_waitcnt lgkmcnt(4)\n\t"
    "v_mfma_f32_16x16x32_bf16 v[140:143], v[20:23], v[204:207], v[140:143]\n\t"
    "ds_read_b64_tr_b16 v[16:17], v3 offset:448\n\t"
    "v_mfma_f32_16x16x32_bf16 v[132:135], v[20:23], v[200:203], v[132:135]\n\t"
    "ds_read_b64_tr_b16 v[18:19], v3 offset:2752\n\t"
    "s_waitcnt lgkmcnt(4)\n\t"
    "v_mfma_f32_16x16x32_bf16 v[144:147], v[24:27], v[200:203], v[144:147]\n\t"
    "ds_read_b64_tr_b16 v[20:21], v4 offset:480\n\t"
    "v_mfma_f32_16x16x32_bf16 v[152:155], v[24:27], v[204:207], v[152:155]\n\t"
    "ds_read_b64_tr_b16 v[22:23], v4 offset:2784\n\t"
    "s_waitcnt lgkmcnt(4)\n\t"
    "v_mfma_f32_16x16x32_bf16 v[156:159], v[28:31], v[204:207], v[156:159]\n\t"
    "ds_read_b64_tr_b16 v[24:25], v3 offset:512\n\t"
    "v_mfma_f32_16x16x32_bf16 v[148:151], v[28:31], v[200:203], v[148:151]\n\t"
    "ds_read_b64_tr_b16 v[26:27], v3 offset:2816\n\t"
    "s_waitcnt lgkmcnt(4)\n\t"
    "v_mfma_f32_16x16x32_bf16 v[160:163], v[16:19], v[200:203], v[160:163]\n\t"
    "ds_read_b64_tr_b16 v[28:29], v4 offset:544\n\t"
    "v_mfma_f32_16x16x32_bf16 v[168:171], v[16:19], v[204:207], v[168:171]\n\t"
    "ds_read_b64_tr_b16 v[30:31], v4 offset:2848\n\t"
    "s_waitcnt lgkmcnt(4)\n\t"
    "v_mfma_f32_16x16x32_bf16 v[172:175], v[20:23], v[204:207], v[172:175]\n\t"
    "v_mfma_f32_16x16x32_bf16 v[164:167], v[20:23], v[200:203], v[164:167]\n\t"
    "s_waitcnt lgkmcnt(0)\n\t"
    "s_barrier\n\t"
    "v_add_u32 v3, s69, v3\n\t"
    "v_add_u32 v4, s69, v4\n\t"
    "s_sub_u32 s68, 0, s68\n\t"
    "s_sub_u32 s69, 0, s69\n\t"
    "v_mfma_f32_16x16x32_bf16 v[176:179], v[24:27], v[200:203], v[176:179]\n\t"
    "ds_read_b64_tr_b16 v[16:17], v1 offset:0\n\t"
    "ds_read_b64_tr_b16 v[18:19], v1 offset:2304\n\t"
    "v_mfma_f32_16x16x32_bf16 v[184:187], v[24:27], v[204:207], v[184:187]\n\t"
    "ds_read_b64_tr_b16 v[20:21], v2 offset:32\n\t"
    "ds_read_b64_tr_b16 v[22:23], v2 offset:2336\n\t"
    "v_mfma_f32_16x16x32_bf16 v[188:191], v[28:31], v[204:207], v[188:191]\n\t"
    "v_mfma_f32_16x16x32_bf16 v[180:183], v[28:31], v[200:203], v[180:183]\n\t"
    "s_waitcnt lgkmcnt(2)\n\t"
    "v_mfma_f32_16x16x32_bf16 v[48:51], v[16:19], v[208:211], v[48:51]\n\t"
    "ds_read_b64_tr_b16 v[24:25], v1 offset:64\n\t"
    "ds_read_b64_tr_b16 v[26:27], v1 offset:2368\n\t"
    "v_mfma_f32_16x16x32_bf16 v[56:59], v[16:19], v[212:215], v[56:59]\n\t"
    "ds_read_b64_tr_b16 v[28:29], v2 offset:96\n\t"
    "ds_read_b64_tr_b16 v[30:31], v2 offset:2400\n\t"
    "s_waitcnt lgkmcnt(4)\n\t"
    "v_mfma_f32_16x16x32_bf16 v[60:63], v[20:23], v[212:215], v[60:63]\n\t"
    "ds_read_b64_tr_b16 v[16:17], v1 offset:128\n\t"
    "v_mfma_f32_16x16x32_bf16 v[52:55], v[20:23], v[208:211], v[52:55]\n\t"
    "ds_read_b64_tr_b16 v[18:19], v1 offset:2432\n\t"
    "s_waitcnt lgkmcnt(4)\n\t"
    "v_mfma_f32_16x16x32_bf16 v[64:67], v[24:27], v[208:211], v[64:67]\n\t"
    "ds_read_b64_tr_b16 v[20:21], v2 offset:160\n\t"
    "v_mfma_f32_16x16x32_bf16 v[72:75], v[24:27], v[212:215], v[72:75]\n\t"
    "ds_read_b64_tr_b16 v[22:23], v2 offset:2464\n\t"
    "s_waitcnt lgkmcnt(4)\n\t"
    "v_mfma_f32_16x16x32_bf16 v[76:79], v[28:31], v[212:215], v[76:79]\n\t"
    "ds_read_b64_tr_b16 v[24:25], v1 offset:192\n\t"
    "v_mfma_f32_16x16x32_bf16 v[68:71], v[28:31], v[208:211], v[68:71]\n\t"
    "ds_read_b64_tr_b16 v[26:27], v1 offset:2496\n\t"
    "s_waitcnt lgkmcnt(4)\n\t"
    "v_mfma_f32_16x16x32_bf16 v[80:83], v[16:19], v[208:211], v[80:83]\n\t"
    "ds_read_b64_tr_b16 v[28:29], v2 offset:224\n\t"
    "v_mfma_f32_16x16x32_bf16 v[88:91], v[16:19], v[212:215], v[88:91]\n\t"
    "ds_read_b64_tr_b16 v[30:31], v2 offset:2528\n\t"
    "s_waitcnt lgkmcnt(4)\n\t"
    "v_mfma_f32_16x16x32_bf16 v[92:95], v[20:23], v[212:215], v[92:95]\n\t"
    "ds_read_b64_tr_b16 v[16:17], v1 offset:256\n\t"
    "v_mfma_f32_16x16x32_bf16 v[84:87], v[20:23], v[208:211], v[84:87]\n\t"
    "ds_read_b64_tr_b16 v[18:19], v1 offset:2560\n\t"
    "s_waitcnt lgkmcnt(4)\n\t"
    "v_mfma_f32_16x16x32_bf16 v[96:99], v[24:27], v[208:211], v[96:99]\n\t"
    "ds_read_b64_tr_b16 v[20:21], v2 offset:288\n\t"
    "v_mfma_f32_16x16x32_bf16 v[104:107], v[24:27], v[212:215], v[104:107]\n\t"
    "ds_read_b64_tr_b16 v[22:23], v2 offset:2592\n\t"
    "s_waitcnt lgkmcnt(4)\n\t"
    "v_mfma_f32_16x16x32_bf16 v[108:111], v[28:31], v[212:215], v[108:111]\n\t"
    "ds_read_b64_tr_b16 v[24:25], v1 offset:320\n\t"
    "v_mfma_f32_16x16x32_bf16 v[100:103], v[28:31], v[208:211], v[100:103]\n\t"
    "ds_read_b64_tr_b16 v[26:27], v1 offset:2624\n\t"
    "s_waitcnt lgkmcnt(4)\n\t"
    "v_mfma_f32_16x16x32_bf16 v[112:115], v[16:19], v[208:211], v[112:115]\n\t"
    "ds_read_b64_tr_b16 v[28:29], v2 offset:352\n\t"
    "v_mfma_f32_16x16x32_bf16 v[120:123], v[16:19], v[212:215], v[120:123]\n\t"
    "ds_read_b64_tr_b16 v[30:31], v2 offset:2656\n\t"
    "s_waitcnt lgkmcnt(4)\n\t"
    "v_mfma_f32_16x16x32_bf16 v[124:127], v[20:23], v[212:215], v[124:127]\n\t"
    "ds_read_b64_tr_b16 v[16:17], v1 offset:384\n\t"
    "v_mfma_f32_16x16x32_bf16 v[116:119], v[20:23], v[208:211], v[116:119]\n\t"
    "ds_read_b64_tr_b16 v[18:19], v1 offset:2688\n\t"
    "s_waitcnt lgkmcnt(4)\n\t"
    "v_mfma_f32_16x16x32_bf16 v[128:131], v[24:27], v[208:211], v[128:131]\n\t"
    "ds_read_b64_tr_b16 v[20:21], v2 offset:416\n\t"
    "v_mfma_f32_16x16x32_bf16 v[136:139], v[24:27], v[212:215], v[136:139]\n\t"
    "ds_read_b64_tr_b16 v[22:23], v2 offset:2720\n\t"
    "s_waitcnt lgkmcnt(4)\n\t"
    "v_mfma_f32_16x16x32_bf16 v[140:143], v[28:31], v[212:215], v[140:143]\n\t"
    "ds_read_b64_tr_b16 v[24:25], v1 offset:448\n\t"
    "v_mfma_f32_16x16x32_bf16 v[132:135], v[28:31], v[208:211], v[132:135]\n\t"
    "ds_read_b64_tr_b16 v[26:27], v1 offset:2752\n\t"
    "s_waitcnt lgkmcnt(4)\n\t"
    "v_mfma_f32_16x16x32_bf16 v[144:147], v[16:19], v[208:211], v[144:147]\n\t"
    "ds_read_b64_tr_b16 v[28:29], v2 offset:480\n\t"
    "v_mfma_f32_16x16x32_bf16 v[152:155], v[16:19], v[212:215], v[152:155]\n\t"
    "ds_read_b64_tr_b16 v[30:31], v2 offset:2784\n\t"
    "s_waitcnt lgkmcnt(4)\n\t"
    "v_mfma_f32_16x16x32_bf16 v[156:159], v[20:23], v[212:215], v[156:159]\n\t"
    "ds_read_b64_tr_b16 v[16:17], v1 offset:512\n\t"
    "v_mfma_f32_16x16x32_bf16 v[148:151], v[20:23], v[208:211], v[148:151]\n\t"
    "ds_read_b64_tr_b16 v[18:19], v1 offset:2816\n\t"
    "s_waitcnt lgkmcnt(4)\n\t"
    "v_mfma_f32_16x16x32_bf16 v[160:163], v[24:27], v[208:211], v[160:163]\n\t"
    "ds_read_b64_tr_b16 v[20:21], v2 offset:544\n\t"
    "v_mfma_f32_16x16x32_bf16 v[168:171], v[24:27], v[212:215], v[168:171]\n\t"
    "ds_read_b64_tr_b16 v[22:23], v2 offset:2848\n\t"
    "s_waitcnt lgkmcnt(4)\n\t"
    "v_mfma_f32_16x16x32_bf16 v[172:175], v[28:31], v[212:215], v[172:175]\n\t"
    "v_mfma_f32_16x16x32_bf16 v[164:167], v[28:31], v[208:211], v[164:167]\n\t"
    "s_waitcnt lgkmcnt(0)\n\t"
    "s_barrier\n\t"
    "v_add_u32 v1, s68, v1\n\t"
    "v_add_u32 v2, s68, v2\n\t"
    "v_add_u32 v5, s68, v5\n\t"
    "v_add_u32 v6, s68, v6\n\t"
    "v_mfma_f32_16x16x32_bf16 v[176:179], v[16:19], v[208:211], v[176:179]\n\t"
    "ds_read_b64_tr_b16 v[24:25], v3 offset:0\n\t"
    "ds_read_b64_tr_b16 v[26:27], v3 offset:2304\n\t"
    "v_mfma_f32_16x16x32_bf16 v[184:187], v[16:19], v[212:215], v[184:187]\n\t"
    "ds_read_b64_tr_b16 v[28:29], v4 offset:32\n\t"
    "ds_read_b64_tr_b16 v[30:31], v4 offset:2336\n\t"
    "v_mfma_f32_16x16x32_bf16 v[188:191], v[20:23], v[212:215], v[188:191]\n\t"
    "v_mfma_f32_16x16x32_bf16 v[180:183], v[20:23], v[208:211], v[180:183]\n\t"
    "s_waitcnt lgkmcnt(2)\n\t"
    "v_mfma_f32_16x16x32_bf16 v[48:51], v[24:27], v[216:219], v[48:51]\n\t"
    "ds_read_b128 v[192:195], v5 offset:18432\n\t"
    "ds_read_b64_tr_b16 v[16:17], v3 offset:64\n\t"
    "ds_read_b64_tr_b16 v[18:19], v3 offset:2368\n\t"
    "v_mfma_f32_16x16x32_bf16 v[56:59], v[24:27], v[220:223], v[56:59]\n\t"
    "ds_read_b128 v[196:199], v5 offset:18944\n\t"
    "ds_read_b64_tr_b16 v[20:21], v4 offset:96\n\t"
    "ds_read_b64_tr_b16 v[22:23], v4 offset:2400\n\t"
    "s_waitcnt lgkmcnt(6)\n\t"
    "v_mfma_f32_16x16x32_bf16 v[60:63], v[28:31], v[220:223], v[60:63]\n\t"
    "ds_read_b128 v[200:203], v6 offset:18432\n\t"
    "ds_read_b64_tr_b16 v[24:25], v3 offset:128\n\t"
    "v_mfma_f32_16x16x32_bf16 v[52:55], v[28:31], v[216:219], v[52:55]\n\t"
    "ds_read_b64_tr_b16 v[26:27], v3 offset:2432\n\t"
    "s_waitcnt lgkmcnt(6)\n\t"
    "v_mfma_f32_16x16x32_bf16 v[64:67], v[16:19], v[216:219], v[64:67]\n\t"
    "ds_read_b128 v[204:207], v6 offset:18944\n\t"
    "ds_read_b64_tr_b16 v[28:29], v4 offset:160\n\t"
    "v_mfma_f32_16x16x32_bf16 v[72:75], v[16:19], v[220:223], v[72:75]\n\t"
    "ds_read_b64_tr_b16 v[30:31], v4 offset:2464\n\t"
    "s_waitcnt lgkmcnt(6)\n\t"
    "v_mfma_f32_16x16x32_bf16 v[76:79], v[20:23], v[220:223], v[76:79]\n\t"
    "ds_read_b64_tr_b16 v[16:17], v3 offset:192\n\t"
    "v_mfma_f32_16x16x32_bf16 v[68:71], v[20:23], v[216:219], v[68:71]\n\t"
    "ds_read_b64_tr_b16 v[18:19], v3 offset:2496\n\t"
    "s_waitcnt lgkmcnt(5)\n\t"
    "v_mfma_f32_16x16x32_bf16 v[80:83], v[24:27], v[216:219], v[80:83]\n\t"
    "ds_read_b64_tr_b16 v[20:21], v4 offset:224\n\t"
    "v_mfma_f32_16x16x32_bf16 v[88:91], v[24:27], v[220:223], v[88:91]\n\t"
    "ds_read_b64_tr_b16 v[22:23], v4 offset:2528\n\t"
    "s_waitcnt lgkmcnt(4)\n\t"
    "v_mfma_f32_16x16x32_bf16 v[92:95], v[28:31], v[220:223], v[92:95]\n\t"
    "ds_read_b64_tr_b16 v[24:25], v3 offset:256\n\t"
    "v_mfma_f32_16x16x32_bf16 v[84:87], v[28:31], v[216:219], v[84:87]\n\t"
    "ds_read_b64_tr_b16 v[26:27], v3 offset:2560\n\t"
    "s_waitcnt lgkmcnt(4)\n\t"
    "v_mfma_f32_16x16x32_bf16 v[96:99], v[16:19], v[216:219], v[96:99]\n\t"
    "ds_read_b64_tr_b16 v[28:29], v4 offset:288\n\t"
    "v_mfma_f32_16x16x32_bf16 v[104:107], v[16:19], v[220:223], v[104:107]\n\t"
    "ds_read_b64_tr_b16 v[30:31], v4 offset:2592\n\t"
    "s_waitcnt lgkmcnt(4)\n\t"
    "v_mfma_f32_16x16x32_bf16 v[108:111], v[20:23], v[220:223], v[108:111]\n\t"
    "ds_read_b64_tr_b16 v[16:17], v3 offset:320\n\t"
    "v_mfma_f32_16x16x32_bf16 v[100:103], v[20:23], v[216:219], v[100:103]\n\t"
    "ds_read_b64_tr_b16 v[18:19], v3 offset:2624\n\t"
    "s_waitcnt lgkmcnt(4)\n\t"
    "v_mfma_f32_16x16x32_bf16 v[112:115], v[24:27], v[216:219], v[112:115]\n\t"
    "ds_read_b64_tr_b16 v[20:21], v4 offset:352\n\t"
    "v_mfma_f32_16x16x32_bf16 v[120:123], v[24:27], v[220:223], v[120:123]\n\t"
    "ds_read_b64_tr_b16 v[22:23], v4 offset:2656\n\t"
    "s_waitcnt lgkmcnt(4)\n\t"
    "v_mfma_f32_16x16x32_bf16 v[124:127], v[28:31], v[220:223], v[124:127]\n\t"
    "ds_read_b64_tr_b16 v[24:25], v3 offset:384\n\t"
    "v_mfma_f32_16x16x32_bf16 v[116:119], v[28:31], v[216:219], v[116:119]\n\t"
    "ds_read_b64_tr_b16 v[26:27], v3 offset:2688\n\t"
    "s_waitcnt lgkmcnt(4)\n\t"
    "v_mfma_f32_16x16x32_bf16 v[128:131], v[16:19], v[216:219], v[128:131]\n\t"
    "ds_read_b64_tr_b16 v[28:29], v4 offset:416\n\t"
    "v_mfma_f32_16x16x32_bf16 v[136:139], v[16:19], v[220:223], v[136:139]\n\t"
    "ds_read_b64_tr_b16 v[30:31], v4 offset:2720\n\t"
    "s_waitcnt lgkmcnt(4)\n\t"
    "v_mfma_f32_16x16x32_bf16 v[140:143], v[20:23], v[220:223], v[140:143]\n\t"
    "ds_read_b64_tr_b16 v[16:17], v3 offset:448\n\t"
    "v_mfma_f32_16x16x32_bf16 v[132:135], v[20:23], v[216:219], v[132:135]\n\t"
    "ds_read_b64_tr_b16 v[18:19], v3 offset:2752\n\t"
    "s_waitcnt lgkmcnt(4)\n\t"
    "v_mfma_f32_16x16x32_bf16 v[144:147], v[24:27], v[216:219], v[144:147]\n\t"
    "ds_read_b64_tr_b16 v[20:21], v4 offset:480\n\t"
    "v_mfma_f32_16x16x32_bf16 v[152:155], v[24:27], v[220:223], v[152:155]\n\t"
    "ds_read_b64_tr_b16 v[22:23], v4 offset:2784\n\t"
    "s_waitcnt lgkmcnt(4)\n\t"
    "v_mfma_f32_16x16x32_bf16 v[156:159], v[28:31], v[220:223], v[156:159]\n\t"
    "ds_read_b64_tr_b16 v[24:25], v3 offset:512\n\t"
    "v_mfma_f32_16x16x32_bf16 v[148:151], v[28:31], v[216:219], v[148:151]\n\t"
    "ds_read_b64_tr_b16 v[26:27], v3 offset:2816\n\t"
    "s_waitcnt lgkmcnt(4)\n\t"
    "v_mfma_f32_16x16x32_bf16 v[160:163], v[16:19], v[216:219], v[160:163]\n\t"
    "ds_read_b64_tr_b16 v[28:29], v4 offset:544\n\t"
    "v_mfma_f32_16x16x32_bf16 v[168:171], v[16:19], v[220:223], v[168:171]\n\t"
    "ds_read_b64_tr_b16 v[30:31], v4 offset:2848\n\t"
    "s_waitcnt lgkmcnt(4)\n\t"
    "v_mfma_f32_16x16x32_bf16 v[172:175], v[20:23], v[220:223], v[172:175]\n\t"
    "v_mfma_f32_16x16x32_bf16 v[164:167], v[20:23], v[216:219], v[164:167]\n\t"
    "s_waitcnt lgkmcnt(0)\n\t"
    "s_barrier\n\t"
    "v_add_u32 v3, s69, v3\n\t"
    "v_add_u32 v4, s69, v4\n\t"
    "s_sub_u32 s68, 0, s68\n\t"
    "s_sub_u32 s69, 0, s69\n\t"
    "v_mfma_f32_16x16x32_bf16 v[176:179], v[24:27], v[216:219], v[176:179]\n\t"
    "ds_read_b64_tr_b16 v[16:17], v1 offset:0\n\t"
    "ds_read_b64_tr_b16 v[18:19], v1 offset:2304\n\t"
    "v_mfma_f32_16x16x32_bf16 v[184:187], v[24:27], v[220:223], v[184:187]\n\t"
    "ds_read_b64_tr_b16 v[20:21], v2 offset:32\n\t"
    "ds_read_b64_tr_b16 v[22:23], v2 offset:2336\n\t"
    "v_mfma_f32_16x16x32_bf16 v[188:191], v[28:31], v[220:223], v[188:191]\n\t"
    "v_mfma_f32_16x16x32_bf16 v[180:183], v[28:31], v[216:219], v[180:183]\n\t"
    "s_waitcnt lgkmcnt(2)\n\t"
    "v_mfma_f32_16x16x32_bf16 v[48:51], v[16:19], v[224:227], v[48:51]\n\t"
    "ds_read_b64_tr_b16 v[24:25], v1 offset:64\n\t"
    "ds_read_b64_tr_b16 v[26:27], v1 offset:2368\n\t"
    "v_lshlrev_b32 v11, 16, v192\n\t"
    "v_and_b32 v15, 0xffff0000, v192\n\t"
    "v_mfma_f32_16x16x32_bf16 v[56:59], v[16:19], v[228:231], v[56:59]\n\t"
    "ds_read_b64_tr_b16 v[28:29], v2 offset:96\n\t"
    "ds_read_b64_tr_b16 v[30:31], v2 offset:2400\n\t"
    "s_waitcnt lgkmcnt(4)\n\t"
    "v_mfma_f32_16x16x32_bf16 v[60:63], v[20:23], v[228:231], v[60:63]\n\t"
    "ds_read_b64_tr_b16 v[16:17], v1 offset:128\n\t"
    "v_mul_f32 v11, v7, v11\n\t"
    "v_mul_f32 v15, v7, v15\n\t"
    "v_mfma_f32_16x16x32_bf16 v[52:55], v[20:23], v[224:227], v[52:55]\n\t"
    "ds_read_b64_tr_b16 v[18:19], v1 offset:2432\n\t"
    "s_waitcnt lgkmcnt(4)\n\t"
    "v_mfma_f32_16x16x32_bf16 v[64:67], v[24:27], v[224:227], v[64:67]\n\t"
    "ds_read_b64_tr_b16 v[20:21], v2 offset:160\n\t"
    "v_cvt_pk_bf16_f32 v192, v11, v15\n\t"
    "v_mfma_f32_16x16x32_bf16 v[72:75], v[24:27], v[228:231], v[72:75]\n\t"
    "ds_read_b64_tr_b16 v[22:23], v2 offset:2464\n\t"
    "s_waitcnt lgkmcnt(4)\n\t"
    "v_mfma_f32_16x16x32_bf16 v[76:79], v[28:31], v[228:231], v[76:79]\n\t"
    "ds_read_b64_tr_b16 v[24:25], v1 offset:192\n\t"
    "v_lshlrev_b32 v11, 16, v193\n\t"
    "v_and_b32 v15, 0xffff0000, v193\n\t"
    "v_mfma_f32_16x16x32_bf16 v[68:71], v[28:31], v[224:227], v[68:71]\n\t"
    "ds_read_b64_tr_b16 v[26:27], v1 offset:2496\n\t"
    "s_waitcnt lgkmcnt(4)\n\t"
    "v_mfma_f32_16x16x32_bf16 v[80:83], v[16:19], v[224:227], v[80:83]\n\t"
    "ds_read_b64_tr_b16 v[28:29], v2 offset:224\n\t"
    "v_mul_f32 v11, v7, v11\n\t"
    "v_mul_f32 v15, v7, v15\n\t"
    "v_mfma_f32_16x16x32_bf16 v[88:91], v[16:19], v[228:231], v[88:91]\n\t"
    "ds_read_b64_tr_b16 v[30:31], v2 offset:2528\n\t"
    "s_waitcnt lgkmcnt(4)\n\t"
    "v_mfma_f32_16x16x32_bf16 v[92:95], v[20:23], v[228:231], v[92:95]\n\t"
    "ds_read_b64_tr_b16 v[16:17], v1 offset:256\n\t"
    "v_cvt_pk_bf16_f32 v193, v11, v15\n\t"
    "v_mfma_f32_16x16x32_bf16 v[84:87], v[20:23], v[224:227], v[84:87]\n\t"
    "ds_read_b64_tr_b16 v[18:19], v1 offset:2560\n\t"
    "s_waitcnt lgkmcnt(4)\n\t"
    "v_mfma_f32_16x16x32_bf16 v[96:99], v[24:27], v[224:227], v[96:99]\n\t"
    "ds_read_b64_tr_b16 v[20:21], v2 offset:288\n\t"
    "v_lshlrev_b32 v11, 16, v194\n\t"
    "v_and_b32 v15, 0xffff0000, v194\n\t"
    "v_mfma_f32_16x16x32_bf16 v[104:107], v[24:27], v[228:231], v[104:107]\n\t"
    "ds_read_b64_tr_b16 v[22:23], v2 offset:2592\n\t"
    "s_waitcnt lgkmcnt(4)\n\t"
    "v_mfma_f32_16x16x32_bf16 v[108:111], v[28:31], v[228:231], v[108:111]\n\t"
    "ds_read_b64_tr_b16 v[24:25], v1 offset:320\n\t"
    "v_mul_f32 v11, v7, v11\n\t"
    "v_mul_f32 v15, v7, v15\n\t"
    "v_mfma_f32_16x16x32_bf16 v[100:103], v[28:31], v[224:227], v[100:103]\n\t"
    "ds_read_b64_tr_b16 v[26:27], v1 offset:2624\n\t"
    "s_waitcnt lgkmcnt(4)\n\t"
    "v_mfma_f32_16x16x32_bf16 v[112:115], v[16:19], v[224:227], v[112:115]\n\t"
    "ds_read_b64_tr_b16 v[28:29], v2 offset:352\n\t"
    "v_cvt_pk_bf16_f32 v194, v11, v15\n\t"
    "v_mfma_f32_16x16x32_bf16 v[120:123], v[16:19], v[228:231], v[120:123]\n\t"
    "ds_read_b64_tr_b16 v[30:31], v2 offset:2656\n\t"
    "s_waitcnt lgkmcnt(4)\n\t"
    "v_mfma_f32_16x16x32_bf16 v[124:127], v[20:23], v[228:231], v[124:127]\n\t"
    "ds_read_b64_tr_b16 v[16:17], v1 offset:384\n\t"
    "v_lshlrev_b32 v11, 16, v195\n\t"
    "v_and_b32 v15, 0xffff0000, v195\n\t"
    "v_mfma_f32_16x16x32_bf16 v[116:119], v[20:23], v[224:227], v[116:119]\n\t"
    "ds_read_b64_tr_b16 v[18:19], v1 offset:2688\n\t"
    "s_waitcnt lgkmcnt(4)\n\t"
    "v_mfma_f32_16x16x32_bf16 v[128:131], v[24:27], v[224:227], v[128:131]\n\t"
    "ds_read_b64_tr_b16 v[20:21], v2 offset:416\n\t"
    "v_mul_f32 v11, v7, v11\n\t"
    "v_mul_f32 v15, v7, v15\n\t"
    "v_mfma_f32_16x16x32_bf16 v[136:139], v[24:27], v[228:231], v[136:139]\n\t"
    "ds_read_b64_tr_b16 v[22:23], v2 offset:2720\n\t"
    "s_waitcnt lgkmcnt(4)\n\t"
    "v_mfma_f32_16x16x32_bf16 v[140:143], v[28:31], v[228:231], v[140:143]\n\t"
    "ds_read_b64_tr_b16 v[24:25], v1 offset:448\n\t"
    "v_cvt_pk_bf16_f32 v195, v11, v15\n\t"
    "v_mfma_f32_16x16x32_bf16 v[132:135], v[28:31], v[224:227], v[132:135]\n\t"
    "ds_read_b64_tr_b16 v[26:27], v1 offset:2752\n\t"
    "s_waitcnt lgkmcnt(4)\n\t"
    "v_mfma_f32_16x16x32_bf16 v[144:147], v[16:19], v[224:227], v[144:147]\n\t"
    "ds_read_b64_tr_b16 v[28:29], v2 offset:480\n\t"
    "v_lshlrev_b32 v11, 16, v196\n\t"
    "v_and_b32 v15, 0xffff0000, v196\n\t"
    "v_mfma_f32_16x16x32_bf16 v[152:155], v[16:19], v[228:231], v[152:155]\n\t"
    "ds_read_b64_tr_b16 v[30:31], v2 offset:2784\n\t"
    "s_waitcnt lgkmcnt(4)\n\t"
    "v_mfma_f32_16x16x32_bf16 v[156:159], v[20:23], v[228:231], v[156:159]\n\t"
    "ds_read_b64_tr_b16 v[16:17], v1 offset:512\n\t"
    "v_mul_f32 v11, v8, v11\n\t"
    "v_mul_f32 v15, v8, v15\n\t"
    "v_mfma_f32_16x16x32_bf16 v[148:151], v[20:23], v[224:227], v[148:151]\n\t"
    "ds_read_b64_tr_b16 v[18:19], v1 offset:2816\n\t"
    "s_waitcnt lgkmcnt(4)\n\t"
    "v_mfma_f32_16x16x32_bf16 v[160:163], v[24:27], v[224:227], v[160:163]\n\t"
    "ds_read_b64_tr_b16 v[20:21], v2 offset:544\n\t"
    "v_cvt_pk_bf16_f32 v196, v11, v15\n\t"
    "v_mfma_f32_16x16x32_bf16 v[168:171], v[24:27], v[228:231], v[168:171]\n\t"
    "ds_read_b64_tr_b16 v[22:23], v2 offset:2848\n\t"
    "s_waitcnt lgkmcnt(4)\n\t"
    "v_mfma_f32_16x16x32_bf16 v[172:175], v[28:31], v[228:231], v[172:175]\n\t"
    "v_lshlrev_b32 v11, 16, v197\n\t"
    "v_and_b32 v15, 0xffff0000, v197\n\t"
    "v_mfma_f32_16x16x32_bf16 v[164:167], v[28:31], v[224:227], v[164:167]\n\t"
    "s_waitcnt lgkmcnt(0)\n\t"
    "s_barrier\n\t"
    "v_add_u32 v1, s68, v1\n\t"
    "v_add_u32 v2, s68, v2\n\t"
    "v_add_u32 v5, s68, v5\n\t"
    "v_add_u32 v6, s68, v6\n\t"
    "v_mfma_f32_16x16x32_bf16 v[176:179], v[16:19], v[224:227], v[176:179]\n\t"
    "ds_read_b64_tr_b16 v[24:25], v3 offset:0\n\t"
    "ds_read_b64_tr_b16 v[26:27], v3 offset:2304\n\t"
    "v_mul_f32 v11, v8, v11\n\t"
    "v_mul_f32 v15, v8, v15\n\t"
    "v_mfma_f32_16x16x32_bf16 v[184:187], v[16:19], v[228:231], v[184:187]\n\t"
    "ds_read_b64_tr_b16 v[28:29], v4 offset:32\n\t"
    "ds_read_b64_tr_b16 v[30:31], v4 offset:2336\n\t"
    "v_mfma_f32_16x16x32_bf16 v[188:191], v[20:23], v[228:231], v[188:191]\n\t"
    "v_cvt_pk_bf16_f32 v197, v11, v15\n\t"
    "v_mfma_f32_16x16x32_bf16 v[180:183], v[20:23], v[224:227], v[180:183]\n\t"
    "s_waitcnt lgkmcnt(2)\n\t"
    "v_mfma_f32_16x16x32_bf16 v[48:51], v[24:27], v[232:235], v[48:51]\n\t"
    "ds_read_b128 v[208:211], v5 offset:18432\n\t"
    "ds_read_b64_tr_b16 v[16:17], v3 offset:64\n\t"
    "ds_read_b64_tr_b16 v[18:19], v3 offset:2368\n\t"
    "v_lshlrev_b32 v11, 16, v198\n\t"
    "v_and_b32 v15, 0xffff0000, v198\n\t"
    "v_mfma_f32_16x16x32_bf16 v[56:59], v[24:27], v[236:239], v[56:59]\n\t"
    "ds_read_b128 v[212:215], v5 offset:18944\n\t"
    "ds_read_b64_tr_b16 v[20:21], v4 offset:96\n\t"
    "ds_read_b64_tr_b16 v[22:23], v4 offset:2400\n\t"
    "s_waitcnt lgkmcnt(6)\n\t"
    "v_mfma_f32_16x16x32_bf16 v[60:63], v[28:31], v[236:239], v[60:63]\n\t"
    "ds_read_b128 v[216:219], v6 offset:18432\n\t"
    "ds_read_b64_tr_b16 v[24:25], v3 offset:128\n\t"
    "v_mul_f32 v11, v8, v11\n\t"
    "v_mul_f32 v15, v8, v15\n\t"
    "v_mfma_f32_16x16x32_bf16 v[52:55], v[28:31], v[232:235], v[52:55]\n\t"
    "ds_read_b64_tr_b16 v[26:27], v3 offset:2432\n\t"
    "s_waitcnt lgkmcnt(6)\n\t"
    "v_mfma_f32_16x16x32_bf16 v[64:67], v[16:19], v[232:235], v[64:67]\n\t"
    "ds_read_b128 v[220:223], v6 offset:18944\n\t"
    "ds_read_b64_tr_b16 v[28:29], v4 offset:160\n\t"
    "v_cvt_pk_bf16_f32 v198, v11, v15\n\t"
    "v_mfma_f32_16x16x32_bf16 v[72:75], v[16:19], v[236:239], v[72:75]\n\t"
    "ds_read_b64_tr_b16 v[30:31], v4 offset:2464\n\t"
    "s_waitcnt lgkmcnt(6)\n\t"
    "v_mfma_f32_16x16x32_bf16 v[76:79], v[20:23], v[236:239], v[76:79]\n\t"
    "ds_read_b64_tr_b16 v[16:17], v3 offset:192\n\t"
    "v_lshlrev_b32 v11, 16, v199\n\t"
    "v_and_b32 v15, 0xffff0000, v199\n\t"
    "v_mfma_f32_16x16x32_bf16 v[68:71], v[20:23], v[232:235], v[68:71]\n\t"
    "ds_read_b64_tr_b16 v[18:19], v3 offset:2496\n\t"
    "s_waitcnt lgkmcnt(5)\n\t"
    "v_mfma_f32_16x16x32_bf16 v[80:83], v[24:27], v[232:235], v[80:83]\n\t"
    "ds_read_b64_tr_b16 v[20:21], v4 offset:224\n\t"
    "v_mul_f32 v11, v8, v11\n\t"
    "v_mul_f32 v15, v8, v15\n\t"
    "v_mfma_f32_16x16x32_bf16 v[88:91], v[24:27], v[236:239], v[88:91]\n\t"
    "ds_read_b64_tr_b16 v[22:23], v4 offset:2528\n\t"
    "s_waitcnt lgkmcnt(4)\n\t"
    "v_mfma_f32_16x16x32_bf16 v[92:95], v[28:31], v[236:239], v[92:95]\n\t"
    "ds_read_b64_tr_b16 v[24:25], v3 offset:256\n\t"
    "v_cvt_pk_bf16_f32 v199, v11, v15\n\t"
    "v_mfma_f32_16x16x32_bf16 v[84:87], v[28:31], v[232:235], v[84:87]\n\t"
    "ds_read_b64_tr_b16 v[26:27], v3 offset:2560\n\t"
    "s_waitcnt lgkmcnt(4)\n\t"
    "v_mfma_f32_16x16x32_bf16 v[96:99], v[16:19], v[232:235], v[96:99]\n\t"
    "ds_read_b64_tr_b16 v[28:29], v4 offset:288\n\t"
    "v_lshlrev_b32 v11, 16, v200\n\t"
    "v_and_b32 v15, 0xffff0000, v200\n\t"
    "v_mfma_f32_16x16x32_bf16 v[104:107], v[16:19], v[236:239], v[104:107]\n\t"
    "ds_read_b64_tr_b16 v[30:31], v4 offset:2592\n\t"
    "s_waitcnt lgkmcnt(4)\n\t"
    "v_mfma_f32_16x16x32_bf16 v[108:111], v[20:23], v[236:239], v[108:111]\n\t"
    "ds_read_b64_tr_b16 v[16:17], v3 offset:320\n\t"
    "v_mul_f32 v11, v7, v11\n\t"
    "v_mul_f32 v15, v7, v15\n\t"
    "v_mfma_f32_16x16x32_bf16 v[100:103], v[20:23], v[232:235], v[100:103]\n\t"
    "ds_read_b64_tr_b16 v[18:19], v3 offset:2624\n\t"
    "s_waitcnt lgkmcnt(4)\n\t"
    "v_mfma_f32_16x16x32_bf16 v[112:115], v[24:27], v[232:235], v[112:115]\n\t"
    "ds_read_b64_tr_b16 v[20:21], v4 offset:352\n\t"
    "v_cvt_pk_bf16_f32 v200, v11, v15\n\t"
    "v_mfma_f32_16x16x32_bf16 v[120:123], v[24:27], v[236:239], v[120:123]\n\t"
    "ds_read_b64_tr_b16 v[22:23], v4 offset:2656\n\t"
    "s_waitcnt lgkmcnt(4)\n\t"
    "v_mfma_f32_16x16x32_bf16 v[124:127], v[28:31], v[236:239], v[124:127]\n\t"
    "ds_read_b64_tr_b16 v[24:25], v3 offset:384\n\t"
    "v_lshlrev_b32 v11, 16, v201\n\t"
    "v_and_b32 v15, 0xffff0000, v201\n\t"
    "v_mfma_f32_16x16x32_bf16 v[116:119], v[28:31], v[232:235], v[116:119]\n\t"
    "ds_read_b64_tr_b16 v[26:27], v3 offset:2688\n\t"
    "s_waitcnt lgkmcnt(4)\n\t"
    "v_mfma_f32_16x16x32_bf16 v[128:131], v[16:19], v[232:235], v[128:131]\n\t"
    "ds_read_b64_tr_b16 v[28:29], v4 offset:416\n\t"
    "v_mul_f32 v11, v7, v11\n\t"
    "v_mul_f32 v15, v7, v15\n\t"
    "v_mfma_f32_16x16x32_bf16 v[136:139], v[16:19], v[236:239], v[136:139]\n\t"
    "ds_read_b64_tr_b16 v[30:31], v4 offset:2720\n\t"
    "s_waitcnt lgkmcnt(4)\n\t"
    "v_mfma_f32_16x16x32_bf16 v[140:143], v[20:23], v[236:239], v[140:143]\n\t"
    "ds_read_b64_tr_b16 v[16:17], v3 offset:448\n\t"
    "v_cvt_pk_bf16_f32 v201, v11, v15\n\t"
    "v_mfma_f32_16x16x32_bf16 v[132:135], v[20:23], v[232:235], v[132:135]\n\t"
    "ds_read_b64_tr_b16 v[18:19], v3 offset:2752\n\t"
    "s_waitcnt lgkmcnt(4)\n\t"
    "v_mfma_f32_16x16x32_bf16 v[144:147], v[24:27], v[232:235], v[144:147]\n\t"
    "ds_read_b64_tr_b16 v[20:21], v4 offset:480\n\t"
    "v_lshlrev_b32 v11, 16, v202\n\t"
    "v_and_b32 v15, 0xffff0000, v202\n\t"
    "v_mfma_f32_16x16x32_bf16 v[152:155], v[24:27], v[236:239], v[152:155]\n\t"
    "ds_read_b64_tr_b16 v[22:23], v4 offset:2784\n\t"
    "s_waitcnt lgkmcnt(4)\n\t"
    "v_mfma_f32_16x16x32_bf16 v[156:159], v[28:31], v[236:239], v[156:159]\n\t"
    "ds_read_b64_tr_b16 v[24:25], v3 offset:512\n\t"
    "v_mul_f32 v11, v7, v11\n\t"
    "v_mul_f32 v15, v7, v15\n\t"
    "v_mfma_f32_16x16x32_bf16 v[148:151], v[28:31], v[232:235], v[148:151]\n\t"
    "ds_read_b64_tr_b16 v[26:27], v3 offset:2816\n\t"
    "s_waitcnt lgkmcnt(4)\n\t"
    "v_mfma_f32_16x16x32_bf16 v[160:163], v[16:19], v[232:235], v[160:163]\n\t"
    "ds_read_b64_tr_b16 v[28:29], v4 offset:544\n\t"
    "v_cvt_pk_bf16_f32 v202, v11, v15\n\t"
    "v_mfma_f32_16x16x32_bf16 v[168:171], v[16:19], v[236:239], v[168:171]\n\t"
    "ds_read_b64_tr_b16 v[30:31], v4 offset:2848\n\t"
    "s_waitcnt lgkmcnt(4)\n\t"
    "v_mfma_f32_16x16x32_bf16 v[172:175], v[20:23], v[236:239], v[172:175]\n\t"
    "v_lshlrev_b32 v11, 16, v203\n\t"
    "v_and_b32 v15, 0xffff0000, v203\n\t"
    "v_mfma_f32_16x16x32_bf16 v[164:167], v[20:23], v[232:235], v[164:167]\n\t"
    "s_waitcnt lgkmcnt(0)\n\t"
    "s_barrier\n\t"
    "v_add_u32 v3, s69, v3\n\t"
    "v_add_u32 v4, s69, v4\n\t"
    "s_sub_u32 s68, 0, s68\n\t"
    "s_sub_u32 s69, 0, s69\n\t"
    "v_mfma_f32_16x16x32_bf16 v[176:179], v[24:27], v[232:235], v[176:179]\n\t"
    "ds_read_b64_tr_b16 v[16:17], v1 offset:0\n\t"
    "ds_read_b64_tr_b16 v[18:19], v1 offset:2304\n\t"
    "v_mul_f32 v11, v7, v11\n\t"
    "v_mul_f32 v15, v7, v15\n\t"
    "v_mfma_f32_16x16x32_bf16 v[184:187], v[24:27], v[236:239], v[184:187]\n\t"
    "ds_read_b64_tr_b16 v[20:21], v2 offset:32\n\t"
    "ds_read_b64_tr_b16 v[22:23], v2 offset:2336\n\t"
    "v_mfma_f32_16x16x32_bf16 v[188:191], v[28:31], v[236:239], v[188:191]\n\t"
    "v_cvt_pk_bf16_f32 v203, v11, v15\n\t"
    "v_mfma_f32_16x16x32_bf16 v[180:183], v[28:31], v[232:235], v[180:183]\n\t"
    "s_waitcnt lgkmcnt(2)\n\t"
    "v_mfma_f32_16x16x32_bf16 v[48:51], v[16:19], v[240:243], v[48:51]\n\t"
    "ds_read_b64_tr_b16 v[24:25], v1 offset:64\n\t"
    "ds_read_b64_tr_b16 v[26:27], v1 offset:2368\n\t"
    "v_lshlrev_b32 v11, 16, v204\n\t"
    "v_and_b32 v15, 0xffff0000, v204\n\t"
    "v_mfma_f32_16x16x32_bf16 v[56:59], v[16:19], v[244:247], v[56:59]\n\t"
    "ds_read_b64_tr_b16 v[28:29], v2 offset:96\n\t"
    "ds_read_b64_tr_b16 v[30:31], v2 offset:2400\n\t"
    "s_waitcnt lgkmcnt(4)\n\t"
    "v_mfma_f32_16x16x32_bf16 v[60:63], v[20:23], v[244:247], v[60:63]\n\t"
    "ds_read_b64_tr_b16 v[16:17], v1 offset:128\n\t"
    "v_mul_f32 v11, v8, v11\n\t"
    "v_mul_f32 v15, v8, v15\n\t"
    "v_mfma_f32_16x16x32_bf16 v[52:55], v[20:23], v[240:243], v[52:55]\n\t"
    "ds_read_b64_tr_b16 v[18:19], v1 offset:2432\n\t"
    "s_waitcnt lgkmcnt(4)\n\t"
    "v_mfma_f32_16x16x32_bf16 v[64:67], v[24:27], v[240:243], v[64:67]\n\t"
    "ds_read_b64_tr_b16 v[20:21], v2 offset:160\n\t"
    "v_cvt_pk_bf16_f32 v204, v11, v15\n\t"
    "v_mfma_f32_16x16x32_bf16 v[72:75], v[24:27], v[244:247], v[72:75]\n\t"
    "ds_read_b64_tr_b16 v[22:23], v2 offset:2464\n\t"
    "s_waitcnt lgkmcnt(4)\n\t"
    "v_mfma_f32_16x16x32_bf16 v[76:79], v[28:31], v[244:247], v[76:79]\n\t"
    "ds_read_b64_tr_b16 v[24:25], v1 offset:192\n\t"
    "v_lshlrev_b32 v11, 16, v205\n\t"
    "v_and_b32 v15, 0xffff0000, v205\n\t"
    "v_mfma_f32_16x16x32_bf16 v[68:71], v[28:31], v[240:243], v[68:71]\n\t"
    "ds_read_b64_tr_b16 v[26:27], v1 offset:2496\n\t"
    "s_waitcnt lgkmcnt(4)\n\t"
    "v_mfma_f32_16x16x32_bf16 v[80:83], v[16:19], v[240:243], v[80:83]\n\t"
    "ds_read_b64_tr_b16 v[28:29], v2 offset:224\n\t"
    "v_mul_f32 v11, v8, v11\n\t"
    "v_mul_f32 v15, v8, v15\n\t"
    "v_mfma_f32_16x16x32_bf16 v[88:91], v[16:19], v[244:247], v[88:91]\n\t"
    "ds_read_b64_tr_b16 v[30:31], v2 offset:2528\n\t"
    "s_waitcnt lgkmcnt(4)\n\t"
    "v_mfma_f32_16x16x32_bf16 v[92:95], v[20:23], v[244:247], v[92:95]\n\t"
    "ds_read_b64_tr_b16 v[16:17], v1 offset:256\n\t"
    "v_cvt_pk_bf16_f32 v205, v11, v15\n\t"
    "v_mfma_f32_16x16x32_bf16 v[84:87], v[20:23], v[240:243], v[84:87]\n\t"
    "ds_read_b64_tr_b16 v[18:19], v1 offset:2560\n\t"
    "s_waitcnt lgkmcnt(4)\n\t"
    "v_mfma_f32_16x16x32_bf16 v[96:99], v[24:27], v[240:243], v[96:99]\n\t"
    "ds_read_b64_tr_b16 v[20:21], v2 offset:288\n\t"
    "v_lshlrev_b32 v11, 16, v206\n\t"
    "v_and_b32 v15, 0xffff0000, v206\n\t"
    "v_mfma_f32_16x16x32_bf16 v[104:107], v[24:27], v[244:247], v[104:107]\n\t"
    "ds_read_b64_tr_b16 v[22:23], v2 offset:2592\n\t"
    "s_waitcnt lgkmcnt(4)\n\t"
    "v_mfma_f32_16x16x32_bf16 v[108:111], v[28:31], v[244:247], v[108:111]\n\t"
    "ds_read_b64_tr_b16 v[24:25], v1 offset:320\n\t"
    "v_mul_f32 v11, v8, v11\n\t"
    "v_mul_f32 v15, v8, v15\n\t"
    "v_mfma_f32_16x16x32_bf16 v[100:103], v[28:31], v[240:243], v[100:103]\n\t"
    "ds_read_b64_tr_b16 v[26:27], v1 offset:2624\n\t"
    "s_waitcnt lgkmcnt(4)\n\t"
    "v_mfma_f32_16x16x32_bf16 v[112:115], v[16:19], v[240:243], v[112:115]\n\t"
    "ds_read_b64_tr_b16 v[28:29], v2 offset:352\n\t"
    "v_cvt_pk_bf16_f32 v206, v11, v15\n\t"
    "v_mfma_f32_16x16x32_bf16 v[120:123], v[16:19], v[244:247], v[120:123]\n\t"
    "ds_read_b64_tr_b16 v[30:31], v2 offset:2656\n\t"
    "s_waitcnt lgkmcnt(4)\n\t"
    "v_mfma_f32_16x16x32_bf16 v[124:127], v[20:23], v[244:247], v[124:127]\n\t"
    "ds_read_b64_tr_b16 v[16:17], v1 offset:384\n\t"
    "v_lshlrev_b32 v11, 16, v207\n\t"
    "v_and_b32 v15, 0xffff0000, v207\n\t"
    "v_mfma_f32_16x16x32_bf16 v[116:119], v[20:23], v[240:243], v[116:119]\n\t"
    "ds_read_b64_tr_b16 v[18:19], v1 offset:2688\n\t"
    "s_waitcnt lgkmcnt(4)\n\t"
    "v_mfma_f32_16x16x32_bf16 v[128:131], v[24:27], v[240:243], v[128:131]\n\t"
    "ds_read_b64_tr_b16 v[20:21], v2 offset:416\n\t"
    "v_mul_f32 v11, v8, v11\n\t"
    "v_mul_f32 v15, v8, v15\n\t"
    "v_mfma_f32_16x16x32_bf16 v[136:139], v[24:27], v[244:247], v[136:139]\n\t"
    "ds_read_b64_tr_b16 v[22:23], v2 offset:2720\n\t"
    "s_waitcnt lgkmcnt(4)\n\t"
    "v_mfma_f32_16x16x32_bf16 v[140:143], v[28:31], v[244:247], v[140:143]\n\t"
    "ds_read_b64_tr_b16 v[24:25], v1 offset:448\n\t"
    "v_cvt_pk_bf16_f32 v207, v11, v15\n\t"
    "v_mfma_f32_16x16x32_bf16 v[132:135], v[28:31], v[240:243], v[132:135]\n\t"
    "ds_read_b64_tr_b16 v[26:27], v1 offset:2752\n\t"
    "s_waitcnt lgkmcnt(4)\n\t"
    "v_mfma_f32_16x16x32_bf16 v[144:147], v[16:19], v[240:243], v[144:147]\n\t"
    "ds_read_b64_tr_b16 v[28:29], v2 offset:480\n\t"
    "v_lshlrev_b32 v11, 16, v208\n\t"
    "v_and_b32 v15, 0xffff0000, v208\n\t"
    "v_mfma_f32_16x16x32_bf16 v[152:155], v[16:19], v[244:247], v[152:155]\n\t"
    "ds_read_b64_tr_b16 v[30:31], v2 offset:2784\n\t"
    "s_waitcnt lgkmcnt(4)\n\t"
    "v_mfma_f32_16x16x32_bf16 v[156:159], v[20:23], v[244:247], v[156:159]\n\t"
    "ds_read_b64_tr_b16 v[16:17], v1 offset:512\n\t"
    "v_mul_f32 v11, v7, v11\n\t"
    "v_mul_f32 v15, v7, v15\n\t"
    "v_mfma_f32_16x16x32_bf16 v[148:151], v[20:23], v[240:243], v[148:151]\n\t"
    "ds_read_b64_tr_b16 v[18:19], v1 offset:2816\n\t"
    "s_waitcnt lgkmcnt(4)\n\t"
    "v_mfma_f32_16x16x32_bf16 v[160:163], v[24:27], v[240:243], v[160:163]\n\t"
    "ds_read_b64_tr_b16 v[20:21], v2 offset:544\n\t"
    "v_cvt_pk_bf16_f32 v208, v11, v15\n\t"
    "v_mfma_f32_16x16x32_bf16 v[168:171], v[24:27], v[244:247], v[168:171]\n\t"
    "ds_read_b64_tr_b16 v[22:23], v2 offset:2848\n\t"
    "s_waitcnt lgkmcnt(4)\n\t"
    "v_mfma_f32_16x16x32_bf16 v[172:175], v[28:31], v[244:247], v[172:175]\n\t"
    "v_lshlrev_b32 v11, 16, v209\n\t"
    "v_and_b32 v15, 0xffff0000, v209\n\t"
    "v_mfma_f32_16x16x32_bf16 v[164:167], v[28:31], v[240:243], v[164:167]\n\t"
    "s_waitcnt lgkmcnt(0)\n\t"
    "s_barrier\n\t"
    "v_add_u32 v1, s68, v1\n\t"
    "v_add_u32 v2, s68, v2\n\t"
    "v_add_u32 v5, s68, v5\n\t"
    "v_add_u32 v6, s68, v6\n\t"
    "v_mfma_f32_16x16x32_bf16 v[176:179], v[16:19], v[240:243], v[176:179]\n\t"
    "ds_read_b64_tr_b16 v[24:25], v3 offset:0\n\t"
    "ds_read_b64_tr_b16 v[26:27], v3 offset:2304\n\t"
    "v_mul_f32 v11, v7, v11\n\t"
    "v_mul_f32 v15, v7, v15\n\t"
    "v_mfma_f32_16x16x32_bf16 v[184:187], v[16:19], v[244:247], v[184:187]\n\t"
    "ds_read_b64_tr_b16 v[28:29], v4 offset:32\n\t"
    "ds_read_b64_tr_b16 v[30:31], v4 offset:2336\n\t"
    "v_mfma_f32_16x16x32_bf16 v[188:191], v[20:23], v[244:247], v[188:191]\n\t"
    "v_cvt_pk_bf16_f32 v209, v11, v15\n\t"
    "v_mfma_f32_16x16x32_bf16 v[180:183], v[20:23], v[240:243], v[180:183]\n\t"
    "s_waitcnt lgkmcnt(2)\n\t"
    "v_mfma_f32_16x16x32_bf16 v[48:51], v[24:27], v[248:251], v[48:51]\n\t"
    "ds_read_b128 v[224:227], v5 offset:18432\n\t"
    "ds_read_b64_tr_b16 v[16:17], v3 offset:64\n\t"
    "ds_read_b64_tr_b16 v[18:19], v3 offset:2368\n\t"
    "v_lshlrev_b32 v11, 16, v210\n\t"
    "v_and_b32 v15, 0xffff0000, v210\n\t"
    "v_mfma_f32_16x16x32_bf16 v[56:59], v[24:27], v[252:255], v[56:59]\n\t"
    "ds_read_b128 v[228:231], v5 offset:18944\n\t"
    "ds_read_b64_tr_b16 v[20:21], v4 offset:96\n\t"
    "ds_read_b64_tr_b16 v[22:23], v4 offset:2400\n\t"
    "s_waitcnt lgkmcnt(6)\n\t"
    "v_mfma_f32_16x16x32_bf16 v[60:63], v[28:31], v[252:255], v[60:63]\n\t"
    "ds_read_b128 v[232:235], v6 offset:18432\n\t"
    "ds_read_b64_tr_b16 v[24:25], v3 offset:128\n\t"
    "v_mul_f32 v11, v7, v11\n\t"
    "v_mul_f32 v15, v7, v15\n\t"
    "v_mfma_f32_16x16x32_bf16 v[52:55], v[28:31], v[248:251], v[52:55]\n\t"
    "ds_read_b64_tr_b16 v[26:27], v3 offset:2432\n\t"
    "s_waitcnt lgkmcnt(6)\n\t"
    "v_mfma_f32_16x16x32_bf16 v[64:67], v[16:19], v[248:251], v[64:67]\n\t"
    "ds_read_b128 v[236:239], v6 offset:18944\n\t"
    "ds_read_b64_tr_b16 v[28:29], v4 offset:160\n\t"
    "v_cvt_pk_bf16_f32 v210, v11, v15\n\t"
    "v_mfma_f32_16x16x32_bf16 v[72:75], v[16:19], v[252:255], v[72:75]\n\t"
    "ds_read_b64_tr_b16 v[30:31], v4 offset:2464\n\t"
    "s_waitcnt lgkmcnt(6)\n\t"
    "v_mfma_f32_16x16x32_bf16 v[76:79], v[20:23], v[252:255], v[76:79]\n\t"
    "ds_read_b64_tr_b16 v[16:17], v3 offset:192\n\t"
    "v_lshlrev_b32 v11, 16, v211\n\t"
    "v_and_b32 v15, 0xffff0000, v211\n\t"
    "v_mfma_f32_16x16x32_bf16 v[68:71], v[20:23], v[248:251], v[68:71]\n\t"
    "ds_read_b64_tr_b16 v[18:19], v3 offset:2496\n\t"
    "s_waitcnt lgkmcnt(5)\n\t"
    "v_mfma_f32_16x16x32_bf16 v[80:83], v[24:27], v[248:251], v[80:83]\n\t"
    "ds_read_b64_tr_b16 v[20:21], v4 offset:224\n\t"
    "v_mul_f32 v11, v7, v11\n\t"
    "v_mul_f32 v15, v7, v15\n\t"
    "v_mfma_f32_16x16x32_bf16 v[88:91], v[24:27], v[252:255], v[88:91]\n\t"
    "ds_read_b64_tr_b16 v[22:23], v4 offset:2528\n\t"
    "s_waitcnt lgkmcnt(4)\n\t"
    "v_mfma_f32_16x16x32_bf16 v[92:95], v[28:31], v[252:255], v[92:95]\n\t"
    "ds_read_b64_tr_b16 v[24:25], v3 offset:256\n\t"
    "v_cvt_pk_bf16_f32 v211, v11, v15\n\t"
    "v_mfma_f32_16x16x32_bf16 v[84:87], v[28:31], v[248:251], v[84:87]\n\t"
    "ds_read_b64_tr_b16 v[26:27], v3 offset:2560\n\t"
    "s_waitcnt lgkmcnt(4)\n\t"
    "v_mfma_f32_16x16x32_bf16 v[96:99], v[16:19], v[248:251], v[96:99]\n\t"
    "ds_read_b64_tr_b16 v[28:29], v4 offset:288\n\t"
    "v_lshlrev_b32 v11, 16, v212\n\t"
    "v_and_b32 v15, 0xffff0000, v212\n\t"
    "v_mfma_f32_16x16x32_bf16 v[104:107], v[16:19], v[252:255], v[104:107]\n\t"
    "ds_read_b64_tr_b16 v[30:31], v4 offset:2592\n\t"
    "s_waitcnt lgkmcnt(4)\n\t"
    "v_mfma_f32_16x16x32_bf16 v[108:111], v[20:23], v[252:255], v[108:111]\n\t"
    "ds_read_b64_tr_b16 v[16:17], v3 offset:320\n\t"
    "v_mul_f32 v11, v8, v11\n\t"
    "v_mul_f32 v15, v8, v15\n\t"
    "v_mfma_f32_16x16x32_bf16 v[100:103], v[20:23], v[248:251], v[100:103]\n\t"
    "ds_read_b64_tr_b16 v[18:19], v3 offset:2624\n\t"
    "s_waitcnt lgkmcnt(4)\n\t"
    "v_mfma_f32_16x16x32_bf16 v[112:115], v[24:27], v[248:251], v[112:115]\n\t"
    "ds_read_b64_tr_b16 v[20:21], v4 offset:352\n\t"
    "v_cvt_pk_bf16_f32 v212, v11, v15\n\t"
    "v_mfma_f32_16x16x32_bf16 v[120:123], v[24:27], v[252:255], v[120:123]\n\t"
    "ds_read_b64_tr_b16 v[22:23], v4 offset:2656\n\t"
    "s_waitcnt lgkmcnt(4)\n\t"
    "v_mfma_f32_16x16x32_bf16 v[124:127], v[28:31], v[252:255], v[124:127]\n\t"
    "ds_read_b64_tr_b16 v[24:25], v3 offset:384\n\t"
    "v_lshlrev_b32 v11, 16, v213\n\t"
    "v_and_b32 v15, 0xffff0000, v213\n\t"
    "v_mfma_f32_16x16x32_bf16 v[116:119], v[28:31], v[248:251], v[116:119]\n\t"
    "ds_read_b64_tr_b16 v[26:27], v3 offset:2688\n\t"
    "s_waitcnt lgkmcnt(4)\n\t"
    "v_mfma_f32_16x16x32_bf16 v[128:131], v[16:19], v[248:251], v[128:131]\n\t"
    "ds_read_b64_tr_b16 v[28:29], v4 offset:416\n\t"
    "v_mul_f32 v11, v8, v11\n\t"
    "v_mul_f32 v15, v8, v15\n\t"
    "v_mfma_f32_16x16x32_bf16 v[136:139], v[16:19], v[252:255], v[136:139]\n\t"
    "ds_read_b64_tr_b16 v[30:31], v4 offset:2720\n\t"
    "s_waitcnt lgkmcnt(4)\n\t"
    "v_mfma_f32_16x16x32_bf16 v[140:143], v[20:23], v[252:255], v[140:143]\n\t"
    "ds_read_b64_tr_b16 v[16:17], v3 offset:448\n\t"
    "v_cvt_pk_bf16_f32 v213, v11, v15\n\t"
    "v_mfma_f32_16x16x32_bf16 v[132:135], v[20:23], v[248:251], v[132:135]\n\t"
    "ds_read_b64_tr_b16 v[18:19], v3 offset:2752\n\t"
    "s_waitcnt lgkmcnt(4)\n\t"
    "v_mfma_f32_16x16x32_bf16 v[144:147], v[24:27], v[248:251], v[144:147]\n\t"
    "ds_read_b64_tr_b16 v[20:21], v4 offset:480\n\t"
    "v_lshlrev_b32 v11, 16, v214\n\t"
    "v_and_b32 v15, 0xffff0000, v214\n\t"
    "v_mfma_f32_16x16x32_bf16 v[152:155], v[24:27], v[252:255], v[152:155]\n\t"
    "ds_read_b64_tr_b16 v[22:23], v4 offset:2784\n\t"
    "s_waitcnt lgkmcnt(4)\n\t"
    "v_mfma_f32_16x16x32_bf16 v[156:159], v[28:31], v[252:255], v[156:159]\n\t"
    "ds_read_b64_tr_b16 v[24:25], v3 offset:512\n\t"
    "v_mul_f32 v11, v8, v11\n\t"
    "v_mul_f32 v15, v8, v15\n\t"
    "v_mfma_f32_16x16x32_bf16 v[148:151], v[28:31], v[248:251], v[148:151]\n\t"
    "ds_read_b64_tr_b16 v[26:27], v3 offset:2816\n\t"
    "s_waitcnt lgkmcnt(4)\n\t"
    "v_mfma_f32_16x16x32_bf16 v[160:163], v[16:19], v[248:251], v[160:163]\n\t"
    "ds_read_b64_tr_b16 v[28:29], v4 offset:544\n\t"
    "v_cvt_pk_bf16_f32 v214, v11, v15\n\t"
    "v_mfma_f32_16x16x32_bf16 v[168:171], v[16:19], v[252:255], v[168:171]\n\t"
    "ds_read_b64_tr_b16 v[30:31], v4 offset:2848\n\t"
    "s_waitcnt lgkmcnt(4)\n\t"
    "v_mfma_f32_16x16x32_bf16 v[172:175], v[20:23], v[252:255], v[172:175]\n\t"
    "v_lshlrev_b32 v11, 16, v215\n\t"
    "v_and_b32 v15, 0xffff0000, v215\n\t"
    "v_mfma_f32_16x16x32_bf16 v[164:167], v[20:23], v[248:251], v[164:167]\n\t"
    "s_waitcnt lgkmcnt(0)\n\t"
    "s_barrier\n\t"
    "v_add_u32 v3, s69, v3\n\t"
    "v_add_u32 v4, s69, v4\n\t"
    "s_sub_u32 s68, 0, s68\n\t"
    "s_sub_u32 s69, 0, s69\n\t"
    "v_mfma_f32_16x16x32_bf16 v[176:179], v[24:27], v[248:251], v[176:179]\n\t"
    "ds_read_b128 v[32:35], v5 offset:55296\n\t"
    "ds_read_b128 v[36:39], v5 offset:55808\n\t"
    "ds_read_b64_tr_b16 v[16:17], v1 offset:0\n\t"
    "v_mul_f32 v11, v8, v11\n\t"
    "v_mul_f32 v15, v8, v15\n\t"
    "v_mfma_f32_16x16x32_bf16 v[184:187], v[24:27], v[252:255], v[184:187]\n\t"
    "ds_read_b64_tr_b16 v[18:19], v1 offset:2304\n\t"
    "ds_read_b64_tr_b16 v[20:21], v2 offset:32\n\t"
    "ds_read_b64_tr_b16 v[22:23], v2 offset:2336\n\t"
    "v_mfma_f32_16x16x32_bf16 v[188:191], v[28:31], v[252:255], v[188:191]\n\t"
    "v_cvt_pk_bf16_f32 v215, v11, v15\n\t"
    "v_mfma_f32_16x16x32_bf16 v[180:183], v[28:31], v[248:251], v[180:183]\n\t"
    "s_waitcnt lgkmcnt(2)\n\t"
    "v_mfma_f32_16x16x32_bf16 v[48:51], v[16:19], v[32:35], v[48:51]\n\t"
    "ds_read_b64_tr_b16 v[24:25], v1 offset:64\n\t"
    "ds_read_b64_tr_b16 v[26:27], v1 offset:2368\n\t"
    "v_lshlrev_b32 v11, 16, v216\n\t"
    "v_and_b32 v15, 0xffff0000, v216\n\t"
    "v_mfma_f32_16x16x32_bf16 v[56:59], v[16:19], v[36:39], v[56:59]\n\t"
    "ds_read_b64_tr_b16 v[28:29], v2 offset:96\n\t"
    "ds_read_b64_tr_b16 v[30:31], v2 offset:2400\n\t"
    "s_waitcnt lgkmcnt(4)\n\t"
    "v_mfma_f32_16x16x32_bf16 v[60:63], v[20:23], v[36:39], v[60:63]\n\t"
    "ds_read_b128 v[40:43], v6 offset:55296\n\t"
    "ds_read_b128 v[44:47], v6 offset:55808\n\t"
    "v_mul_f32 v11, v7, v11\n\t"
    "v_mul_f32 v15, v7, v15\n\t"
    "v_mfma_f32_16x16x32_bf16 v[52:55], v[20:23], v[32:35], v[52:55]\n\t"
    "ds_read_b64_tr_b16 v[16:17], v1 offset:128\n\t"
    "ds_read_b64_tr_b16 v[18:19], v1 offset:2432\n\t"
    "s_waitcnt lgkmcnt(6)\n\t"
    "v_mfma_f32_16x16x32_bf16 v[64:67], v[24:27], v[32:35], v[64:67]\n\t"
    "ds_read_b64_tr_b16 v[20:21], v2 offset:160\n\t"
    "v_cvt_pk_bf16_f32 v216, v11, v15\n\t"
    "v_mfma_f32_16x16x32_bf16 v[72:75], v[24:27], v[36:39], v[72:75]\n\t"
    "ds_read_b64_tr_b16 v[22:23], v2 offset:2464\n\t"
    "s_waitcnt lgkmcnt(6)\n\t"
    "v_mfma_f32_16x16x32_bf16 v[76:79], v[28:31], v[36:39], v[76:79]\n\t"
    "ds_read_b64_tr_b16 v[24:25], v1 offset:192\n\t"
    "v_lshlrev_b32 v11, 16, v217\n\t"
    "v_and_b32 v15, 0xffff0000, v217\n\t"
    "v_mfma_f32_16x16x32_bf16 v[68:71], v[28:31], v[32:35], v[68:71]\n\t"
    "ds_read_b64_tr_b16 v[26:27], v1 offset:2496\n\t"
    "s_waitcnt lgkmcnt(4)\n\t"
    "v_mfma_f32_16x16x32_bf16 v[80:83], v[16:19], v[32:35], v[80:83]\n\t"
    "ds_read_b64_tr_b16 v[28:29], v2 offset:224\n\t"
    "v_mul_f32 v11, v7, v11\n\t"
    "v_mul_f32 v15, v7, v15\n\t"
    "v_mfma_f32_16x16x32_bf16 v[88:91], v[16:19], v[36:39], v[88:91]\n\t"
    "ds_read_b64_tr_b16 v[30:31], v2 offset:2528\n\t"
    "s_waitcnt lgkmcnt(4)\n\t"
    "v_mfma_f32_16x16x32_bf16 v[92:95], v[20:23], v[36:39], v[92:95]\n\t"
    "ds_read_b64_tr_b16 v[16:17], v1 offset:256\n\t"
    "v_cvt_pk_bf16_f32 v217, v11, v15\n\t"
    "v_mfma_f32_16x16x32_bf16 v[84:87], v[20:23], v[32:35], v[84:87]\n\t"
    "ds_read_b64_tr_b16 v[18:19], v1 offset:2560\n\t"
    "s_waitcnt lgkmcnt(4)\n\t"
    "v_mfma_f32_16x16x32_bf16 v[96:99], v[24:27], v[32:35], v[96:99]\n\t"
    "ds_read_b64_tr_b16 v[20:21], v2 offset:288\n\t"
    "v_lshlrev_b32 v11, 16, v218\n\t"
    "v_and_b32 v15, 0xffff0000, v218\n\t"
    "v_mfma_f32_16x16x32_bf16 v[104:107], v[24:27], v[36:39], v[104:107]\n\t"
    "ds_read_b64_tr_b16 v[22:23], v2 offset:2592\n\t"
    "s_waitcnt lgkmcnt(4)\n\t"
    "v_mfma_f32_16x16x32_bf16 v[108:111], v[28:31], v[36:39], v[108:111]\n\t"
    "ds_read_b64_tr_b16 v[24:25], v1 offset:320\n\t"
    "v_mul_f32 v11, v7, v11\n\t"
    "v_mul_f32 v15, v7, v15\n\t"
    "v_mfma_f32_16x16x32_bf16 v[100:103], v[28:31], v[32:35], v[100:103]\n\t"
    "ds_read_b64_tr_b16 v[26:27], v1 offset:2624\n\t"
    "s_waitcnt lgkmcnt(4)\n\t"
    "v_mfma_f32_16x16x32_bf16 v[112:115], v[16:19], v[32:35], v[112:115]\n\t"
    "ds_read_b64_tr_b16 v[28:29], v2 offset:352\n\t"
    "v_cvt_pk_bf16_f32 v218, v11, v15\n\t"
    "v_mfma_f32_16x16x32_bf16 v[120:123], v[16:19], v[36:39], v[120:123]\n\t"
    "ds_read_b64_tr_b16 v[30:31], v2 offset:2656\n\t"
    "s_waitcnt lgkmcnt(4)\n\t"
    "v_mfma_f32_16x16x32_bf16 v[124:127], v[20:23], v[36:39], v[124:127]\n\t"
    "ds_read_b64_tr_b16 v[16:17], v1 offset:384\n\t"
    "v_lshlrev_b32 v11, 16, v219\n\t"
    "v_and_b32 v15, 0xffff0000, v219\n\t"
    "v_mfma_f32_16x16x32_bf16 v[116:119], v[20:23], v[32:35], v[116:119]\n\t"
    "ds_read_b64_tr_b16 v[18:19], v1 offset:2688\n\t"
    "s_waitcnt lgkmcnt(4)\n\t"
    "v_mfma_f32_16x16x32_bf16 v[128:131], v[24:27], v[32:35], v[128:131]\n\t"
    "ds_read_b64_tr_b16 v[20:21], v2 offset:416\n\t"
    "v_mul_f32 v11, v7, v11\n\t"
    "v_mul_f32 v15, v7, v15\n\t"
    "v_mfma_f32_16x16x32_bf16 v[136:139], v[24:27], v[36:39], v[136:139]\n\t"
    "ds_read_b64_tr_b16 v[22:23], v2 offset:2720\n\t"
    "s_waitcnt lgkmcnt(4)\n\t"
    "v_mfma_f32_16x16x32_bf16 v[140:143], v[28:31], v[36:39], v[140:143]\n\t"
    "ds_read_b64_tr_b16 v[24:25], v1 offset:448\n\t"
    "v_cvt_pk_bf16_f32 v219, v11, v15\n\t"
    "v_mfma_f32_16x16x32_bf16 v[132:135], v[28:31], v[32:35], v[132:135]\n\t"
    "ds_read_b64_tr_b16 v[26:27], v1 offset:2752\n\t"
    "s_waitcnt lgkmcnt(4)\n\t"
    "v_mfma_f32_16x16x32_bf16 v[144:147], v[16:19], v[32:35], v[144:147]\n\t"
    "ds_read_b64_tr_b16 v[28:29], v2 offset:480\n\t"
    "v_lshlrev_b32 v11, 16, v220\n\t"
    "v_and_b32 v15, 0xffff0000, v220\n\t"
    "v_mfma_f32_16x16x32_bf16 v[152:155], v[16:19], v[36:39], v[152:155]\n\t"
    "ds_read_b64_tr_b16 v[30:31], v2 offset:2784\n\t"
    "s_waitcnt lgkmcnt(4)\n\t"
    "v_mfma_f32_16x16x32_bf16 v[156:159], v[20:23], v[36:39], v[156:159]\n\t"
    "ds_read_b64_tr_b16 v[16:17], v1 offset:512\n\t"
    "v_mul_f32 v11, v8, v11\n\t"
    "v_mul_f32 v15, v8, v15\n\t"
    "v_mfma_f32_16x16x32_bf16 v[148:151], v[20:23], v[32:35], v[148:151]\n\t"
    "ds_read_b64_tr_b16 v[18:19], v1 offset:2816\n\t"
    "s_waitcnt lgkmcnt(4)\n\t"
    "v_mfma_f32_16x16x32_bf16 v[160:163], v[24:27], v[32:35], v[160:163]\n\t"
    "ds_read_b64_tr_b16 v[20:21], v2 offset:544\n\t"
    "v_cvt_pk_bf16_f32 v220, v11, v15\n\t"
    "v_mfma_f32_16x16x32_bf16 v[168:171], v[24:27], v[36:39], v[168:171]\n\t"
    "ds_read_b64_tr_b16 v[22:23], v2 offset:2848\n\t"
    "s_waitcnt lgkmcnt(4)\n\t"
    "v_mfma_f32_16x16x32_bf16 v[172:175], v[28:31], v[36:39], v[172:175]\n\t"
    "v_lshlrev_b32 v11, 16, v221\n\t"
    "v_and_b32 v15, 0xffff0000, v221\n\t"
    "v_mfma_f32_16x16x32_bf16 v[164:167], v[28:31], v[32:35], v[164:167]\n\t"
    "s_waitcnt lgkmcnt(0)\n\t"
    "s_barrier\n\t"
    "v_add_u32 v1, s68, v1\n\t"
    "v_add_u32 v2, s68, v2\n\t"
    "v_add_u32 v5, s68, v5\n\t"
    "v_add_u32 v6, s68, v6\n\t"
    "v_mfma_f32_16x16x32_bf16 v[176:179], v[16:19], v[32:35], v[176:179]\n\t"
    "ds_read_b64_tr_b16 v[24:25], v3 offset:0\n\t"
    "ds_read_b64_tr_b16 v[26:27], v3 offset:2304\n\t"
    "v_mul_f32 v11, v8, v11\n\t"
    "v_mul_f32 v15, v8, v15\n\t"
    "v_mfma_f32_16x16x32_bf16 v[184:187], v[16:19], v[36:39], v[184:187]\n\t"
    "ds_read_b64_tr_b16 v[28:29], v4 offset:32\n\t"
    "ds_read_b64_tr_b16 v[30:31], v4 offset:2336\n\t"
    "v_mfma_f32_16x16x32_bf16 v[188:191], v[20:23], v[36:39], v[188:191]\n\t"
    "v_cvt_pk_bf16_f32 v221, v11, v15\n\t"
    "v_mfma_f32_16x16x32_bf16 v[180:183], v[20:23], v[32:35], v[180:183]\n\t"
    "s_waitcnt lgkmcnt(2)\n\t"
    "v_mfma_f32_16x16x32_bf16 v[48:51], v[24:27], v[40:43], v[48:51]\n\t"
    "ds_read_b128 v[240:243], v5 offset:18432\n\t"
    "ds_read_b64_tr_b16 v[16:17], v3 offset:64\n\t"
    "ds_read_b64_tr_b16 v[18:19], v3 offset:2368\n\t"
    "v_lshlrev_b32 v11, 16, v222\n\t"
    "v_and_b32 v15, 0xffff0000, v222\n\t"
    "v_mfma_f32_16x16x32_bf16 v[56:59], v[24:27], v[44:47], v[56:59]\n\t"
    "ds_read_b128 v[244:247], v5 offset:18944\n\t"
    "ds_read_b64_tr_b16 v[20:21], v4 offset:96\n\t"
    "ds_read_b64_tr_b16 v[22:23], v4 offset:2400\n\t"
    "s_waitcnt lgkmcnt(6)\n\t"
    "v_mfma_f32_16x16x32_bf16 v[60:63], v[28:31], v[44:47], v[60:63]\n\t"
    "ds_read_b128 v[248:251], v6 offset:18432\n\t"
    "ds_read_b64_tr_b16 v[24:25], v3 offset:128\n\t"
    "v_mul_f32 v11, v8, v11\n\t"
    "v_mul_f32 v15, v8, v15\n\t"
    "v_mfma_f32_16x16x32_bf16 v[52:55], v[28:31], v[40:43], v[52:55]\n\t"
    "ds_read_b64_tr_b16 v[26:27], v3 offset:2432\n\t"
    "s_waitcnt lgkmcnt(6)\n\t"
    "v_mfma_f32_16x16x32_bf16 v[64:67], v[16:19], v[40:43], v[64:67]\n\t"
    "ds_read_b128 v[252:255], v6 offset:18944\n\t"
    "ds_read_b64_tr_b16 v[28:29], v4 offset:160\n\t"
    "v_cvt_pk_bf16_f32 v222, v11, v15\n\t"
    "v_mfma_f32_16x16x32_bf16 v[72:75], v[16:19], v[44:47], v[72:75]\n\t"
    "ds_read_b64_tr_b16 v[30:31], v4 offset:2464\n\t"
    "s_waitcnt lgkmcnt(6)\n\t"
    "v_mfma_f32_16x16x32_bf16 v[76:79], v[20:23], v[44:47], v[76:79]\n\t"
    "ds_read_b64_tr_b16 v[16:17], v3 offset:192\n\t"
    "v_lshlrev_b32 v11, 16, v223\n\t"
    "v_and_b32 v15, 0xffff0000, v223\n\t"
    "v_mfma_f32_16x16x32_bf16 v[68:71], v[20:23], v[40:43], v[68:71]\n\t"
    "ds_read_b64_tr_b16 v[18:19], v3 offset:2496\n\t"
    "s_waitcnt lgkmcnt(5)\n\t"
    "v_mfma_f32_16x16x32_bf16 v[80:83], v[24:27], v[40:43], v[80:83]\n\t"
    "ds_read_b64_tr_b16 v[20:21], v4 offset:224\n\t"
    "v_mul_f32 v11, v8, v11\n\t"
    "v_mul_f32 v15, v8, v15\n\t"
    "v_mfma_f32_16x16x32_bf16 v[88:91], v[24:27], v[44:47], v[88:91]\n\t"
    "ds_read_b64_tr_b16 v[22:23], v4 offset:2528\n\t"
    "s_waitcnt lgkmcnt(4)\n\t"
    "v_mfma_f32_16x16x32_bf16 v[92:95], v[28:31], v[44:47], v[92:95]\n\t"
    "ds_read_b64_tr_b16 v[24:25], v3 offset:256\n\t"
    "v_cvt_pk_bf16_f32 v223, v11, v15\n\t"
    "v_mfma_f32_16x16x32_bf16 v[84:87], v[28:31], v[40:43], v[84:87]\n\t"
    "ds_read_b64_tr_b16 v[26:27], v3 offset:2560\n\t"
    "s_waitcnt lgkmcnt(4)\n\t"
    "v_mfma_f32_16x16x32_bf16 v[96:99], v[16:19], v[40:43], v[96:99]\n\t"
    "ds_read_b64_tr_b16 v[28:29], v4 offset:288\n\t"
    "v_lshlrev_b32 v11, 16, v224\n\t"
    "v_and_b32 v15, 0xffff0000, v224\n\t"
    "v_mfma_f32_16x16x32_bf16 v[104:107], v[16:19], v[44:47], v[104:107]\n\t"
    "ds_read_b64_tr_b16 v[30:31], v4 offset:2592\n\t"
    "s_waitcnt lgkmcnt(4)\n\t"
    "v_mfma_f32_16x16x32_bf16 v[108:111], v[20:23], v[44:47], v[108:111]\n\t"
    "ds_read_b64_tr_b16 v[16:17], v3 offset:320\n\t"
    "v_mul_f32 v11, v7, v11\n\t"
    "v_mul_f32 v15, v7, v15\n\t"
    "v_mfma_f32_16x16x32_bf16 v[100:103], v[20:23], v[40:43], v[100:103]\n\t"
    "ds_read_b64_tr_b16 v[18:19], v3 offset:2624\n\t"
    "s_waitcnt lgkmcnt(4)\n\t"
    "v_mfma_f32_16x16x32_bf16 v[112:115], v[24:27], v[40:43], v[112:115]\n\t"
    "ds_read_b64_tr_b16 v[20:21], v4 offset:352\n\t"
    "v_cvt_pk_bf16_f32 v224, v11, v15\n\t"
    "v_mfma_f32_16x16x32_bf16 v[120:123], v[24:27], v[44:47], v[120:123]\n\t"
    "ds_read_b64_tr_b16 v[22:23], v4 offset:2656\n\t"
    "s_waitcnt lgkmcnt(4)\n\t"
    "v_mfma_f32_16x16x32_bf16 v[124:127], v[28:31], v[44:47], v[124:127]\n\t"
    "ds_read_b64_tr_b16 v[24:25], v3 offset:384\n\t"
    "v_lshlrev_b32 v11, 16, v225\n\t"
    "v_and_b32 v15, 0xffff0000, v225\n\t"
    "v_mfma_f32_16x16x32_bf16 v[116:119], v[28:31], v[40:43], v[116:119]\n\t"
    "ds_read_b64_tr_b16 v[26:27], v3 offset:2688\n\t"
    "s_waitcnt lgkmcnt(4)\n\t"
    "v_mfma_f32_16x16x32_bf16 v[128:131], v[16:19], v[40:43], v[128:131]\n\t"
    "ds_read_b64_tr_b16 v[28:29], v4 offset:416\n\t"
    "v_mul_f32 v11, v7, v11\n\t"
    "v_mul_f32 v15, v7, v15\n\t"
    "v_mfma_f32_16x16x32_bf16 v[136:139], v[16:19], v[44:47], v[136:139]\n\t"
    "ds_read_b64_tr_b16 v[30:31], v4 offset:2720\n\t"
    "s_waitcnt lgkmcnt(4)\n\t"
    "v_mfma_f32_16x16x32_bf16 v[140:143], v[20:23], v[44:47], v[140:143]\n\t"
    "ds_read_b64_tr_b16 v[16:17], v3 offset:448\n\t"
    "v_cvt_pk_bf16_f32 v225, v11, v15\n\t"
    "v_mfma_f32_16x16x32_bf16 v[132:135], v[20:23], v[40:43], v[132:135]\n\t"
    "ds_read_b64_tr_b16 v[18:19], v3 offset:2752\n\t"
    "s_waitcnt lgkmcnt(4)\n\t"
    "v_mfma_f32_16x16x32_bf16 v[144:147], v[24:27], v[40:43], v[144:147]\n\t"
    "ds_read_b64_tr_b16 v[20:21], v4 offset:480\n\t"
    "v_lshlrev_b32 v11, 16, v226\n\t"
    "v_and_b32 v15, 0xffff0000, v226\n\t"
    "v_mfma_f32_16x16x32_bf16 v[152:155], v[24:27], v[44:47], v[152:155]\n\t"
    "ds_read_b64_tr_b16 v[22:23], v4 offset:2784\n\t"
    "s_waitcnt lgkmcnt(4)\n\t"
    "v_mfma_f32_16x16x32_bf16 v[156:159], v[28:31], v[44:47], v[156:159]\n\t"
    "ds_read_b64_tr_b16 v[24:25], v3 offset:512\n\t"
    "v_mul_f32 v11, v7, v11\n\t"
    "v_mul_f32 v15, v7, v15\n\t"
    "v_mfma_f32_16x16x32_bf16 v[148:151], v[28:31], v[40:43], v[148:151]\n\t"
    "ds_read_b64_tr_b16 v[26:27], v3 offset:2816\n\t"
    "s_waitcnt lgkmcnt(4)\n\t"
    "v_mfma_f32_16x16x32_bf16 v[160:163], v[16:19], v[40:43], v[160:163]\n\t"
    "ds_read_b64_tr_b16 v[28:29], v4 offset:544\n\t"
    "v_cvt_pk_bf16_f32 v226, v11, v15\n\t"
    "v_mfma_f32_16x16x32_bf16 v[168:171], v[16:19], v[44:47], v[168:171]\n\t"
    "ds_read_b64_tr_b16 v[30:31], v4 offset:2848\n\t"
    "s_waitcnt lgkmcnt(4)\n\t"
    "v_mfma_f32_16x16x32_bf16 v[172:175], v[20:23], v[44:47], v[172:175]\n\t"
    "v_lshlrev_b32 v11, 16, v227\n\t"
    "v_and_b32 v15, 0xffff0000, v227\n\t"
    "v_mfma_f32_16x16x32_bf16 v[164:167], v[20:23], v[40:43], v[164:167]\n\t"
    "s_waitcnt lgkmcnt(0)\n\t"
    "s_barrier\n\t"
    "v_add_u32 v3, s69, v3\n\t"
    "v_add_u32 v4, s69, v4\n\t"
    "s_sub_u32 s68, 0, s68\n\t"
    "s_sub_u32 s69, 0, s69\n\t"
    "v_mfma_f32_16x16x32_bf16 v[176:179], v[24:27], v[40:43], v[176:179]\n\t"
    "ds_read_b128 v[32:35], v5 offset:55296\n\t"
    "ds_read_b128 v[36:39], v5 offset:55808\n\t"
    "ds_read_b64_tr_b16 v[16:17], v1 offset:0\n\t"
    "v_mul_f32 v11, v7, v11\n\t"
    "v_mul_f32 v15, v7, v15\n\t"
    "v_mfma_f32_16x16x32_bf16 v[184:187], v[24:27], v[44:47], v[184:187]\n\t"
    "ds_read_b64_tr_b16 v[18:19], v1 offset:2304\n\t"
    "ds_read_b64_tr_b16 v[20:21], v2 offset:32\n\t"
    "ds_read_b64_tr_b16 v[22:23], v2 offset:2336\n\t"
    "v_mfma_f32_16x16x32_bf16 v[188:191], v[28:31], v[44:47], v[188:191]\n\t"
    "v_cvt_pk_bf16_f32 v227, v11, v15\n\t"
    "v_mfma_f32_16x16x32_bf16 v[180:183], v[28:31], v[40:43], v[180:183]\n\t"
    "s_waitcnt lgkmcnt(2)\n\t"
    "v_mfma_f32_16x16x32_bf16 v[48:51], v[16:19], v[32:35], v[48:51]\n\t"
    "ds_read_b64_tr_b16 v[24:25], v1 offset:64\n\t"
    "ds_read_b64_tr_b16 v[26:27], v1 offset:2368\n\t"
    "v_lshlrev_b32 v11, 16, v228\n\t"
    "v_and_b32 v15, 0xffff0000, v228\n\t"
    "v_mfma_f32_16x16x32_bf16 v[56:59], v[16:19], v[36:39], v[56:59]\n\t"
    "ds_read_b64_tr_b16 v[28:29], v2 offset:96\n\t"
    "ds_read_b64_tr_b16 v[30:31], v2 offset:2400\n\t"
    "s_waitcnt lgkmcnt(4)\n\t"
    "v_mfma_f32_16x16x32_bf16 v[60:63], v[20:23], v[36:39], v[60:63]\n\t"
    "ds_read_b128 v[40:43], v6 offset:55296\n\t"
    "ds_read_b128 v[44:47], v6 offset:55808\n\t"
    "v_mul_f32 v11, v8, v11\n\t"
    "v_mul_f32 v15, v8, v15\n\t"
    "v_mfma_f32_16x16x32_bf16 v[52:55], v[20:23], v[32:35], v[52:55]\n\t"
    "ds_read_b64_tr_b16 v[16:17], v1 offset:128\n\t"
    "ds_read_b64_tr_b16 v[18:19], v1 offset:2432\n\t"
    "s_waitcnt lgkmcnt(6)\n\t"
    "v_mfma_f32_16x16x32_bf16 v[64:67], v[24:27], v[32:35], v[64:67]\n\t"
    "ds_read_b64_tr_b16 v[20:21], v2 offset:160\n\t"
    "v_cvt_pk_bf16_f32 v228, v11, v15\n\t"
    "v_mfma_f32_16x16x32_bf16 v[72:75], v[24:27], v[36:39], v[72:75]\n\t"
    "ds_read_b64_tr_b16 v[22:23], v2 offset:2464\n\t"
    "s_waitcnt lgkmcnt(6)\n\t"
    "v_mfma_f32_16x16x32_bf16 v[76:79], v[28:31], v[36:39], v[76:79]\n\t"
    "ds_read_b64_tr_b16 v[24:25], v1 offset:192\n\t"
    "v_lshlrev_b32 v11, 16, v229\n\t"
    "v_and_b32 v15, 0xffff0000, v229\n\t"
    "v_mfma_f32_16x16x32_bf16 v[68:71], v[28:31], v[32:35], v[68:71]\n\t"
    "ds_read_b64_tr_b16 v[26:27], v1 offset:2496\n\t"
    "s_waitcnt lgkmcnt(4)\n\t"
    "v_mfma_f32_16x16x32_bf16 v[80:83], v[16:19], v[32:35], v[80:83]\n\t"
    "ds_read_b64_tr_b16 v[28:29], v2 offset:224\n\t"
    "v_mul_f32 v11, v8, v11\n\t"
    "v_mul_f32 v15, v8, v15\n\t"
    "v_mfma_f32_16x16x32_bf16 v[88:91], v[16:19], v[36:39], v[88:91]\n\t"
    "ds_read_b64_tr_b16 v[30:31], v2 offset:2528\n\t"
    "s_waitcnt lgkmcnt(4)\n\t"
    "v_mfma_f32_16x16x32_bf16 v[92:95], v[20:23], v[36:39], v[92:95]\n\t"
    "ds_read_b64_tr_b16 v[16:17], v1 offset:256\n\t"
    "v_cvt_pk_bf16_f32 v229, v11, v15\n\t"
    "v_mfma_f32_16x16x32_bf16 v[84:87], v[20:23], v[32:35], v[84:87]\n\t"
    "ds_read_b64_tr_b16 v[18:19], v1 offset:2560\n\t"
    "s_waitcnt lgkmcnt(4)\n\t"
    "v_mfma_f32_16x16x32_bf16 v[96:99], v[24:27], v[32:35], v[96:99]\n\t"
    "ds_read_b64_tr_b16 v[20:21], v2 offset:288\n\t"
    "v_lshlrev_b32 v11, 16, v230\n\t"
    "v_and_b32 v15, 0xffff0000, v230\n\t"
    "v_mfma_f32_16x16x32_bf16 v[104:107], v[24:27], v[36:39], v[104:107]\n\t"
    "ds_read_b64_tr_b16 v[22:23], v2 offset:2592\n\t"
    "s_waitcnt lgkmcnt(4)\n\t"
    "v_mfma_f32_16x16x32_bf16 v[108:111], v[28:31], v[36:39], v[108:111]\n\t"
    "ds_read_b64_tr_b16 v[24:25], v1 offset:320\n\t"
    "v_mul_f32 v11, v8, v11\n\t"
    "v_mul_f32 v15, v8, v15\n\t"
    "v_mfma_f32_16x16x32_bf16 v[100:103], v[28:31], v[32:35], v[100:103]\n\t"
    "ds_read_b64_tr_b16 v[26:27], v1 offset:2624\n\t"
    "s_waitcnt lgkmcnt(4)\n\t"
    "v_mfma_f32_16x16x32_bf16 v[112:115], v[16:19], v[32:35], v[112:115]\n\t"
    "ds_read_b64_tr_b16 v[28:29], v2 offset:352\n\t"
    "v_cvt_pk_bf16_f32 v230, v11, v15\n\t"
    "v_mfma_f32_16x16x32_bf16 v[120:123], v[16:19], v[36:39], v[120:123]\n\t"
    "ds_read_b64_tr_b16 v[30:31], v2 offset:2656\n\t"
    "s_waitcnt lgkmcnt(4)\n\t"
    "v_mfma_f32_16x16x32_bf16 v[124:127], v[20:23], v[36:39], v[124:127]\n\t"
    "ds_read_b64_tr_b16 v[16:17], v1 offset:384\n\t"
    "v_lshlrev_b32 v11, 16, v231\n\t"
    "v_and_b32 v15, 0xffff0000, v231\n\t"
    "v_mfma_f32_16x16x32_bf16 v[116:119], v[20:23], v[32:35], v[116:119]\n\t"
    "ds_read_b64_tr_b16 v[18:19], v1 offset:2688\n\t"
    "s_waitcnt lgkmcnt(4)\n\t"
    "v_mfma_f32_16x16x32_bf16 v[128:131], v[24:27], v[32:35], v[128:131]\n\t"
    "ds_read_b64_tr_b16 v[20:21], v2 offset:416\n\t"
    "v_mul_f32 v11, v8, v11\n\t"
    "v_mul_f32 v15, v8, v15\n\t"
    "v_mfma_f32_16x16x32_bf16 v[136:139], v[24:27], v[36:39], v[136:139]\n\t"
    "ds_read_b64_tr_b16 v[22:23], v2 offset:2720\n\t"
    "s_waitcnt lgkmcnt(4)\n\t"
    "v_mfma_f32_16x16x32_bf16 v[140:143], v[28:31], v[36:39], v[140:143]\n\t"
    "ds_read_b64_tr_b16 v[24:25], v1 offset:448\n\t"
    "v_cvt_pk_bf16_f32 v231, v11, v15\n\t"
    "v_mfma_f32_16x16x32_bf16 v[132:135], v[28:31], v[32:35], v[132:135]\n\t"
    "ds_read_b64_tr_b16 v[26:27], v1 offset:2752\n\t"
    "s_waitcnt lgkmcnt(4)\n\t"
    "v_mfma_f32_16x16x32_bf16 v[144:147], v[16:19], v[32:35], v[144:147]\n\t"
    "ds_read_b64_tr_b16 v[28:29], v2 offset:480\n\t"
    "v_lshlrev_b32 v11, 16, v232\n\t"
    "v_and_b32 v15, 0xffff0000, v232\n\t"
    "v_mfma_f32_16x16x32_bf16 v[152:155], v[16:19], v[36:39], v[152:155]\n\t"
    "ds_read_b64_tr_b16 v[30:31], v2 offset:2784\n\t"
    "s_waitcnt lgkmcnt(4)\n\t"
    "v_mfma_f32_16x16x32_bf16 v[156:159], v[20:23], v[36:39], v[156:159]\n\t"
    "ds_read_b64_tr_b16 v[16:17], v1 offset:512\n\t"
    "v_mul_f32 v11, v7, v11\n\t"
    "v_mul_f32 v15, v7, v15\n\t"
    "v_mfma_f32_16x16x32_bf16 v[148:151], v[20:23], v[32:35], v[148:151]\n\t"
    "ds_read_b64_tr_b16 v[18:19], v1 offset:2816\n\t"
    "s_waitcnt lgkmcnt(4)\n\t"
    "v_mfma_f32_16x16x32_bf16 v[160:163], v[24:27], v[32:35], v[160:163]\n\t"
    "ds_read_b64_tr_b16 v[20:21], v2 offset:544\n\t"
    "v_cvt_pk_bf16_f32 v232, v11, v15\n\t"
    "v_mfma_f32_16x16x32_bf16 v[168:171], v[24:27], v[36:39], v[168:171]\n\t"
    "ds_read_b64_tr_b16 v[22:23], v2 offset:2848\n\t"
    "s_waitcnt lgkmcnt(4)\n\t"
    "v_mfma_f32_16x16x32_bf16 v[172:175], v[28:31], v[36:39], v[172:175]\n\t"
    "v_lshlrev_b32 v11, 16, v233\n\t"
    "v_and_b32 v15, 0xffff0000, v233\n\t"
    "v_mfma_f32_16x16x32_bf16 v[164:167], v[28:31], v[32:35], v[164:167]\n\t"
    "s_waitcnt lgkmcnt(0)\n\t"
    "s_barrier\n\t"
    "v_add_u32 v1, s68, v1\n\t"
    "v_add_u32 v2, s68, v2\n\t"
    "v_add_u32 v5, s68, v5\n\t"
    "v_add_u32 v6, s68, v6\n\t"
    "v_mfma_f32_16x16x32_bf16 v[176:179], v[16:19], v[32:35], v[176:179]\n\t"
    "ds_read_b64_tr_b16 v[24:25], v3 offset:0\n\t"
    "ds_read_b64_tr_b16 v[26:27], v3 offset:2304\n\t"
    "v_mul_f32 v11, v7, v11\n\t"
    "v_mul_f32 v15, v7, v15\n\t"
    "v_mfma_f32_16x16x32_bf16 v[184:187], v[16:19], v[36:39], v[184:187]\n\t"
    "ds_read_b64_tr_b16 v[28:29], v4 offset:32\n\t"
    "ds_read_b64_tr_b16 v[30:31], v4 offset:2336\n\t"
    "v_mfma_f32_16x16x32_bf16 v[188:191], v[20:23], v[36:39], v[188:191]\n\t"
    "v_cvt_pk_bf16_f32 v233, v11, v15\n\t"
    "v_mfma_f32_16x16x32_bf16 v[180:183], v[20:23], v[32:35], v[180:183]\n\t"
    "s_waitcnt lgkmcnt(2)\n\t"
    "v_mfma_f32_16x16x32_bf16 v[48:51], v[24:27], v[40:43], v[48:51]\n\t"
    "ds_read_b64_tr_b16 v[16:17], v3 offset:64\n\t"
    "ds_read_b64_tr_b16 v[18:19], v3 offset:2368\n\t"
    "v_lshlrev_b32 v11, 16, v234\n\t"
    "v_and_b32 v15, 0xffff0000, v234\n\t"
    "v_mfma_f32_16x16x32_bf16 v[56:59], v[24:27], v[44:47], v[56:59]\n\t"
    "ds_read_b64_tr_b16 v[20:21], v4 offset:96\n\t"
    "ds_read_b64_tr_b16 v[22:23], v4 offset:2400\n\t"
    "s_waitcnt lgkmcnt(4)\n\t"
    "v_mfma_f32_16x16x32_bf16 v[60:63], v[28:31], v[44:47], v[60:63]\n\t"
    "ds_read_b64_tr_b16 v[24:25], v3 offset:128\n\t"
    "v_mul_f32 v11, v7, v11\n\t"
    "v_mul_f32 v15, v7, v15\n\t"
    "v_mfma_f32_16x16x32_bf16 v[52:55], v[28:31], v[40:43], v[52:55]\n\t"
    "ds_read_b64_tr_b16 v[26:27], v3 offset:2432\n\t"
    "s_waitcnt lgkmcnt(4)\n\t"
    "v_mfma_f32_16x16x32_bf16 v[64:67], v[16:19], v[40:43], v[64:67]\n\t"
    "ds_read_b64_tr_b16 v[28:29], v4 offset:160\n\t"
    "v_cvt_pk_bf16_f32 v234, v11, v15\n\t"
    "v_mfma_f32_16x16x32_bf16 v[72:75], v[16:19], v[44:47], v[72:75]\n\t"
    "ds_read_b64_tr_b16 v[30:31], v4 offset:2464\n\t"
    "s_waitcnt lgkmcnt(4)\n\t"
    "v_mfma_f32_16x16x32_bf16 v[76:79], v[20:23], v[44:47], v[76:79]\n\t"
    "ds_read_b64_tr_b16 v[16:17], v3 offset:192\n\t"
    "v_lshlrev_b32 v11, 16, v235\n\t"
    "v_and_b32 v15, 0xffff0000, v235\n\t"
    "v_mfma_f32_16x16x32_bf16 v[68:71], v[20:23], v[40:43], v[68:71]\n\t"
    "ds_read_b64_tr_b16 v[18:19], v3 offset:2496\n\t"
    "s_waitcnt lgkmcnt(4)\n\t"
    "v_mfma_f32_16x16x32_bf16 v[80:83], v[24:27], v[40:43], v[80:83]\n\t"
    "ds_read_b64_tr_b16 v[20:21], v4 offset:224\n\t"
    "v_mul_f32 v11, v7, v11\n\t"
    "v_mul_f32 v15, v7, v15\n\t"
    "v_mfma_f32_16x16x32_bf16 v[88:91], v[24:27], v[44:47], v[88:91]\n\t"
    "ds_read_b64_tr_b16 v[22:23], v4 offset:2528\n\t"
    "s_waitcnt lgkmcnt(4)\n\t"
    "v_mfma_f32_16x16x32_bf16 v[92:95], v[28:31], v[44:47], v[92:95]\n\t"
    "ds_read_b64_tr_b16 v[24:25], v3 offset:256\n\t"
    "v_cvt_pk_bf16_f32 v235, v11, v15\n\t"
    "v_mfma_f32_16x16x32_bf16 v[84:87], v[28:31], v[40:43], v[84:87]\n\t"
    "ds_read_b64_tr_b16 v[26:27], v3 offset:2560\n\t"
    "s_waitcnt lgkmcnt(4)\n\t"
    "v_mfma_f32_16x16x32_bf16 v[96:99], v[16:19], v[40:43], v[96:99]\n\t"
    "ds_read_b64_tr_b16 v[28:29], v4 offset:288\n\t"
    "v_lshlrev_b32 v11, 16, v236\n\t"
    "v_and_b32 v15, 0xffff0000, v236\n\t"
    "v_mfma_f32_16x16x32_bf16 v[104:107], v[16:19], v[44:47], v[104:107]\n\t"
    "ds_read_b64_tr_b16 v[30:31], v4 offset:2592\n\t"
    "s_waitcnt lgkmcnt(4)\n\t"
    "v_mfma_f32_16x16x32_bf16 v[108:111], v[20:23], v[44:47], v[108:111]\n\t"
    "ds_read_b64_tr_b16 v[16:17], v3 offset:320\n\t"
    "v_mul_f32 v11, v8, v11\n\t"
    "v_mul_f32 v15, v8, v15\n\t"
    "v_mfma_f32_16x16x32_bf16 v[100:103], v[20:23], v[40:43], v[100:103]\n\t"
    "ds_read_b64_tr_b16 v[18:19], v3 offset:2624\n\t"
    "s_waitcnt lgkmcnt(4)\n\t"
    "v_mfma_f32_16x16x32_bf16 v[112:115], v[24:27], v[40:43], v[112:115]\n\t"
    "ds_read_b64_tr_b16 v[20:21], v4 offset:352\n\t"
    "v_cvt_pk_bf16_f32 v236, v11, v15\n\t"
    "v_mfma_f32_16x16x32_bf16 v[120:123], v[24:27], v[44:47], v[120:123]\n\t"
    "ds_read_b64_tr_b16 v[22:23], v4 offset:2656\n\t"
    "s_waitcnt lgkmcnt(4)\n\t"
    "v_mfma_f32_16x16x32_bf16 v[124:127], v[28:31], v[44:47], v[124:127]\n\t"
    "ds_read_b64_tr_b16 v[24:25], v3 offset:384\n\t"
    "v_lshlrev_b32 v11, 16, v237\n\t"
    "v_and_b32 v15, 0xffff0000, v237\n\t"
    "v_mfma_f32_16x16x32_bf16 v[116:119], v[28:31], v[40:43], v[116:119]\n\t"
    "ds_read_b64_tr_b16 v[26:27], v3 offset:2688\n\t"
    "s_waitcnt lgkmcnt(4)\n\t"
    "v_mfma_f32_16x16x32_bf16 v[128:131], v[16:19], v[40:43], v[128:131]\n\t"
    "ds_read_b64_tr_b16 v[28:29], v4 offset:416\n\t"
    "v_mul_f32 v11, v8, v11\n\t"
    "v_mul_f32 v15, v8, v15\n\t"
    "v_mfma_f32_16x16x32_bf16 v[136:139], v[16:19], v[44:47], v[136:139]\n\t"
    "ds_read_b64_tr_b16 v[30:31], v4 offset:2720\n\t"
    "s_waitcnt lgkmcnt(4)\n\t"
    "v_mfma_f32_16x16x32_bf16 v[140:143], v[20:23], v[44:47], v[140:143]\n\t"
    "ds_read_b64_tr_b16 v[16:17], v3 offset:448\n\t"
    "v_cvt_pk_bf16_f32 v237, v11, v15\n\t"
    "v_mfma_f32_16x16x32_bf16 v[132:135], v[20:23], v[40:43], v[132:135]\n\t"
    "ds_read_b64_tr_b16 v[18:19], v3 offset:2752\n\t"
    "s_waitcnt lgkmcnt(4)\n\t"
    "v_mfma_f32_16x16x32_bf16 v[144:147], v[24:27], v[40:43], v[144:147]\n\t"
    "ds_read_b64_tr_b16 v[20:21], v4 offset:480\n\t"
    "v_lshlrev_b32 v11, 16, v238\n\t"
    "v_and_b32 v15, 0xffff0000, v238\n\t"
    "v_mfma_f32_16x16x32_bf16 v[152:155], v[24:27], v[44:47], v[152:155]\n\t"
    "ds_read_b64_tr_b16 v[22:23], v4 offset:2784\n\t"
    "s_waitcnt lgkmcnt(4)\n\t"
    "v_mfma_f32_16x16x32_bf16 v[156:159], v[28:31], v[44:47], v[156:159]\n\t"
    "ds_read_b64_tr_b16 v[24:25], v3 offset:512\n\t"
    "v_mul_f32 v11, v8, v11\n\t"
    "v_mul_f32 v15, v8, v15\n\t"
    "v_mfma_f32_16x16x32_bf16 v[148:151], v[28:31], v[40:43], v[148:151]\n\t"
    "ds_read_b64_tr_b16 v[26:27], v3 offset:2816\n\t"
    "s_waitcnt lgkmcnt(4)\n\t"
    "v_mfma_f32_16x16x32_bf16 v[160:163], v[16:19], v[40:43], v[160:163]\n\t"
    "ds_read_b64_tr_b16 v[28:29], v4 offset:544\n\t"
    "v_cvt_pk_bf16_f32 v238, v11, v15\n\t"
    "v_mfma_f32_16x16x32_bf16 v[168:171], v[16:19], v[44:47], v[168:171]\n\t"
    "ds_read_b64_tr_b16 v[30:31], v4 offset:2848\n\t"
    "s_waitcnt lgkmcnt(4)\n\t"
    "v_mfma_f32_16x16x32_bf16 v[172:175], v[20:23], v[44:47], v[172:175]\n\t"
    "v_lshlrev_b32 v11, 16, v239\n\t"
    "v_and_b32 v15, 0xffff0000, v239\n\t"
    "v_mfma_f32_16x16x32_bf16 v[164:167], v[20:23], v[40:43], v[164:167]\n\t"
    "s_waitcnt lgkmcnt(0)\n\t"
    "s_barrier\n\t"
    "v_add_u32 v3, s69, v3\n\t"
    "v_add_u32 v4, s69, v4\n\t"
    "s_sub_u32 s68, 0, s68\n\t"
    "s_sub_u32 s69, 0, s69\n\t"
    "v_mfma_f32_16x16x32_bf16 v[176:179], v[24:27], v[40:43], v[176:179]\n\t"
    "ds_read_b128 v[32:35], v5 offset:55296\n\t"
    "ds_read_b128 v[36:39], v5 offset:55808\n\t"
    "ds_read_b64_tr_b16 v[16:17], v1 offset:0\n\t"
    "v_mul_f32 v11, v8, v11\n\t"
    "v_mul_f32 v15, v8, v15\n\t"
    "v_mfma_f32_16x16x32_bf16 v[184:187], v[24:27], v[44:47], v[184:187]\n\t"
    "ds_read_b64_tr_b16 v[18:19], v1 offset:2304\n\t"
    "ds_read_b64_tr_b16 v[20:21], v2 offset:32\n\t"
    "ds_read_b64_tr_b16 v[22:23], v2 offset:2336\n\t"
    "v_mfma_f32_16x16x32_bf16 v[188:191], v[28:31], v[44:47], v[188:191]\n\t"
    "v_cvt_pk_bf16_f32 v239, v11, v15\n\t"
    "v_mfma_f32_16x16x32_bf16 v[180:183], v[28:31], v[40:43], v[180:183]\n\t"
    "s_waitcnt lgkmcnt(2)\n\t"
    "v_mfma_f32_16x16x32_bf16 v[48:51], v[16:19], v[32:35], v[48:51]\n\t"
    "ds_read_b64_tr_b16 v[24:25], v1 offset:64\n\t"
    "ds_read_b64_tr_b16 v[26:27], v1 offset:2368\n\t"
    "v_lshlrev_b32 v11, 16, v240\n\t"
    "v_and_b32 v15, 0xffff0000, v240\n\t"
    "v_mfma_f32_16x16x32_bf16 v[56:59], v[16:19], v[36:39], v[56:59]\n\t"
    "ds_read_b64_tr_b16 v[28:29], v2 offset:96\n\t"
    "ds_read_b64_tr_b16 v[30:31], v2 offset:2400\n\t"
    "s_waitcnt lgkmcnt(4)\n\t"
    "v_mfma_f32_16x16x32_bf16 v[60:63], v[20:23], v[36:39], v[60:63]\n\t"
    "ds_read_b128 v[40:43], v6 offset:55296\n\t"
    "ds_read_b128 v[44:47], v6 offset:55808\n\t"
    "v_mul_f32 v11, v7, v11\n\t"
    "v_mul_f32 v15, v7, v15\n\t"
    "v_mfma_f32_16x16x32_bf16 v[52:55], v[20:23], v[32:35], v[52:55]\n\t"
    "ds_read_b64_tr_b16 v[16:17], v1 offset:128\n\t"
    "ds_read_b64_tr_b16 v[18:19], v1 offset:2432\n\t"
    "s_waitcnt lgkmcnt(6)\n\t"
    "v_mfma_f32_16x16x32_bf16 v[64:67], v[24:27], v[32:35], v[64:67]\n\t"
    "ds_read_b64_tr_b16 v[20:21], v2 offset:160\n\t"
    "v_cvt_pk_bf16_f32 v240, v11, v15\n\t"
    "v_mfma_f32_16x16x32_bf16 v[72:75], v[24:27], v[36:39], v[72:75]\n\t"
    "ds_read_b64_tr_b16 v[22:23], v2 offset:2464\n\t"
    "s_waitcnt lgkmcnt(6)\n\t"
    "v_mfma_f32_16x16x32_bf16 v[76:79], v[28:31], v[36:39], v[76:79]\n\t"
    "ds_read_b64_tr_b16 v[24:25], v1 offset:192\n\t"
    "v_lshlrev_b32 v11, 16, v241\n\t"
    "v_and_b32 v15, 0xffff0000, v241\n\t"
    "v_mfma_f32_16x16x32_bf16 v[68:71], v[28:31], v[32:35], v[68:71]\n\t"
    "ds_read_b64_tr_b16 v[26:27], v1 offset:2496\n\t"
    "s_waitcnt lgkmcnt(4)\n\t"
    "v_mfma_f32_16x16x32_bf16 v[80:83], v[16:19], v[32:35], v[80:83]\n\t"
    "ds_read_b64_tr_b16 v[28:29], v2 offset:224\n\t"
    "v_mul_f32 v11, v7, v11\n\t"
    "v_mul_f32 v15, v7, v15\n\t"
    "v_mfma_f32_16x16x32_bf16 v[88:91], v[16:19], v[36:39], v[88:91]\n\t"
    "ds_read_b64_tr_b16 v[30:31], v2 offset:2528\n\t"
    "s_waitcnt lgkmcnt(4)\n\t"
    "v_mfma_f32_16x16x32_bf16 v[92:95], v[20:23], v[36:39], v[92:95]\n\t"
    "ds_read_b64_tr_b16 v[16:17], v1 offset:256\n\t"
    "v_cvt_pk_bf16_f32 v241, v11, v15\n\t"
    "v_mfma_f32_16x16x32_bf16 v[84:87], v[20:23], v[32:35], v[84:87]\n\t"
    "ds_read_b64_tr_b16 v[18:19], v1 offset:2560\n\t"
    "s_waitcnt lgkmcnt(4)\n\t"
    "v_mfma_f32_16x16x32_bf16 v[96:99], v[24:27], v[32:35], v[96:99]\n\t"
    "ds_read_b64_tr_b16 v[20:21], v2 offset:288\n\t"
    "v_lshlrev_b32 v11, 16, v242\n\t"
    "v_and_b32 v15, 0xffff0000, v242\n\t"
    "v_mfma_f32_16x16x32_bf16 v[104:107], v[24:27], v[36:39], v[104:107]\n\t"
    "ds_read_b64_tr_b16 v[22:23], v2 offset:2592\n\t"
    "s_waitcnt lgkmcnt(4)\n\t"
    "v_mfma_f32_16x16x32_bf16 v[108:111], v[28:31], v[36:39], v[108:111]\n\t"
    "ds_read_b64_tr_b16 v[24:25], v1 offset:320\n\t"
    "v_mul_f32 v11, v7, v11\n\t"
    "v_mul_f32 v15, v7, v15\n\t"
    "v_mfma_f32_16x16x32_bf16 v[100:103], v[28:31], v[32:35], v[100:103]\n\t"
    "ds_read_b64_tr_b16 v[26:27], v1 offset:2624\n\t"
    "s_waitcnt lgkmcnt(4)\n\t"
    "v_mfma_f32_16x16x32_bf16 v[112:115], v[16:19], v[32:35], v[112:115]\n\t"
    "ds_read_b64_tr_b16 v[28:29], v2 offset:352\n\t"
    "v_cvt_pk_bf16_f32 v242, v11, v15\n\t"
    "v_mfma_f32_16x16x32_bf16 v[120:123], v[16:19], v[36:39], v[120:123]\n\t"
    "ds_read_b64_tr_b16 v[30:31], v2 offset:2656\n\t"
    "s_waitcnt lgkmcnt(4)\n\t"
    "v_mfma_f32_16x16x32_bf16 v[124:127], v[20:23], v[36:39], v[124:127]\n\t"
    "ds_read_b64_tr_b16 v[16:17], v1 offset:384\n\t"
    "v_lshlrev_b32 v11, 16, v243\n\t"
    "v_and_b32 v15, 0xffff0000, v243\n\t"
    "v_mfma_f32_16x16x32_bf16 v[116:119], v[20:23], v[32:35], v[116:119]\n\t"
    "ds_read_b64_tr_b16 v[18:19], v1 offset:2688\n\t"
    "s_waitcnt lgkmcnt(4)\n\t"
    "v_mfma_f32_16x16x32_bf16 v[128:131], v[24:27], v[32:35], v[128:131]\n\t"
    "ds_read_b64_tr_b16 v[20:21], v2 offset:416\n\t"
    "v_mul_f32 v11, v7, v11\n\t"
    "v_mul_f32 v15, v7, v15\n\t"
    "v_mfma_f32_16x16x32_bf16 v[136:139], v[24:27], v[36:39], v[136:139]\n\t"
    "ds_read_b64_tr_b16 v[22:23], v2 offset:2720\n\t"
    "s_waitcnt lgkmcnt(4)\n\t"
    "v_mfma_f32_16x16x32_bf16 v[140:143], v[28:31], v[36:39], v[140:143]\n\t"
    "ds_read_b64_tr_b16 v[24:25], v1 offset:448\n\t"
    "v_cvt_pk_bf16_f32 v243, v11, v15\n\t"
    "v_mfma_f32_16x16x32_bf16 v[132:135], v[28:31], v[32:35], v[132:135]\n\t"
    "ds_read_b64_tr_b16 v[26:27], v1 offset:2752\n\t"
    "s_waitcnt lgkmcnt(4)\n\t"
    "v_mfma_f32_16x16x32_bf16 v[144:147], v[16:19], v[32:35], v[144:147]\n\t"
    "ds_read_b64_tr_b16 v[28:29], v2 offset:480\n\t"
    "v_lshlrev_b32 v11, 16, v244\n\t"
    "v_and_b32 v15, 0xffff0000, v244\n\t"
    "v_mfma_f32_16x16x32_bf16 v[152:155], v[16:19], v[36:39], v[152:155]\n\t"
    "ds_read_b64_tr_b16 v[30:31], v2 offset:2784\n\t"
    "s_waitcnt lgkmcnt(4)\n\t"
    "v_mfma_f32_16x16x32_bf16 v[156:159], v[20:23], v[36:39], v[156:159]\n\t"
    "ds_read_b64_tr_b16 v[16:17], v1 offset:512\n\t"
    "v_mul_f32 v11, v8, v11\n\t"
    "v_mul_f32 v15, v8, v15\n\t"
    "v_mfma_f32_16x16x32_bf16 v[148:151], v[20:23], v[32:35], v[148:151]\n\t"
    "ds_read_b64_tr_b16 v[18:19], v1 offset:2816\n\t"
    "s_waitcnt lgkmcnt(4)\n\t"
    "v_mfma_f32_16x16x32_bf16 v[160:163], v[24:27], v[32:35], v[160:163]\n\t"
    "ds_read_b64_tr_b16 v[20:21], v2 offset:544\n\t"
    "v_cvt_pk_bf16_f32 v244, v11, v15\n\t"
    "v_mfma_f32_16x16x32_bf16 v[168:171], v[24:27], v[36:39], v[168:171]\n\t"
    "ds_read_b64_tr_b16 v[22:23], v2 offset:2848\n\t"
    "s_waitcnt lgkmcnt(4)\n\t"
    "v_mfma_f32_16x16x32_bf16 v[172:175], v[28:31], v[36:39], v[172:175]\n\t"
    "v_lshlrev_b32 v11, 16, v245\n\t"
    "v_and_b32 v15, 0xffff0000, v245\n\t"
    "v_mfma_f32_16x16x32_bf16 v[164:167], v[28:31], v[32:35], v[164:167]\n\t"
    "s_waitcnt lgkmcnt(0)\n\t"
    "s_barrier\n\t"
    "v_add_u32 v1, s68, v1\n\t"
    "v_add_u32 v2, s68, v2\n\t"
    "v_add_u32 v5, s68, v5\n\t"
    "v_add_u32 v6, s68, v6\n\t"
    "v_mfma_f32_16x16x32_bf16 v[176:179], v[16:19], v[32:35], v[176:179]\n\t"
    "ds_read_b64_tr_b16 v[24:25], v3 offset:0\n\t"
    "ds_read_b64_tr_b16 v[26:27], v3 offset:2304\n\t"
    "v_mul_f32 v11, v8, v11\n\t"
    "v_mul_f32 v15, v8, v15\n\t"
    "v_mfma_f32_16x16x32_bf16 v[184:187], v[16:19], v[36:39], v[184:187]\n\t"
    "ds_read_b64_tr_b16 v[28:29], v4 offset:32\n\t"
    "ds_read_b64_tr_b16 v[30:31], v4 offset:2336\n\t"
    "v_mfma_f32_16x16x32_bf16 v[188:191], v[20:23], v[36:39], v[188:191]\n\t"
    "v_cvt_pk_bf16_f32 v245, v11, v15\n\t"
    "v_mfma_f32_16x16x32_bf16 v[180:183], v[20:23], v[32:35], v[180:183]\n\t"
    "s_waitcnt lgkmcnt(2)\n\t"
    "v_mfma_f32_16x16x32_bf16 v[48:51], v[24:27], v[40:43], v[48:51]\n\t"
    "ds_read_b64_tr_b16 v[16:17], v3 offset:64\n\t"
    "ds_read_b64_tr_b16 v[18:19], v3 offset:2368\n\t"
    "v_lshlrev_b32 v11, 16, v246\n\t"
    "v_and_b32 v15, 0xffff0000, v246\n\t"
    "v_mfma_f32_16x16x32_bf16 v[56:59], v[24:27], v[44:47], v[56:59]\n\t"
    "ds_read_b64_tr_b16 v[20:21], v4 offset:96\n\t"
    "ds_read_b64_tr_b16 v[22:23], v4 offset:2400\n\t"
    "s_waitcnt lgkmcnt(4)\n\t"
    "v_mfma_f32_16x16x32_bf16 v[60:63], v[28:31], v[44:47], v[60:63]\n\t"
    "ds_read_b64_tr_b16 v[24:25], v3 offset:128\n\t"
    "v_mul_f32 v11, v8, v11\n\t"
    "v_mul_f32 v15, v8, v15\n\t"
    "v_mfma_f32_16x16x32_bf16 v[52:55], v[28:31], v[40:43], v[52:55]\n\t"
    "ds_read_b64_tr_b16 v[26:27], v3 offset:2432\n\t"
    "s_waitcnt lgkmcnt(4)\n\t"
    "v_mfma_f32_16x16x32_bf16 v[64:67], v[16:19], v[40:43], v[64:67]\n\t"
    "ds_read_b64_tr_b16 v[28:29], v4 offset:160\n\t"
    "v_cvt_pk_bf16_f32 v246, v11, v15\n\t"
    "v_mfma_f32_16x16x32_bf16 v[72:75], v[16:19], v[44:47], v[72:75]\n\t"
    "ds_read_b64_tr_b16 v[30:31], v4 offset:2464\n\t"
    "s_waitcnt lgkmcnt(4)\n\t"
    "v_mfma_f32_16x16x32_bf16 v[76:79], v[20:23], v[44:47], v[76:79]\n\t"
    "ds_read_b64_tr_b16 v[16:17], v3 offset:192\n\t"
    "v_lshlrev_b32 v11, 16, v247\n\t"
    "v_and_b32 v15, 0xffff0000, v247\n\t"
    "v_mfma_f32_16x16x32_bf16 v[68:71], v[20:23], v[40:43], v[68:71]\n\t"
    "ds_read_b64_tr_b16 v[18:19], v3 offset:2496\n\t"
    "s_waitcnt lgkmcnt(4)\n\t"
    "v_mfma_f32_16x16x32_bf16 v[80:83], v[24:27], v[40:43], v[80:83]\n\t"
    "ds_read_b64_tr_b16 v[20:21], v4 offset:224\n\t"
    "v_mul_f32 v11, v8, v11\n\t"
    "v_mul_f32 v15, v8, v15\n\t"
    "v_mfma_f32_16x16x32_bf16 v[88:91], v[24:27], v[44:47], v[88:91]\n\t"
    "ds_read_b64_tr_b16 v[22:23], v4 offset:2528\n\t"
    "s_waitcnt lgkmcnt(4)\n\t"
    "v_mfma_f32_16x16x32_bf16 v[92:95], v[28:31], v[44:47], v[92:95]\n\t"
    "ds_read_b64_tr_b16 v[24:25], v3 offset:256\n\t"
    "v_cvt_pk_bf16_f32 v247, v11, v15\n\t"
    "v_mfma_f32_16x16x32_bf16 v[84:87], v[28:31], v[40:43], v[84:87]\n\t"
    "ds_read_b64_tr_b16 v[26:27], v3 offset:2560\n\t"
    "s_waitcnt lgkmcnt(4)\n\t"
    "v_mfma_f32_16x16x32_bf16 v[96:99], v[16:19], v[40:43], v[96:99]\n\t"
    "ds_read_b64_tr_b16 v[28:29], v4 offset:288\n\t"
    "v_lshlrev_b32 v11, 16, v248\n\t"
    "v_and_b32 v15, 0xffff0000, v248\n\t"
    "v_mfma_f32_16x16x32_bf16 v[104:107], v[16:19], v[44:47], v[104:107]\n\t"
    "ds_read_b64_tr_b16 v[30:31], v4 offset:2592\n\t"
    "s_waitcnt lgkmcnt(4)\n\t"
    "v_mfma_f32_16x16x32_bf16 v[108:111], v[20:23], v[44:47], v[108:111]\n\t"
    "ds_read_b64_tr_b16 v[16:17], v3 offset:320\n\t"
    "v_mul_f32 v11, v7, v11\n\t"
    "v_mul_f32 v15, v7, v15\n\t"
    "v_mfma_f32_16x16x32_bf16 v[100:103], v[20:23], v[40:43], v[100:103]\n\t"
    "ds_read_b64_tr_b16 v[18:19], v3 offset:2624\n\t"
    "s_waitcnt lgkmcnt(4)\n\t"
    "v_mfma_f32_16x16x32_bf16 v[112:115], v[24:27], v[40:43], v[112:115]\n\t"
    "ds_read_b64_tr_b16 v[20:21], v4 offset:352\n\t"
    "v_cvt_pk_bf16_f32 v248, v11, v15\n\t"
    "v_mfma_f32_16x16x32_bf16 v[120:123], v[24:27], v[44:47], v[120:123]\n\t"
    "ds_read_b64_tr_b16 v[22:23], v4 offset:2656\n\t"
    "s_waitcnt lgkmcnt(4)\n\t"
    "v_mfma_f32_16x16x32_bf16 v[124:127], v[28:31], v[44:47], v[124:127]\n\t"
    "ds_read_b64_tr_b16 v[24:25], v3 offset:384\n\t"
    "v_lshlrev_b32 v11, 16, v249\n\t"
    "v_and_b32 v15, 0xffff0000, v249\n\t"
    "v_mfma_f32_16x16x32_bf16 v[116:119], v[28:31], v[40:43], v[116:119]\n\t"
    "ds_read_b64_tr_b16 v[26:27], v3 offset:2688\n\t"
    "s_waitcnt lgkmcnt(4)\n\t"
    "v_mfma_f32_16x16x32_bf16 v[128:131], v[16:19], v[40:43], v[128:131]\n\t"
    "ds_read_b64_tr_b16 v[28:29], v4 offset:416\n\t"
    "v_mul_f32 v11, v7, v11\n\t"
    "v_mul_f32 v15, v7, v15\n\t"
    "v_mfma_f32_16x16x32_bf16 v[136:139], v[16:19], v[44:47], v[136:139]\n\t"
    "ds_read_b64_tr_b16 v[30:31], v4 offset:2720\n\t"
    "s_waitcnt lgkmcnt(4)\n\t"
    "v_mfma_f32_16x16x32_bf16 v[140:143], v[20:23], v[44:47], v[140:143]\n\t"
    "ds_read_b64_tr_b16 v[16:17], v3 offset:448\n\t"
    "v_cvt_pk_bf16_f32 v249, v11, v15\n\t"
    "v_mfma_f32_16x16x32_bf16 v[132:135], v[20:23], v[40:43], v[132:135]\n\t"
    "ds_read_b64_tr_b16 v[18:19], v3 offset:2752\n\t"
    "s_waitcnt lgkmcnt(4)\n\t"
    "v_mfma_f32_16x16x32_bf16 v[144:147], v[24:27], v[40:43], v[144:147]\n\t"
    "ds_read_b64_tr_b16 v[20:21], v4 offset:480\n\t"
    "v_lshlrev_b32 v11, 16, v250\n\t"
    "v_and_b32 v15, 0xffff0000, v250\n\t"
    "v_mfma_f32_16x16x32_bf16 v[152:155], v[24:27], v[44:47], v[152:155]\n\t"
    "ds_read_b64_tr_b16 v[22:23], v4 offset:2784\n\t"
    "s_waitcnt lgkmcnt(4)\n\t"
    "v_mfma_f32_16x16x32_bf16 v[156:159], v[28:31], v[44:47], v[156:159]\n\t"
    "ds_read_b64_tr_b16 v[24:25], v3 offset:512\n\t"
    "v_mul_f32 v11, v7, v11\n\t"
    "v_mul_f32 v15, v7, v15\n\t"
    "v_mfma_f32_16x16x32_bf16 v[148:151], v[28:31], v[40:43], v[148:151]\n\t"
    "ds_read_b64_tr_b16 v[26:27], v3 offset:2816\n\t"
    "s_waitcnt lgkmcnt(4)\n\t"
    "v_mfma_f32_16x16x32_bf16 v[160:163], v[16:19], v[40:43], v[160:163]\n\t"
    "ds_read_b64_tr_b16 v[28:29], v4 offset:544\n\t"
    "v_cvt_pk_bf16_f32 v250, v11, v15\n\t"
    "v_mfma_f32_16x16x32_bf16 v[168:171], v[16:19], v[44:47], v[168:171]\n\t"
    "ds_read_b64_tr_b16 v[30:31], v4 offset:2848\n\t"
    "s_waitcnt lgkmcnt(4)\n\t"
    "v_mfma_f32_16x16x32_bf16 v[172:175], v[20:23], v[44:47], v[172:175]\n\t"
    "v_lshlrev_b32 v11, 16, v251\n\t"
    "v_and_b32 v15, 0xffff0000, v251\n\t"
    "v_mfma_f32_16x16x32_bf16 v[164:167], v[20:23], v[40:43], v[164:167]\n\t"
    "s_waitcnt lgkmcnt(0)\n\t"
    "s_barrier\n\t"
    "v_add_u32 v3, s69, v3\n\t"
    "v_add_u32 v4, s69, v4\n\t"
    "s_sub_u32 s68, 0, s68\n\t"
    "s_sub_u32 s69, 0, s69\n\t"
    "v_mfma_f32_16x16x32_bf16 v[176:179], v[24:27], v[40:43], v[176:179]\n\t"
    "ds_read_b128 v[32:35], v5 offset:55296\n\t"
    "ds_read_b128 v[36:39], v5 offset:55808\n\t"
    "ds_read_b64_tr_b16 v[16:17], v1 offset:0\n\t"
    "v_mul_f32 v11, v7, v11\n\t"
    "v_mul_f32 v15, v7, v15\n\t"
    "v_mfma_f32_16x16x32_bf16 v[184:187], v[24:27], v[44:47], v[184:187]\n\t"
    "ds_read_b64_tr_b16 v[18:19], v1 offset:2304\n\t"
    "ds_read_b64_tr_b16 v[20:21], v2 offset:32\n\t"
    "ds_read_b64_tr_b16 v[22:23], v2 offset:2336\n\t"
    "v_mfma_f32_16x16x32_bf16 v[188:191], v[28:31], v[44:47], v[188:191]\n\t"
    "v_cvt_pk_bf16_f32 v251, v11, v15\n\t"
    "v_mfma_f32_16x16x32_bf16 v[180:183], v[28:31], v[40:43], v[180:183]\n\t"
    "s_waitcnt lgkmcnt(2)\n\t"
    "v_mfma_f32_16x16x32_bf16 v[48:51], v[16:19], v[32:35], v[48:51]\n\t"
    "ds_read_b64_tr_b16 v[24:25], v1 offset:64\n\t"
    "ds_read_b64_tr_b16 v[26:27], v1 offset:2368\n\t"
    "v_lshlrev_b32 v11, 16, v252\n\t"
    "v_and_b32 v15, 0xffff0000, v252\n\t"
    "v_mfma_f32_16x16x32_bf16 v[56:59], v[16:19], v[36:39], v[56:59]\n\t"
    "ds_read_b64_tr_b16 v[28:29], v2 offset:96\n\t"
    "ds_read_b64_tr_b16 v[30:31], v2 offset:2400\n\t"
    "s_waitcnt lgkmcnt(4)\n\t"
    "v_mfma_f32_16x16x32_bf16 v[60:63], v[20:23], v[36:39], v[60:63]\n\t"
    "ds_read_b128 v[40:43], v6 offset:55296\n\t"
    "ds_read_b128 v[44:47], v6 offset:55808\n\t"
    "v_mul_f32 v11, v8, v11\n\t"
    "v_mfma_f32_16x16x32_bf16 v[52:55], v[20:23], v[32:35], v[52:55]\n\t"
    "ds_read_b64_tr_b16 v[16:17], v1 offset:128\n\t"
    "ds_read_b64_tr_b16 v[18:19], v1 offset:2432\n\t"
    "s_waitcnt lgkmcnt(6)\n\t"
    "v_mfma_f32_16x16x32_bf16 v[64:67], v[24:27], v[32:35], v[64:67]\n\t"
    "ds_read_b64_tr_b16 v[20:21], v2 offset:160\n\t"
    "v_mul_f32 v15, v8, v15\n\t"
    "v_mfma_f32_16x16x32_bf16 v[72:75], v[24:27], v[36:39], v[72:75]\n\t"
    "ds_read_b64_tr_b16 v[22:23], v2 offset:2464\n\t"
    "s_waitcnt lgkmcnt(6)\n\t"
    "v_mfma_f32_16x16x32_bf16 v[76:79], v[28:31], v[36:39], v[76:79]\n\t"
    "ds_read_b64_tr_b16 v[24:25], v1 offset:192\n\t"
    "v_cvt_pk_bf16_f32 v252, v11, v15\n\t"
    "v_mfma_f32_16x16x32_bf16 v[68:71], v[28:31], v[32:35], v[68:71]\n\t"
    "ds_read_b64_tr_b16 v[26:27], v1 offset:2496\n\t"
    "s_waitcnt lgkmcnt(4)\n\t"
    "v_mfma_f32_16x16x32_bf16 v[80:83], v[16:19], v[32:35], v[80:83]\n\t"
    "ds_read_b64_tr_b16 v[28:29], v2 offset:224\n\t"
    "v_lshlrev_b32 v11, 16, v253\n\t"
    "v_mfma_f32_16x16x32_bf16 v[88:91], v[16:19], v[36:39], v[88:91]\n\t"
    "ds_read_b64_tr_b16 v[30:31], v2 offset:2528\n\t"
    "s_waitcnt lgkmcnt(4)\n\t"
    "v_mfma_f32_16x16x32_bf16 v[92:95], v[20:23], v[36:39], v[92:95]\n\t"
    "ds_read_b64_tr_b16 v[16:17], v1 offset:256\n\t"
    "v_and_b32 v15, 0xffff0000, v253\n\t"
    "v_mfma_f32_16x16x32_bf16 v[84:87], v[20:23], v[32:35], v[84:87]\n\t"
    "ds_read_b64_tr_b16 v[18:19], v1 offset:2560\n\t"
    "s_waitcnt lgkmcnt(4)\n\t"
    "v_mfma_f32_16x16x32_bf16 v[96:99], v[24:27], v[32:35], v[96:99]\n\t"
    "ds_read_b64_tr_b16 v[20:21], v2 offset:288\n\t"
    "v_mul_f32 v11, v8, v11\n\t"
    "v_mfma_f32_16x16x32_bf16 v[104:107], v[24:27], v[36:39], v[104:107]\n\t"
    "ds_read_b64_tr_b16 v[22:23], v2 offset:2592\n\t"
    "s_waitcnt lgkmcnt(4)\n\t"
    "v_mfma_f32_16x16x32_bf16 v[108:111], v[28:31], v[36:39], v[108:111]\n\t"
    "ds_read_b64_tr_b16 v[24:25], v1 offset:320\n\t"
    "v_mul_f32 v15, v8, v15\n\t"
    "v_mfma_f32_16x16x32_bf16 v[100:103], v[28:31], v[32:35], v[100:103]\n\t"
    "ds_read_b64_tr_b16 v[26:27], v1 offset:2624\n\t"
    "s_waitcnt lgkmcnt(4)\n\t"
    "v_mfma_f32_16x16x32_bf16 v[112:115], v[16:19], v[32:35], v[112:115]\n\t"
    "ds_read_b64_tr_b16 v[28:29], v2 offset:352\n\t"
    "v_cvt_pk_bf16_f32 v253, v11, v15\n\t"
    "v_mfma_f32_16x16x32_bf16 v[120:123], v[16:19], v[36:39], v[120:123]\n\t"
    "ds_read_b64_tr_b16 v[30:31], v2 offset:2656\n\t"
    "s_waitcnt lgkmcnt(4)\n\t"
    "v_mfma_f32_16x16x32_bf16 v[124:127], v[20:23], v[36:39], v[124:127]\n\t"
    "ds_read_b64_tr_b16 v[16:17], v1 offset:384\n\t"
    "v_lshlrev_b32 v11, 16, v254\n\t"
    "v_and_b32 v15, 0xffff0000, v254\n\t"
    "v_mfma_f32_16x16x32_bf16 v[116:119], v[20:23], v[32:35], v[116:119]\n\t"
    "ds_read_b64_tr_b16 v[18:19], v1 offset:2688\n\t"
    "s_waitcnt lgkmcnt(4)\n\t"
    "v_mfma_f32_16x16x32_bf16 v[128:131], v[24:27], v[32:35], v[128:131]\n\t"
    "ds_read_b64_tr_b16 v[20:21], v2 offset:416\n\t"
    "v_mul_f32 v11, v8, v11\n\t"
    "v_mfma_f32_16x16x32_bf16 v[136:139], v[24:27], v[36:39], v[136:139]\n\t"
    "ds_read_b64_tr_b16 v[22:23], v2 offset:2720\n\t"
    "s_waitcnt lgkmcnt(4)\n\t"
    "v_mfma_f32_16x16x32_bf16 v[140:143], v[28:31], v[36:39], v[140:143]\n\t"
    "ds_read_b64_tr_b16 v[24:25], v1 offset:448\n\t"
    "v_mul_f32 v15, v8, v15\n\t"
    "v_mfma_f32_16x16x32_bf16 v[132:135], v[28:31], v[32:35], v[132:135]\n\t"
    "ds_read_b64_tr_b16 v[26:27], v1 offset:2752\n\t"
    "s_waitcnt lgkmcnt(4)\n\t"
    "v_mfma_f32_16x16x32_bf16 v[144:147], v[16:19], v[32:35], v[144:147]\n\t"
    "ds_read_b64_tr_b16 v[28:29], v2 offset:480\n\t"
    "v_cvt_pk_bf16_f32 v254, v11, v15\n\t"
    "v_mfma_f32_16x16x32_bf16 v[152:155], v[16:19], v[36:39], v[152:155]\n\t"
    "ds_read_b64_tr_b16 v[30:31], v2 offset:2784\n\t"
    "s_waitcnt lgkmcnt(4)\n\t"
    "v_mfma_f32_16x16x32_bf16 v[156:159], v[20:23], v[36:39], v[156:159]\n\t"
    "ds_read_b64_tr_b16 v[16:17], v1 offset:512\n\t"
    "v_lshlrev_b32 v11, 16, v255\n\t"
    "v_mfma_f32_16x16x32_bf16 v[148:151], v[20:23], v[32:35], v[148:151]\n\t"
    "ds_read_b64_tr_b16 v[18:19], v1 offset:2816\n\t"
    "s_waitcnt lgkmcnt(4)\n\t"
    "v_mfma_f32_16x16x32_bf16 v[160:163], v[24:27], v[32:35], v[160:163]\n\t"
    "ds_read_b64_tr_b16 v[20:21], v2 offset:544\n\t"
    "v_and_b32 v15, 0xffff0000, v255\n\t"
    "v_mfma_f32_16x16x32_bf16 v[168:171], v[24:27], v[36:39], v[168:171]\n\t"
    "ds_read_b64_tr_b16 v[22:23], v2 offset:2848\n\t"
    "s_waitcnt lgkmcnt(4)\n\t"
    "v_mfma_f32_16x16x32_bf16 v[172:175], v[28:31], v[36:39], v[172:175]\n\t"
    "v_mul_f32 v11, v8, v11\n\t"
    "v_mfma_f32_16x16x32_bf16 v[164:167], v[28:31], v[32:35], v[164:167]\n\t"
    "s_waitcnt lgkmcnt(0)\n\t"
    "s_barrier\n\t"
    "v_add_u32 v1, s68, v1\n\t"
    "v_add_u32 v2, s68, v2\n\t"
    "v_add_u32 v5, s68, v5\n\t"
    "v_add_u32 v6, s68, v6\n\t"
    "v_mfma_f32_16x16x32_bf16 v[176:179], v[16:19], v[32:35], v[176:179]\n\t"
    "ds_read_b64_tr_b16 v[24:25], v3 offset:0\n\t"
    "ds_read_b64_tr_b16 v[26:27], v3 offset:2304\n\t"
    "v_mul_f32 v15, v8, v15\n\t"
    "v_mfma_f32_16x16x32_bf16 v[184:187], v[16:19], v[36:39], v[184:187]\n\t"
    "ds_read_b64_tr_b16 v[28:29], v4 offset:32\n\t"
    "ds_read_b64_tr_b16 v[30:31], v4 offset:2336\n\t"
    "v_mfma_f32_16x16x32_bf16 v[188:191], v[20:23], v[36:39], v[188:191]\n\t"
    "v_cvt_pk_bf16_f32 v255, v11, v15\n\t"
    "v_mfma_f32_16x16x32_bf16 v[180:183], v[20:23], v[32:35], v[180:183]\n\t"
    "s_waitcnt lgkmcnt(2)\n\t"
    "v_mfma_f32_16x16x32_bf16 v[48:51], v[24:27], v[40:43], v[48:51]\n\t"
    "ds_read_b64_tr_b16 v[16:17], v3 offset:64\n\t"
    "ds_read_b64_tr_b16 v[18:19], v3 offset:2368\n\t"
    "v_mfma_f32_16x16x32_bf16 v[56:59], v[24:27], v[44:47], v[56:59]\n\t"
    "ds_read_b64_tr_b16 v[20:21], v4 offset:96\n\t"
    "ds_read_b64_tr_b16 v[22:23], v4 offset:2400\n\t"
    "s_waitcnt lgkmcnt(4)\n\t"
    "v_mfma_f32_16x16x32_bf16 v[60:63], v[28:31], v[44:47], v[60:63]\n\t"
    "ds_read_b64_tr_b16 v[24:25], v3 offset:128\n\t"
    "v_mfma_f32_16x16x32_bf16 v[52:55], v[28:31], v[40:43], v[52:55]\n\t"
    "ds_read_b64_tr_b16 v[26:27], v3 offset:2432\n\t"
    "s_waitcnt lgkmcnt(4)\n\t"
    "v_mfma_f32_16x16x32_bf16 v[64:67], v[16:19], v[40:43], v[64:67]\n\t"
    "ds_read_b64_tr_b16 v[28:29], v4 offset:160\n\t"
    "v_mfma_f32_16x16x32_bf16 v[72:75], v[16:19], v[44:47], v[72:75]\n\t"
    "ds_read_b64_tr_b16 v[30:31], v4 offset:2464\n\t"
    "s_waitcnt lgkmcnt(4)\n\t"
    "v_mfma_f32_16x16x32_bf16 v[76:79], v[20:23], v[44:47], v[76:79]\n\t"
    "ds_read_b64_tr_b16 v[16:17], v3 offset:192\n\t"
    "v_mfma_f32_16x16x32_bf16 v[68:71], v[20:23], v[40:43], v[68:71]\n\t"
    "ds_read_b64_tr_b16 v[18:19], v3 offset:2496\n\t"
    "s_waitcnt lgkmcnt(4)\n\t"
    "v_mfma_f32_16x16x32_bf16 v[80:83], v[24:27], v[40:43], v[80:83]\n\t"
    "ds_read_b64_tr_b16 v[20:21], v4 offset:224\n\t"
    "v_mfma_f32_16x16x32_bf16 v[88:91], v[24:27], v[44:47], v[88:91]\n\t"
    "ds_read_b64_tr_b16 v[22:23], v4 offset:2528\n\t"
    "s_waitcnt lgkmcnt(4)\n\t"
    "v_mfma_f32_16x16x32_bf16 v[92:95], v[28:31], v[44:47], v[92:95]\n\t"
    "ds_read_b64_tr_b16 v[24:25], v3 offset:256\n\t"
    "v_mul_f32 v48, v9, v48\n\t"
    "v_mul_f32 v49, v9, v49\n\t"
    "v_mul_f32 v50, v9, v50\n\t"
    "v_mul_f32 v51, v9, v51\n\t"
    "v_mul_f32 v52, v9, v52\n\t"
    "v_mfma_f32_16x16x32_bf16 v[84:87], v[28:31], v[40:43], v[84:87]\n\t"
    "ds_read_b64_tr_b16 v[26:27], v3 offset:2560\n\t"
    "v_mul_f32 v53, v9, v53\n\t"
    "v_mul_f32 v54, v9, v54\n\t"
    "v_mul_f32 v55, v9, v55\n\t"
    "v_cvt_pk_bf16_f32 v48, v48, v49\n\t"
    "v_cvt_pk_bf16_f32 v49, v50, v51\n\t"
    "s_waitcnt lgkmcnt(4)\n\t"
    "v_mfma_f32_16x16x32_bf16 v[96:99], v[16:19], v[40:43], v[96:99]\n\t"
    "ds_read_b64_tr_b16 v[28:29], v4 offset:288\n\t"
    "v_cvt_pk_bf16_f32 v50, v52, v53\n\t"
    "v_cvt_pk_bf16_f32 v51, v54, v55\n\t"
    "v_pk_max_i16 v48, v48, 0\n\t"
    "v_pk_max_i16 v49, v49, 0\n\t"
    "v_pk_max_i16 v50, v50, 0\n\t"
    "v_mfma_f32_16x16x32_bf16 v[104:107], v[16:19], v[44:47], v[104:107]\n\t"
    "ds_read_b64_tr_b16 v[30:31], v4 offset:2592\n\t"
    "v_pk_max_i16 v51, v51, 0\n\t"
    "v_mul_f32 v56, v10, v56\n\t"
    "v_mul_f32 v57, v10, v57\n\t"
    "v_mul_f32 v58, v10, v58\n\t"
    "v_mul_f32 v59, v10, v59\n\t"
    "s_waitcnt lgkmcnt(4)\n\t"
    "v_mfma_f32_16x16x32_bf16 v[108:111], v[20:23], v[44:47], v[108:111]\n\t"
    "ds_read_b64_tr_b16 v[16:17], v3 offset:320\n\t"
    "v_mul_f32 v60, v10, v60\n\t"
    "v_mul_f32 v61, v10, v61\n\t"
    "v_mul_f32 v62, v10, v62\n\t"
    "v_mul_f32 v63, v10, v63\n\t"
    "v_cvt_pk_bf16_f32 v52, v56, v57\n\t"
    "v_mfma_f32_16x16x32_bf16 v[100:103], v[20:23], v[40:43], v[100:103]\n\t"
    "ds_read_b64_tr_b16 v[18:19], v3 offset:2624\n\t"
    "v_cvt_pk_bf16_f32 v53, v58, v59\n\t"
    "v_cvt_pk_bf16_f32 v54, v60, v61\n\t"
    "v_cvt_pk_bf16_f32 v55, v62, v63\n\t"
    "v_pk_max_i16 v52, v52, 0\n\t"
    "v_pk_max_i16 v53, v53, 0\n\t"
    "s_waitcnt lgkmcnt(4)\n\t"
    "v_mfma_f32_16x16x32_bf16 v[112:115], v[24:27], v[40:43], v[112:115]\n\t"
    "ds_read_b64_tr_b16 v[20:21], v4 offset:352\n\t"
    "v_pk_max_i16 v54, v54, 0\n\t"
    "v_pk_max_i16 v55, v55, 0\n\t"
    "v_mul_f32 v64, v9, v64\n\t"
    "v_mul_f32 v65, v9, v65\n\t"
    "v_mul_f32 v66, v9, v66\n\t"
    "v_mfma_f32_16x16x32_bf16 v[120:123], v[24:27], v[44:47], v[120:123]\n\t"
    "ds_read_b64_tr_b16 v[22:23], v4 offset:2656\n\t"
    "v_mul_f32 v67, v9, v67\n\t"
    "v_mul_f32 v68, v9, v68\n\t"
    "v_mul_f32 v69, v9, v69\n\t"
    "v_mul_f32 v70, v9, v70\n\t"
    "v_mul_f32 v71, v9, v71\n\t"
    "s_waitcnt lgkmcnt(4)\n\t"
    "v_mfma_f32_16x16x32_bf16 v[124:127], v[28:31], v[44:47], v[124:127]\n\t"
    "ds_read_b64_tr_b16 v[24:25], v3 offset:384\n\t"
    "v_cvt_pk_bf16_f32 v64, v64, v65\n\t"
    "v_cvt_pk_bf16_f32 v65, v66, v67\n\t"
    "v_cvt_pk_bf16_f32 v66, v68, v69\n\t"
    "v_cvt_pk_bf16_f32 v67, v70, v71\n\t"
    "v_pk_max_i16 v64, v64, 0\n\t"
    "v_mfma_f32_16x16x32_bf16 v[116:119], v[28:31], v[40:43], v[116:119]\n\t"
    "ds_read_b64_tr_b16 v[26:27], v3 offset:2688\n\t"
    "v_pk_max_i16 v65, v65, 0\n\t"
    "v_pk_max_i16 v66, v66, 0\n\t"
    "v_pk_max_i16 v67, v67, 0\n\t"
    "v_mul_f32 v72, v10, v72\n\t"
    "v_mul_f32 v73, v10, v73\n\t"
    "s_waitcnt lgkmcnt(4)\n\t"
    "v_mfma_f32_16x16x32_bf16 v[128:131], v[16:19], v[40:43], v[128:131]\n\t"
    "ds_read_b64_tr_b16 v[28:29], v4 offset:416\n\t"
    "v_mul_f32 v74, v10, v74\n\t"
    "v_mul_f32 v75, v10, v75\n\t"
    "v_mul_f32 v76, v10, v76\n\t"
    "v_mul_f32 v77, v10, v77\n\t"
    "v_mul_f32 v78, v10, v78\n\t"
    "v_mfma_f32_16x16x32_bf16 v[136:139], v[16:19], v[44:47], v[136:139]\n\t"
    "ds_read_b64_tr_b16 v[30:31], v4 offset:2720\n\t"
    "v_mul_f32 v79, v10, v79\n\t"
    "v_cvt_pk_bf16_f32 v68, v72, v73\n\t"
    "v_cvt_pk_bf16_f32 v69, v74, v75\n\t"
    "v_cvt_pk_bf16_f32 v70, v76, v77\n\t"
    "v_cvt_pk_bf16_f32 v71, v78, v79\n\t"
    "s_waitcnt lgkmcnt(4)\n\t"
    "v_mfma_f32_16x16x32_bf16 v[140:143], v[20:23], v[44:47], v[140:143]\n\t"
    "ds_read_b64_tr_b16 v[16:17], v3 offset:448\n\t"
    "v_pk_max_i16 v68, v68, 0\n\t"
    "v_pk_max_i16 v69, v69, 0\n\t"
    "v_pk_max_i16 v70, v70, 0\n\t"
    "v_pk_max_i16 v71, v71, 0\n\t"
    "v_mul_f32 v80, v9, v80\n\t"
    "v_mfma_f32_16x16x32_bf16 v[132:135], v[20:23], v[40:43], v[132:135]\n\t"
    "ds_read_b64_tr_b16 v[18:19], v3 offset:2752\n\t"
    "v_mul_f32 v81, v9, v81\n\t"
    "v_mul_f32 v82, v9, v82\n\t"
    "v_mul_f32 v83, v9, v83\n\t"
    "v_mul_f32 v84, v9, v84\n\t"
    "s_waitcnt lgkmcnt(4)\n\t"
    "v_mfma_f32_16x16x32_bf16 v[144:147], v[24:27], v[40:43], v[144:147]\n\t"
    "ds_read_b64_tr_b16 v[20:21], v4 offset:480\n\t"
    "v_mul_f32 v85, v9, v85\n\t"
    "v_mul_f32 v86, v9, v86\n\t"
    "v_mul_f32 v87, v9, v87\n\t"
    "v_cvt_pk_bf16_f32 v80, v80, v81\n\t"
    "v_cvt_pk_bf16_f32 v81, v82, v83\n\t"
    "v_mfma_f32_16x16x32_bf16 v[152:155], v[24:27], v[44:47], v[152:155]\n\t"
    "ds_read_b64_tr_b16 v[22:23], v4 offset:2784\n\t"
    "v_cvt_pk_bf16_f32 v82, v84, v85\n\t"
    "v_cvt_pk_bf16_f32 v83, v86, v87\n\t"
    "v_pk_max_i16 v80, v80, 0\n\t"
    "v_pk_max_i16 v81, v81, 0\n\t"
    "v_pk_max_i16 v82, v82, 0\n\t"
    "s_waitcnt lgkmcnt(4)\n\t"
    "v_mfma_f32_16x16x32_bf16 v[156:159], v[28:31], v[44:47], v[156:159]\n\t"
    "ds_read_b64_tr_b16 v[24:25], v3 offset:512\n\t"
    "v_pk_max_i16 v83, v83, 0\n\t"
    "v_mul_f32 v88, v10, v88\n\t"
    "v_mul_f32 v89, v10, v89\n\t"
    "v_mul_f32 v90, v10, v90\n\t"
    "v_mul_f32 v91, v10, v91\n\t"
    "v_mfma_f32_16x16x32_bf16 v[148:151], v[28:31], v[40:43], v[148:151]\n\t"
    "ds_read_b64_tr_b16 v[26:27], v3 offset:2816\n\t"
    "v_mul_f32 v92, v10, v92\n\t"
    "v_mul_f32 v93, v10, v93\n\t"
    "v_mul_f32 v94, v10, v94\n\t"
    "v_mul_f32 v95, v10, v95\n\t"
    "v_cvt_pk_bf16_f32 v84, v88, v89\n\t"
    "s_waitcnt lgkmcnt(4)\n\t"
    "v_mfma_f32_16x16x32_bf16 v[160:163], v[16:19], v[40:43], v[160:163]\n\t"
    "ds_read_b64_tr_b16 v[28:29], v4 offset:544\n\t"
    "v_cvt_pk_bf16_f32 v85, v90, v91\n\t"
    "v_cvt_pk_bf16_f32 v86, v92, v93\n\t"
    "v_cvt_pk_bf16_f32 v87, v94, v95\n\t"
    "v_pk_max_i16 v84, v84, 0\n\t"
    "v_pk_max_i16 v85, v85, 0\n\t"
    "v_mfma_f32_16x16x32_bf16 v[168:171], v[16:19], v[44:47], v[168:171]\n\t"
    "ds_read_b64_tr_b16 v[30:31], v4 offset:2848\n\t"
    "v_pk_max_i16 v86, v86, 0\n\t"
    "v_pk_max_i16 v87, v87, 0\n\t"
    "v_mul_f32 v96, v9, v96\n\t"
    "v_mul_f32 v97, v9, v97\n\t"
    "v_mul_f32 v98, v9, v98\n\t"
    "s_waitcnt lgkmcnt(4)\n\t"
    "v_mfma_f32_16x16x32_bf16 v[172:175], v[20:23], v[44:47], v[172:175]\n\t"
    "v_mul_f32 v99, v9, v99\n\t"
    "v_mul_f32 v100, v9, v100\n\t"
    "v_mul_f32 v101, v9, v101\n\t"
    "v_mul_f32 v102, v9, v102\n\t"
    "v_mul_f32 v103, v9, v103\n\t"
    "v_mfma_f32_16x16x32_bf16 v[164:167], v[20:23], v[40:43], v[164:167]\n\t"
    "v_cvt_pk_bf16_f32 v96, v96, v97\n\t"
    "v_cvt_pk_bf16_f32 v97, v98, v99\n\t"
    "v_cvt_pk_bf16_f32 v98, v100, v101\n\t"
    "v_cvt_pk_bf16_f32 v99, v102, v103\n\t"
    "v_pk_max_i16 v96, v96, 0\n\t"
    "s_waitcnt lgkmcnt(0)\n\t"
    "v_mfma_f32_16x16x32_bf16 v[176:179], v[24:27], v[40:43], v[176:179]\n\t"
    "v_pk_max_i16 v97, v97, 0\n\t"
    "v_pk_max_i16 v98, v98, 0\n\t"
    "v_pk_max_i16 v99, v99, 0\n\t"
    "v_mul_f32 v104, v10, v104\n\t"
    "v_mul_f32 v105, v10, v105\n\t"
    "v_mfma_f32_16x16x32_bf16 v[184:187], v[24:27], v[44:47], v[184:187]\n\t"
    "v_mul_f32 v106, v10, v106\n\t"
    "v_mul_f32 v107, v10, v107\n\t"
    "v_mul_f32 v108, v10, v108\n\t"
    "v_mul_f32 v109, v10, v109\n\t"
    "v_mul_f32 v110, v10, v110\n\t"
    "v_mfma_f32_16x16x32_bf16 v[188:191], v[28:31], v[44:47], v[188:191]\n\t"
    "v_mul_f32 v111, v10, v111\n\t"
    "v_cvt_pk_bf16_f32 v100, v104, v105\n\t"
    "v_cvt_pk_bf16_f32 v101, v106, v107\n\t"
    "v_cvt_pk_bf16_f32 v102, v108, v109\n\t"
    "v_cvt_pk_bf16_f32 v103, v110, v111\n\t"
    "v_mfma_f32_16x16x32_bf16 v[180:183], v[28:31], v[40:43], v[180:183]\n\t"
    "v_pk_max_i16 v100, v100, 0\n\t"
    "v_pk_max_i16 v101, v101, 0\n\t"
    "v_pk_max_i16 v102, v102, 0\n\t"
    "v_pk_max_i16 v103, v103, 0\n\t"
    "v_lshrrev_b32 v14, 2, v12\n\t"
    "v_lshl_add_u32 v14, v13, 2, v14\n\t"
    "v_bfe_u32 v15, v14, 1, 2\n\t"
    "v_lshlrev_b32 v14, 7, v14\n\t"
    "v_and_b32 v11, 3, v12\n\t"
    "v_lshl_add_u32 v14, v11, 3, v14\n\t"
    "v_add_u32 v14, 110592, v14\n\t"
    "v_xor_b32 v11, 0, v15\n\t"
    "v_lshl_add_u32 v1, v11, 5, v14\n\t"
    "v_xor_b32 v11, 1, v15\n\t"
    "v_lshl_add_u32 v2, v11, 5, v14\n\t"
    "v_xor_b32 v11, 2, v15\n\t"
    "v_lshl_add_u32 v3, v11, 5, v14\n\t"
    "v_xor_b32 v11, 3, v15\n\t"
    "v_lshl_add_u32 v4, v11, 5, v14\n\t"
    "v_mov_b32 v5, v0\n\t"
    "v_mov_b32 v6, 147968\n\t"
    "ds_read_b32 v6, v6\n\t"
    "v_mov_b32 v14, 0\n\t"
    "s_mul_i32 s85, s48, 2304\n\t"
    "s_lshl_b32 s66, s64, 6\n\t"
    "s_add_u32 s85, s85, s66\n\t"
    "s_lshl_b32 s66, s49, 2\n\t"
    "s_add_u32 s85, s85, s66\n\t"
    "s_add_u32 s85, s85, 4096\n\t"
    "s_waitcnt lgkmcnt(0)\n\t"
    "s_barrier\n\t"
    "ds_read_b64_tr_b16 v[16:17], v1 offset:0\n\t"
    "ds_read_b64_tr_b16 v[18:19], v1 offset:2048\n\t"
    "ds_read_b64_tr_b16 v[20:21], v2 offset:0\n\t"
    "ds_read_b64_tr_b16 v[22:23], v2 offset:2048\n\t"
    "s_mov_b32 s68, -73728\n\t"
    "s_mov_b32 s69, 73728\n\t"
    "s_mov_b32 s66, 0\n\t"
    "s_lshl_b32 s72, s49, 14\n\t"
    "s_lshl_b32 s73, s64, 12\n\t"
    "s_add_u32 s74, s72, s73\n\t"
    "s_mov_b32 s75, s52\n\t"
    "s_lshl_b32 s72, s75, 18\n\t"
    "s_add_u32 s72, s72, s74\n\t"
    "s_waitcnt lgkmcnt(2)\n\t"
    "v_mfma_f32_16x16x32_bf16 v[56:59], v[16:19], v[48:51], 0\n\t"
    "ds_read_b64_tr_b16 v[24:25], v3 offset:0\n\t"
    "ds_read_b64_tr_b16 v[26:27], v3 offset:2048\n\t"
    "v_mul_f32 v112, v9, v112\n\t"
    "v_mul_f32 v113, v9, v113\n\t"
    "v_mul_f32 v114, v9, v114\n\t"
    "v_mul_f32 v115, v9, v115\n\t"
    "v_mfma_f32_16x16x32_bf16 v[88:91], v[16:19], v[52:55], 0\n\t"
    "ds_read_b64_tr_b16 v[28:29], v4 offset:0\n\t"
    "ds_read_b64_tr_b16 v[30:31], v4 offset:2048\n\t"
    "v_mul_f32 v116, v9, v116\n\t"
    "v_mul_f32 v117, v9, v117\n\t"
    "v_mul_f32 v118, v9, v118\n\t"
    "v_mul_f32 v119, v9, v119\n\t"
    "s_waitcnt lgkmcnt(4)\n\t"
    "v_mfma_f32_16x16x32_bf16 v[92:95], v[20:23], v[52:55], 0\n\t"
    "ds_read_b64_tr_b16 v[16:17], v1 offset:4096\n\t"
    "v_cvt_pk_bf16_f32 v112, v112, v113\n\t"
    "v_cvt_pk_bf16_f32 v113, v114, v115\n\t"
    "v_cvt_pk_bf16_f32 v114, v116, v117\n\t"
    "v_cvt_pk_bf16_f32 v115, v118, v119\n\t"
    "v_mfma_f32_16x16x32_bf16 v[60:63], v[20:23], v[48:51], 0\n\t"
    "ds_read_b64_tr_b16 v[18:19], v1 offset:6144\n\t"
    "v_pk_max_i16 v112, v112, 0\n\t"
    "v_pk_max_i16 v113, v113, 0\n\t"
    "v_pk_max_i16 v114, v114, 0\n\t"
    "v_pk_max_i16 v115, v115, 0\n\t"
    "s_waitcnt lgkmcnt(4)\n\t"
    "v_mfma_f32_16x16x32_bf16 v[72:75], v[24:27], v[48:51], 0\n\t"
    "ds_read_b64_tr_b16 v[20:21], v2 offset:4096\n\t"
    "v_mul_f32 v120, v10, v120\n\t"
    "v_mul_f32 v121, v10, v121\n\t"
    "v_mul_f32 v122, v10, v122\n\t"
    "v_mul_f32 v123, v10, v123\n\t"
    "v_mfma_f32_16x16x32_bf16 v[104:107], v[24:27], v[52:55], 0\n\t"
    "ds_read_b64_tr_b16 v[22:23], v2 offset:6144\n\t"
    "v_mul_f32 v124, v10, v124\n\t"
    "v_mul_f32 v125, v10, v125\n\t"
    "v_mul_f32 v126, v10, v126\n\t"
    "v_mul_f32 v127, v10, v127\n\t"
    "s_waitcnt lgkmcnt(4)\n\t"
    "v_mfma_f32_16x16x32_bf16 v[108:111], v[28:31], v[52:55], 0\n\t"
    "ds_read_b64_tr_b16 v[24:25], v3 offset:4096\n\t"
    "v_cvt_pk_bf16_f32 v116, v120, v121\n\t"
    "v_cvt_pk_bf16_f32 v117, v122, v123\n\t"
    "v_cvt_pk_bf16_f32 v118, v124, v125\n\t"
    "v_cvt_pk_bf16_f32 v119, v126, v127\n\t"
    "v_mfma_f32_16x16x32_bf16 v[76:79], v[28:31], v[48:51], 0\n\t"
    "ds_read_b64_tr_b16 v[26:27], v3 offset:6144\n\t"
    "v_pk_max_i16 v116, v116, 0\n\t"
    "v_pk_max_i16 v117, v117, 0\n\t"
    "v_pk_max_i16 v118, v118, 0\n\t"
    "v_pk_max_i16 v119, v119, 0\n\t"
    "s_waitcnt lgkmcnt(4)\n\t"
    "v_mfma_f32_16x16x32_bf16 v[56:59], v[16:19], v[64:67], v[56:59]\n\t"
    "ds_read_b64_tr_b16 v[28:29], v4 offset:4096\n\t"
    "v_mul_f32 v128, v9, v128\n\t"
    "v_mul_f32 v129, v9, v129\n\t"
    "v_mul_f32 v130, v9, v130\n\t"
    "v_mul_f32 v131, v9, v131\n\t"
    "v_mfma_f32_16x16x32_bf16 v[88:91], v[16:19], v[68:71], v[88:91]\n\t"
    "ds_read_b64_tr_b16 v[30:31], v4 offset:6144\n\t"
    "v_mul_f32 v132, v9, v132\n\t"
    "v_mul_f32 v133, v9, v133\n\t"
    "v_mul_f32 v134, v9, v134\n\t"
    "v_mul_f32 v135, v9, v135\n\t"
    "s_waitcnt lgkmcnt(4)\n\t"
    "v_mfma_f32_16x16x32_bf16 v[92:95], v[20:23], v[68:71], v[92:95]\n\t"
    "ds_read_b64_tr_b16 v[16:17], v1 offset:8192\n\t"
    "v_cvt_pk_bf16_f32 v128, v128, v129\n\t"
    "v_cvt_pk_bf16_f32 v129, v130, v131\n\t"
    "v_cvt_pk_bf16_f32 v130, v132, v133\n\t"
    "v_cvt_pk_bf16_f32 v131, v134, v135\n\t"
    "v_mfma_f32_16x16x32_bf16 v[60:63], v[20:23], v[64:67], v[60:63]\n\t"
    "ds_read_b64_tr_b16 v[18:19], v1 offset:10240\n\t"
    "v_pk_max_i16 v128, v128, 0\n\t"
    "v_pk_max_i16 v129, v129, 0\n\t"
    "v_pk_max_i16 v130, v130, 0\n\t"
    "v_pk_max_i16 v131, v131, 0\n\t"
    "s_waitcnt lgkmcnt(4)\n\t"
    "v_mfma_f32_16x16x32_bf16 v[72:75], v[24:27], v[64:67], v[72:75]\n\t"
    "ds_read_b64_tr_b16 v[20:21], v2 offset:8192\n\t"
    "v_mul_f32 v136, v10, v136\n\t"
    "v_mul_f32 v137, v10, v137\n\t"
    "v_mul_f32 v138, v10, v138\n\t"
    "v_mul_f32 v139, v10, v139\n\t"
    "v_mfma_f32_16x16x32_bf16 v[104:107], v[24:27], v[68:71], v[104:107]\n\t"
    "ds_read_b64_tr_b16 v[22:23], v2 offset:10240\n\t"
    "v_mul_f32 v140, v10, v140\n\t"
    "v_mul_f32 v141, v10, v141\n\t"
    "v_mul_f32 v142, v10, v142\n\t"
    "v_mul_f32 v143, v10, v143\n\t"
    "s_waitcnt lgkmcnt(4)\n\t"
    "v_mfma_f32_16x16x32_bf16 v[108:111], v[28:31], v[68:71], v[108:111]\n\t"
    "ds_read_b64_tr_b16 v[24:25], v3 offset:8192\n\t"
    "v_cvt_pk_bf16_f32 v132, v136, v137\n\t"
    "v_cvt_pk_bf16_f32 v133, v138, v139\n\t"
    "v_cvt_pk_bf16_f32 v134, v140, v141\n\t"
    "v_cvt_pk_bf16_f32 v135, v142, v143\n\t"
    "v_mfma_f32_16x16x32_bf16 v[76:79], v[28:31], v[64:67], v[76:79]\n\t"
    "ds_read_b64_tr_b16 v[26:27], v3 offset:10240\n\t"
    "v_pk_max_i16 v132, v132, 0\n\t"
    "v_pk_max_i16 v133, v133, 0\n\t"
    "v_pk_max_i16 v134, v134, 0\n\t"
    "v_pk_max_i16 v135, v135, 0\n\t"
    "s_waitcnt lgkmcnt(4)\n\t"
    "v_mfma_f32_16x16x32_bf16 v[56:59], v[16:19], v[80:83], v[56:59]\n\t"
    "ds_read_b64_tr_b16 v[28:29], v4 offset:8192\n\t"
    "v_mul_f32 v144, v9, v144\n\t"
    "v_mul_f32 v145, v9, v145\n\t"
    "v_mul_f32 v146, v9, v146\n\t"
    "v_mul_f32 v147, v9, v147\n\t"
    "v_mfma_f32_16x16x32_bf16 v[88:91], v[16:19], v[84:87], v[88:91]\n\t"
    "ds_read_b64_tr_b16 v[30:31], v4 offset:10240\n\t"
    "v_mul_f32 v148, v9, v148\n\t"
    "v_mul_f32 v149, v9, v149\n\t"
    "v_mul_f32 v150, v9, v150\n\t"
    "v_mul_f32 v151, v9, v151\n\t"
    "s_waitcnt lgkmcnt(4)\n\t"
    "v_mfma_f32_16x16x32_bf16 v[92:95], v[20:23], v[84:87], v[92:95]\n\t"
    "ds_read_b64_tr_b16 v[16:17], v1 offset:12288\n\t"
    "v_cvt_pk_bf16_f32 v144, v144, v145\n\t"
    "v_cvt_pk_bf16_f32 v145, v146, v147\n\t"
    "v_cvt_pk_bf16_f32 v146, v148, v149\n\t"
    "v_cvt_pk_bf16_f32 v147, v150, v151\n\t"
    "v_mfma_f32_16x16x32_bf16 v[60:63], v[20:23], v[80:83], v[60:63]\n\t"
    "ds_read_b64_tr_b16 v[18:19], v1 offset:14336\n\t"
    "v_pk_max_i16 v144, v144, 0\n\t"
    "v_pk_max_i16 v145, v145, 0\n\t"
    "v_pk_max_i16 v146, v146, 0\n\t"
    "v_pk_max_i16 v147, v147, 0\n\t"
    "s_waitcnt lgkmcnt(4)\n\t"
    "v_mfma_f32_16x16x32_bf16 v[72:75], v[24:27], v[80:83], v[72:75]\n\t"
    "ds_read_b64_tr_b16 v[20:21], v2 offset:12288\n\t"
    "v_mul_f32 v152, v10, v152\n\t"
    "v_mul_f32 v153, v10, v153\n\t"
    "v_mul_f32 v154, v10, v154\n\t"
    "v_mul_f32 v155, v10, v155\n\t"
    "v_mfma_f32_16x16x32_bf16 v[104:107], v[24:27], v[84:87], v[104:107]\n\t"
    "ds_read_b64_tr_b16 v[22:23], v2 offset:14336\n\t"
    "v_mul_f32 v156, v10, v156\n\t"
    "v_mul_f32 v157, v10, v157\n\t"
    "v_mul_f32 v158, v10, v158\n\t"
    "v_mul_f32 v159, v10, v159\n\t"
    "s_waitcnt lgkmcnt(4)\n\t"
    "v_mfma_f32_16x16x32_bf16 v[108:111], v[28:31], v[84:87], v[108:111]\n\t"
    "ds_read_b64_tr_b16 v[24:25], v3 offset:12288\n\t"
    "v_cvt_pk_bf16_f32 v148, v152, v153\n\t"
    "v_cvt_pk_bf16_f32 v149, v154, v155\n\t"
    "v_cvt_pk_bf16_f32 v150, v156, v157\n\t"
    "v_cvt_pk_bf16_f32 v151, v158, v159\n\t"
    "v_mfma_f32_16x16x32_bf16 v[76:79], v[28:31], v[80:83], v[76:79]\n\t"
    "ds_read_b64_tr_b16 v[26:27], v3 offset:14336\n\t"
    "v_pk_max_i16 v148, v148, 0\n\t"
    "v_pk_max_i16 v149, v149, 0\n\t"
    "v_pk_max_i16 v150, v150, 0\n\t"
    "v_pk_max_i16 v151, v151, 0\n\t"
    "s_waitcnt lgkmcnt(4)\n\t"
    "v_mfma_f32_16x16x32_bf16 v[56:59], v[16:19], v[96:99], v[56:59]\n\t"
    "ds_read_b64_tr_b16 v[28:29], v4 offset:12288\n\t"
    "v_mul_f32 v160, v9, v160\n\t"
    "v_mul_f32 v161, v9, v161\n\t"
    "v_mul_f32 v162, v9, v162\n\t"
    "v_mul_f32 v163, v9, v163\n\t"
    "v_mfma_f32_16x16x32_bf16 v[88:91], v[16:19], v[100:103], v[88:91]\n\t"
    "ds_read_b64_tr_b16 v[30:31], v4 offset:14336\n\t"
    "v_mul_f32 v164, v9, v164\n\t"
    "v_mul_f32 v165, v9, v165\n\t"
    "v_mul_f32 v166, v9, v166\n\t"
    "v_mul_f32 v167, v9, v167\n\t"
    "s_waitcnt lgkmcnt(4)\n\t"
    "v_mfma_f32_16x16x32_bf16 v[92:95], v[20:23], v[100:103], v[92:95]\n\t"
    "ds_read_b64_tr_b16 v[16:17], v1 offset:16384\n\t"
    "v_cvt_pk_bf16_f32 v160, v160, v161\n\t"
    "v_cvt_pk_bf16_f32 v161, v162, v163\n\t"
    "v_cvt_pk_bf16_f32 v162, v164, v165\n\t"
    "v_cvt_pk_bf16_f32 v163, v166, v167\n\t"
    "v_mfma_f32_16x16x32_bf16 v[60:63], v[20:23], v[96:99], v[60:63]\n\t"
    "ds_read_b64_tr_b16 v[18:19], v1 offset:18432\n\t"
    "v_pk_max_i16 v160, v160, 0\n\t"
    "v_pk_max_i16 v161, v161, 0\n\t"
    "v_pk_max_i16 v162, v162, 0\n\t"
    "v_pk_max_i16 v163, v163, 0\n\t"
    "s_waitcnt lgkmcnt(4)\n\t"
    "v_mfma_f32_16x16x32_bf16 v[72:75], v[24:27], v[96:99], v[72:75]\n\t"
    "ds_read_b64_tr_b16 v[20:21], v2 offset:16384\n\t"
    "v_mul_f32 v168, v10, v168\n\t"
    "v_mul_f32 v169, v10, v169\n\t"
    "v_mul_f32 v170, v10, v170\n\t"
    "v_mul_f32 v171, v10, v171\n\t"
    "v_mfma_f32_16x16x32_bf16 v[104:107], v[24:27], v[100:103], v[104:107]\n\t"
    "ds_read_b64_tr_b16 v[22:23], v2 offset:18432\n\t"
    "v_mul_f32 v172, v10, v172\n\t"
    "v_mul_f32 v173, v10, v173\n\t"
    "v_mul_f32 v174, v10, v174\n\t"
    "v_mul_f32 v175, v10, v175\n\t"
    "s_waitcnt lgkmcnt(4)\n\t"
    "v_mfma_f32_16x16x32_bf16 v[108:111], v[28:31], v[100:103], v[108:111]\n\t"
    "ds_read_b64_tr_b16 v[24:25], v3 offset:16384\n\t"
    "v_cvt_pk_bf16_f32 v164, v168, v169\n\t"
    "v_cvt_pk_bf16_f32 v165, v170, v171\n\t"
    "v_cvt_pk_bf16_f32 v166, v172, v173\n\t"
    "v_cvt_pk_bf16_f32 v167, v174, v175\n\t"
    "v_mfma_f32_16x16x32_bf16 v[76:79], v[28:31], v[96:99], v[76:79]\n\t"
    "ds_read_b64_tr_b16 v[26:27], v3 offset:18432\n\t"
    "v_pk_max_i16 v164, v164, 0\n\t"
    "v_pk_max_i16 v165, v165, 0\n\t"
    "v_pk_max_i16 v166, v166, 0\n\t"
    "v_pk_max_i16 v167, v167, 0\n\t"
    "s_waitcnt lgkmcnt(4)\n\t"
    "v_mfma_f32_16x16x32_bf16 v[56:59], v[16:19], v[112:115], v[56:59]\n\t"
    "ds_read_b64_tr_b16 v[28:29], v4 offset:16384\n\t"
    "v_mul_f32 v176, v9, v176\n\t"
    "v_mul_f32 v177, v9, v177\n\t"
    "v_mul_f32 v178, v9, v178\n\t"
    "v_mul_f32 v179, v9, v179\n\t"
    "v_mfma_f32_16x16x32_bf16 v[88:91], v[16:19], v[116:119], v[88:91]\n\t"
    "ds_read_b64_tr_b16 v[30:31], v4 offset:18432\n\t"
    "v_mul_f32 v180, v9, v180\n\t"
    "v_mul_f32 v181, v9, v181\n\t"
    "v_mul_f32 v182, v9, v182\n\t"
    "v_mul_f32 v183, v9, v183\n\t"
    "s_waitcnt lgkmcnt(4)\n\t"
    "v_mfma_f32_16x16x32_bf16 v[92:95], v[20:23], v[116:119], v[92:95]\n\t"
    "ds_read_b64_tr_b16 v[16:17], v1 offset:20480\n\t"
    "v_cvt_pk_bf16_f32 v176, v176, v177\n\t"
    "v_cvt_pk_bf16_f32 v177, v178, v179\n\t"
    "v_cvt_pk_bf16_f32 v178, v180, v181\n\t"
    "v_cvt_pk_bf16_f32 v179, v182, v183\n\t"
    "v_mfma_f32_16x16x32_bf16 v[60:63], v[20:23], v[112:115], v[60:63]\n\t"
    "ds_read_b64_tr_b16 v[18:19], v1 offset:22528\n\t"
    "v_pk_max_i16 v176, v176, 0\n\t"
    "v_pk_max_i16 v177, v177, 0\n\t"
    "v_pk_max_i16 v178, v178, 0\n\t"
    "v_pk_max_i16 v179, v179, 0\n\t"
    "s_waitcnt lgkmcnt(4)\n\t"
    "v_mfma_f32_16x16x32_bf16 v[72:75], v[24:27], v[112:115], v[72:75]\n\t"
    "ds_read_b64_tr_b16 v[20:21], v2 offset:20480\n\t"
    "v_mul_f32 v184, v10, v184\n\t"
    "v_mul_f32 v185, v10, v185\n\t"
    "v_mul_f32 v186, v10, v186\n\t"
    "v_mul_f32 v187, v10, v187\n\t"
    "v_mfma_f32_16x16x32_bf16 v[104:107], v[24:27], v[116:119], v[104:107]\n\t"
    "ds_read_b64_tr_b16 v[22:23], v2 offset:22528\n\t"
    "v_mul_f32 v188, v10, v188\n\t"
    "v_mul_f32 v189, v10, v189\n\t"
    "v_mul_f32 v190, v10, v190\n\t"
    "v_mul_f32 v191, v10, v191\n\t"
    "s_waitcnt lgkmcnt(4)\n\t"
    "v_mfma_f32_16x16x32_bf16 v[108:111], v[28:31], v[116:119], v[108:111]\n\t"
    "ds_read_b64_tr_b16 v[24:25], v3 offset:20480\n\t"
    "v_cvt_pk_bf16_f32 v180, v184, v185\n\t"
    "v_cvt_pk_bf16_f32 v181, v186, v187\n\t"
    "v_cvt_pk_bf16_f32 v182, v188, v189\n\t"
    "v_cvt_pk_bf16_f32 v183, v190, v191\n\t"
    "v_mfma_f32_16x16x32_bf16 v[76:79], v[28:31], v[112:115], v[76:79]\n\t"
    "ds_read_b64_tr_b16 v[26:27], v3 offset:22528\n\t"
    "v_pk_max_i16 v180, v180, 0\n\t"
    "v_pk_max_i16 v181, v181, 0\n\t"
    "v_pk_max_i16 v182, v182, 0\n\t"
    "v_pk_max_i16 v183, v183, 0\n\t"
    "s_waitcnt lgkmcnt(4)\n\t"
    "v_mfma_f32_16x16x32_bf16 v[56:59], v[16:19], v[128:131], v[56:59]\n\t"
    "ds_read_b64_tr_b16 v[28:29], v4 offset:20480\n\t"
    "v_mfma_f32_16x16x32_bf16 v[88:91], v[16:19], v[132:135], v[88:91]\n\t"
    "ds_read_b64_tr_b16 v[30:31], v4 offset:22528\n\t"
    "s_waitcnt lgkmcnt(4)\n\t"
    "v_mfma_f32_16x16x32_bf16 v[92:95], v[20:23], v[132:135], v[92:95]\n\t"
    "ds_read_b64_tr_b16 v[16:17], v1 offset:24576\n\t"
    "v_mfma_f32_16x16x32_bf16 v[60:63], v[20:23], v[128:131], v[60:63]\n\t"
    "ds_read_b64_tr_b16 v[18:19], v1 offset:26624\n\t"
    "s_waitcnt lgkmcnt(4)\n\t"
    "v_mfma_f32_16x16x32_bf16 v[72:75], v[24:27], v[128:131], v[72:75]\n\t"
    "ds_read_b64_tr_b16 v[20:21], v2 offset:24576\n\t"
    "v_mfma_f32_16x16x32_bf16 v[104:107], v[24:27], v[132:135], v[104:107]\n\t"
    "ds_read_b64_tr_b16 v[22:23], v2 offset:26624\n\t"
    "s_waitcnt lgkmcnt(4)\n\t"
    "v_mfma_f32_16x16x32_bf16 v[108:111], v[28:31], v[132:135], v[108:111]\n\t"
    "ds_read_b64_tr_b16 v[24:25], v3 offset:24576\n\t"
    "v_mfma_f32_16x16x32_bf16 v[76:79], v[28:31], v[128:131], v[76:79]\n\t"
    "ds_read_b64_tr_b16 v[26:27], v3 offset:26624\n\t"
    "s_waitcnt lgkmcnt(4)\n\t"
    "v_mfma_f32_16x16x32_bf16 v[56:59], v[16:19], v[144:147], v[56:59]\n\t"
    "ds_read_b64_tr_b16 v[28:29], v4 offset:24576\n\t"
    "v_mfma_f32_16x16x32_bf16 v[88:91], v[16:19], v[148:151], v[88:91]\n\t"
    "ds_read_b64_tr_b16 v[30:31], v4 offset:26624\n\t"
    "s_waitcnt lgkmcnt(4)\n\t"
    "v_mfma_f32_16x16x32_bf16 v[92:95], v[20:23], v[148:151], v[92:95]\n\t"
    "ds_read_b64_tr_b16 v[16:17], v1 offset:28672\n\t"
    "v_mfma_f32_16x16x32_bf16 v[60:63], v[20:23], v[144:147], v[60:63]\n\t"
    "ds_read_b64_tr_b16 v[18:19], v1 offset:30720\n\t"
    "s_waitcnt lgkmcnt(4)\n\t"
    "v_mfma_f32_16x16x32_bf16 v[72:75], v[24:27], v[144:147], v[72:75]\n\t"
    "ds_read_b64_tr_b16 v[20:21], v2 offset:28672\n\t"
    "v_mfma_f32_16x16x32_bf16 v[104:107], v[24:27], v[148:151], v[104:107]\n\t"
    "ds_read_b64_tr_b16 v[22:23], v2 offset:30720\n\t"
    "s_waitcnt lgkmcnt(4)\n\t"
    "v_mfma_f32_16x16x32_bf16 v[108:111], v[28:31], v[148:151], v[108:111]\n\t"
    "ds_read_b64_tr_b16 v[24:25], v3 offset:28672\n\t"
    "v_mfma_f32_16x16x32_bf16 v[76:79], v[28:31], v[144:147], v[76:79]\n\t"
    "ds_read_b64_tr_b16 v[26:27], v3 offset:30720\n\t"
    "s_waitcnt lgkmcnt(4)\n\t"
    "v_mfma_f32_16x16x32_bf16 v[56:59], v[16:19], v[160:163], v[56:59]\n\t"
    "ds_read_b64_tr_b16 v[28:29], v4 offset:28672\n\t"
    "v_mfma_f32_16x16x32_bf16 v[88:91], v[16:19], v[164:167], v[88:91]\n\t"
    "ds_read_b64_tr_b16 v[30:31], v4 offset:30720\n\t"
    "s_waitcnt lgkmcnt(4)\n\t"
    "v_mfma_f32_16x16x32_bf16 v[92:95], v[20:23], v[164:167], v[92:95]\n\t"
    "ds_read_b64_tr_b16 v[16:17], v1 offset:32768\n\t"
    "v_mfma_f32_16x16x32_bf16 v[60:63], v[20:23], v[160:163], v[60:63]\n\t"
    "ds_read_b64_tr_b16 v[18:19], v1 offset:34816\n\t"
    "s_waitcnt lgkmcnt(4)\n\t"
    "v_mfma_f32_16x16x32_bf16 v[72:75], v[24:27], v[160:163], v[72:75]\n\t"
    "ds_read_b64_tr_b16 v[20:21], v2 offset:32768\n\t"
    "v_mfma_f32_16x16x32_bf16 v[104:107], v[24:27], v[164:167], v[104:107]\n\t"
    "ds_read_b64_tr_b16 v[22:23], v2 offset:34816\n\t"
    "s_waitcnt lgkmcnt(4)\n\t"
    "v_mfma_f32_16x16x32_bf16 v[108:111], v[28:31], v[164:167], v[108:111]\n\t"
    "ds_read_b64_tr_b16 v[24:25], v3 offset:32768\n\t"
    "v_mfma_f32_16x16x32_bf16 v[76:79], v[28:31], v[160:163], v[76:79]\n\t"
    "ds_read_b64_tr_b16 v[26:27], v3 offset:34816\n\t"
    "s_waitcnt lgkmcnt(4)\n\t"
    "v_mfma_f32_16x16x32_bf16 v[56:59], v[16:19], v[176:179], v[56:59]\n\t"
    "ds_read_b64_tr_b16 v[28:29], v4 offset:32768\n\t"
    "v_mfma_f32_16x16x32_bf16 v[88:91], v[16:19], v[180:183], v[88:91]\n\t"
    "ds_read_b64_tr_b16 v[30:31], v4 offset:34816\n\t"
    "s_waitcnt lgkmcnt(4)\n\t"
    "v_mfma_f32_16x16x32_bf16 v[92:95], v[20:23], v[180:183], v[92:95]\n\t"
    "v_mfma_f32_16x16x32_bf16 v[60:63], v[20:23], v[176:179], v[60:63]\n\t"
    "s_waitcnt lgkmcnt(0)\n\t"
    "s_barrier\n\t"
    "v_add_u32 v1, s68, v1\n\t"
    "v_add_u32 v2, s68, v2\n\t"
    "v_add_u32 v3, s68, v3\n\t"
    "v_add_u32 v4, s68, v4\n\t"
    "v_mfma_f32_16x16x32_bf16 v[72:75], v[24:27], v[176:179], v[72:75]\n\t"
    "ds_read_b128 v[16:19], v5 offset:0\n\t"
    "v_mfma_f32_16x16x32_bf16 v[104:107], v[24:27], v[180:183], v[104:107]\n\t"
    "ds_read_b128 v[20:23], v5 offset:1024\n\t"
    "v_mfma_f32_16x16x32_bf16 v[108:111], v[28:31], v[180:183], v[108:111]\n\t"
    "v_mfma_f32_16x16x32_bf16 v[76:79], v[28:31], v[176:179], v[76:79]\n\t"
    "s_waitcnt lgkmcnt(1)\n\t"
    "v_mfma_f32_16x16x32_bf16 v[56:59], v[16:19], v[192:195], v[56:59]\n\t"
    "ds_read_b128 v[24:27], v5 offset:2048\n\t"
    "v_mfma_f32_16x16x32_bf16 v[88:91], v[16:19], v[196:199], v[88:91]\n\t"
    "ds_read_b128 v[28:31], v5 offset:3072\n\t"
    "s_waitcnt lgkmcnt(2)\n\t"
    "v_mfma_f32_16x16x32_bf16 v[92:95], v[20:23], v[196:199], v[92:95]\n\t"
    "ds_read_b128 v[16:19], v5 offset:4096\n\t"
    "v_mfma_f32_16x16x32_bf16 v[60:63], v[20:23], v[192:195], v[60:63]\n\t"
    "s_waitcnt lgkmcnt(2)\n\t"
    "v_mfma_f32_16x16x32_bf16 v[72:75], v[24:27], v[192:195], v[72:75]\n\t"
    "ds_read_b128 v[20:23], v5 offset:5120\n\t"
    "v_mfma_f32_16x16x32_bf16 v[104:107], v[24:27], v[196:199], v[104:107]\n\t"
    "s_waitcnt lgkmcnt(2)\n\t"
    "v_mfma_f32_16x16x32_bf16 v[108:111], v[28:31], v[196:199], v[108:111]\n\t"
    "ds_read_b128 v[24:27], v5 offset:6144\n\t"
    "v_mfma_f32_16x16x32_bf16 v[76:79], v[28:31], v[192:195], v[76:79]\n\t"
    "s_waitcnt lgkmcnt(2)\n\t"
    "v_mfma_f32_16x16x32_bf16 v[56:59], v[16:19], v[200:203], v[56:59]\n\t"
    "ds_read_b128 v[28:31], v5 offset:7168\n\t"
    "v_mfma_f32_16x16x32_bf16 v[88:91], v[16:19], v[204:207], v[88:91]\n\t"
    "s_waitcnt lgkmcnt(2)\n\t"
    "v_mfma_f32_16x16x32_bf16 v[92:95], v[20:23], v[204:207], v[92:95]\n\t"
    "ds_read_b128 v[16:19], v5 offset:8192\n\t"
    "v_mfma_f32_16x16x32_bf16 v[60:63], v[20:23], v[200:203], v[60:63]\n\t"
    "s_waitcnt lgkmcnt(2)\n\t"
    "v_mfma_f32_16x16x32_bf16 v[72:75], v[24:27], v[200:203], v[72:75]\n\t"
    "ds_read_b128 v[20:23], v5 offset:9216\n\t"
    "v_mfma_f32_16x16x32_bf16 v[104:107], v[24:27], v[204:207], v[104:107]\n\t"
    "s_waitcnt lgkmcnt(2)\n\t"
    "v_mfma_f32_16x16x32_bf16 v[108:111], v[28:31], v[204:207], v[108:111]\n\t"
    "ds_read_b128 v[24:27], v5 offset:10240\n\t"
    "v_mfma_f32_16x16x32_bf16 v[76:79], v[28:31], v[200:203], v[76:79]\n\t"
    "s_waitcnt lgkmcnt(2)\n\t"
    "v_mfma_f32_16x16x32_bf16 v[56:59], v[16:19], v[208:211], v[56:59]\n\t"
    "ds_read_b128 v[28:31], v5 offset:11264\n\t"
    "v_mfma_f32_16x16x32_bf16 v[88:91], v[16:19], v[212:215], v[88:91]\n\t"
    "s_waitcnt lgkmcnt(2)\n\t"
    "v_mfma_f32_16x16x32_bf16 v[92:95], v[20:23], v[212:215], v[92:95]\n\t"
    "ds_read_b128 v[16:19], v5 offset:12288\n\t"
    "v_mfma_f32_16x16x32_bf16 v[60:63], v[20:23], v[208:211], v[60:63]\n\t"
    "s_waitcnt lgkmcnt(2)\n\t"
    "v_mfma_f32_16x16x32_bf16 v[72:75], v[24:27], v[208:211], v[72:75]\n\t"
    "ds_read_b128 v[20:23], v5 offset:13312\n\t"
    "v_mfma_f32_16x16x32_bf16 v[104:107], v[24:27], v[212:215], v[104:107]\n\t"
    "s_waitcnt lgkmcnt(2)\n\t"
    "v_mfma_f32_16x16x32_bf16 v[108:111], v[28:31], v[212:215], v[108:111]\n\t"
    "ds_read_b128 v[24:27], v5 offset:14336\n\t"
    "v_mfma_f32_16x16x32_bf16 v[76:79], v[28:31], v[208:211], v[76:79]\n\t"
    "s_waitcnt lgkmcnt(2)\n\t"
    "v_mfma_f32_16x16x32_bf16 v[56:59], v[16:19], v[216:219], v[56:59]\n\t"
    "ds_read_b128 v[28:31], v5 offset:15360\n\t"
    "v_mfma_f32_16x16x32_bf16 v[88:91], v[16:19], v[220:223], v[88:91]\n\t"
    "s_waitcnt lgkmcnt(2)\n\t"
    "v_mfma_f32_16x16x32_bf16 v[92:95], v[20:23], v[220:223], v[92:95]\n\t"
    "ds_read_b128 v[16:19], v5 offset:16384\n\t"
    "v_mfma_f32_16x16x32_bf16 v[60:63], v[20:23], v[216:219], v[60:63]\n\t"
    "s_waitcnt lgkmcnt(2)\n\t"
    "v_mfma_f32_16x16x32_bf16 v[72:75], v[24:27], v[216:219], v[72:75]\n\t"
    "ds_read_b128 v[20:23], v5 offset:17408\n\t"
    "v_mfma_f32_16x16x32_bf16 v[104:107], v[24:27], v[220:223], v[104:107]\n\t"
    "s_waitcnt lgkmcnt(2)\n\t"
    "v_mfma_f32_16x16x32_bf16 v[108:111], v[28:31], v[220:223], v[108:111]\n\t"
    "ds_read_b128 v[24:27], v5 offset:18432\n\t"
    "v_mfma_f32_16x16x32_bf16 v[76:79], v[28:31], v[216:219], v[76:79]\n\t"
    "s_waitcnt lgkmcnt(2)\n\t"
    "v_mfma_f32_16x16x32_bf16 v[56:59], v[16:19], v[224:227], v[56:59]\n\t"
    "ds_read_b128 v[28:31], v5 offset:19456\n\t"
    "v_mfma_f32_16x16x32_bf16 v[88:91], v[16:19], v[228:231], v[88:91]\n\t"
    "s_waitcnt lgkmcnt(2)\n\t"
    "v_mfma_f32_16x16x32_bf16 v[92:95], v[20:23], v[228:231], v[92:95]\n\t"
    "ds_read_b128 v[16:19], v5 offset:20480\n\t"
    "v_mfma_f32_16x16x32_bf16 v[60:63], v[20:23], v[224:227], v[60:63]\n\t"
    "s_waitcnt lgkmcnt(2)\n\t"
    "v_mfma_f32_16x16x32_bf16 v[72:75], v[24:27], v[224:227], v[72:75]\n\t"
    "ds_read_b128 v[20:23], v5 offset:21504\n\t"
    "v_mfma_f32_16x16x32_bf16 v[104:107], v[24:27], v[228:231], v[104:107]\n\t"
    "s_waitcnt lgkmcnt(2)\n\t"
    "v_mfma_f32_16x16x32_bf16 v[108:111], v[28:31], v[228:231], v[108:111]\n\t"
    "ds_read_b128 v[24:27], v5 offset:22528\n\t"
    "v_mfma_f32_16x16x32_bf16 v[76:79], v[28:31], v[224:227], v[76:79]\n\t"
    "s_waitcnt lgkmcnt(2)\n\t"
    "v_mfma_f32_16x16x32_bf16 v[56:59], v[16:19], v[232:235], v[56:59]\n\t"
    "ds_read_b128 v[28:31], v5 offset:23552\n\t"
    "v_mfma_f32_16x16x32_bf16 v[88:91], v[16:19], v[236:239], v[88:91]\n\t"
    "s_waitcnt lgkmcnt(2)\n\t"
    "v_mfma_f32_16x16x32_bf16 v[92:95], v[20:23], v[236:239], v[92:95]\n\t"
    "ds_read_b128 v[16:19], v5 offset:24576\n\t"
    "v_mfma_f32_16x16x32_bf16 v[60:63], v[20:23], v[232:235], v[60:63]\n\t"
    "s_waitcnt lgkmcnt(2)\n\t"
    "v_mfma_f32_16x16x32_bf16 v[72:75], v[24:27], v[232:235], v[72:75]\n\t"
    "ds_read_b128 v[20:23], v5 offset:25600\n\t"
    "v_mfma_f32_16x16x32_bf16 v[104:107], v[24:27], v[236:239], v[104:107]\n\t"
    "s_waitcnt lgkmcnt(2)\n\t"
    "v_mfma_f32_16x16x32_bf16 v[108:111], v[28:31], v[236:239], v[108:111]\n\t"
    "ds_read_b128 v[24:27], v5 offset:26624\n\t"
    "v_mfma_f32_16x16x32_bf16 v[76:79], v[28:31], v[232:235], v[76:79]\n\t"
    "s_waitcnt lgkmcnt(2)\n\t"
    "v_mfma_f32_16x16x32_bf16 v[56:59], v[16:19], v[240:243], v[56:59]\n\t"
    "ds_read_b128 v[28:31], v5 offset:27648\n\t"
    "v_mfma_f32_16x16x32_bf16 v[88:91], v[16:19], v[244:247], v[88:91]\n\t"
    "s_waitcnt lgkmcnt(2)\n\t"
    "v_mfma_f32_16x16x32_bf16 v[92:95], v[20:23], v[244:247], v[92:95]\n\t"
    "ds_read_b128 v[16:19], v5 offset:28672\n\t"
    "v_mfma_f32_16x16x32_bf16 v[60:63], v[20:23], v[240:243], v[60:63]\n\t"
    "s_waitcnt lgkmcnt(2)\n\t"
    "v_mfma_f32_16x16x32_bf16 v[72:75], v[24:27], v[240:243], v[72:75]\n\t"
    "ds_read_b128 v[20:23], v5 offset:29696\n\t"
    "v_mfma_f32_16x16x32_bf16 v[104:107], v[24:27], v[244:247], v[104:107]\n\t"
    "s_waitcnt lgkmcnt(2)\n\t"
    "v_mfma_f32_16x16x32_bf16 v[108:111], v[28:31], v[244:247], v[108:111]\n\t"
    "ds_read_b128 v[24:27], v5 offset:30720\n\t"
    "v_mfma_f32_16x16x32_bf16 v[76:79], v[28:31], v[240:243], v[76:79]\n\t"
    "s_waitcnt lgkmcnt(2)\n\t"
    "v_mfma_f32_16x16x32_bf16 v[56:59], v[16:19], v[248:251], v[56:59]\n\t"
    "ds_read_b128 v[28:31], v5 offset:31744\n\t"
    "v_mfma_f32_16x16x32_bf16 v[88:91], v[16:19], v[252:255], v[88:91]\n\t"
    "s_waitcnt lgkmcnt(2)\n\t"
    "v_mfma_f32_16x16x32_bf16 v[92:95], v[20:23], v[252:255], v[92:95]\n\t"
    "v_mfma_f32_16x16x32_bf16 v[60:63], v[20:23], v[248:251], v[60:63]\n\t"
    "s_waitcnt lgkmcnt(0)\n\t"
    "s_barrier\n\t"
    "v_add_u32 v5, s69, v5\n\t"
    "s_sub_u32 s68, 0, s68\n\t"
    "s_sub_u32 s69, 0, s69\n\t"
    "v_mfma_f32_16x16x32_bf16 v[72:75], v[24:27], v[248:251], v[72:75]\n\t"
    "ds_read_b64_tr_b16 v[16:17], v1 offset:0\n\t"
    "ds_read_b64_tr_b16 v[18:19], v1 offset:2048\n\t"
    "v_mfma_f32_16x16x32_bf16 v[104:107], v[24:27], v[252:255], v[104:107]\n\t"
    "ds_read_b64_tr_b16 v[20:21], v2 offset:0\n\t"
    "ds_read_b64_tr_b16 v[22:23], v2 offset:2048\n\t"
    "v_mfma_f32_16x16x32_bf16 v[108:111], v[28:31], v[252:255], v[108:111]\n\t"
    "v_mfma_f32_16x16x32_bf16 v[76:79], v[28:31], v[248:251], v[76:79]\n\t"
    "LC_%=:\n\t"
    "s_waitcnt lgkmcnt(2)\n\t"
    "v_mfma_f32_16x16x32_bf16 v[120:123], v[16:19], v[48:51], 0\n\t"
    "ds_read_b64_tr_b16 v[24:25], v3 offset:0\n\t"
    "ds_read_b64_tr_b16 v[26:27], v3 offset:2048\n\t"
    "v_mfma_f32_16x16x32_bf16 v[152:155], v[16:19], v[52:55], 0\n\t"
    "ds_read_b64_tr_b16 v[28:29], v4 offset:0\n\t"
    "ds_read_b64_tr_b16 v[30:31], v4 offset:2048\n\t"
    "s_waitcnt lgkmcnt(4)\n\t"
    "v_mfma_f32_16x16x32_bf16 v[156:159], v[20:23], v[52:55], 0\n\t"
    "ds_read_b64_tr_b16 v[16:17], v1 offset:4096\n\t"
    "v_mfma_f32_16x16x32_bf16 v[124:127], v[20:23], v[48:51], 0\n\t"
    "ds_read_b64_tr_b16 v[18:19], v1 offset:6144\n\t"
    "s_waitcnt lgkmcnt(4)\n\t"
    "v_mfma_f32_16x16x32_bf16 v[136:139], v[24:27], v[48:51], 0\n\t"
    "ds_read_b64_tr_b16 v[20:21], v2 offset:4096\n\t"
    "v_mfma_f32_16x16x32_bf16 v[168:171], v[24:27], v[52:55], 0\n\t"
    "ds_read_b64_tr_b16 v[22:23], v2 offset:6144\n\t"
    "s_waitcnt lgkmcnt(4)\n\t"
    "v_mfma_f32_16x16x32_bf16 v[172:175], v[28:31], v[52:55], 0\n\t"
    "ds_read_b64_tr_b16 v[24:25], v3 offset:4096\n\t"
    "v_mfma_f32_16x16x32_bf16 v[140:143], v[28:31], v[48:51], 0\n\t"
    "ds_read_b64_tr_b16 v[26:27], v3 offset:6144\n\t"
    "s_waitcnt lgkmcnt(4)\n\t"
    "v_mfma_f32_16x16x32_bf16 v[120:123], v[16:19], v[64:67], v[120:123]\n\t"
    "ds_read_b64_tr_b16 v[28:29], v4 offset:4096\n\t"
    "v_cvt_pk_bf16_f32 v32, v56, v57\n\t"
    "v_mfma_f32_16x16x32_bf16 v[152:155], v[16:19], v[68:71], v[152:155]\n\t"
    "ds_read_b64_tr_b16 v[30:31], v4 offset:6144\n\t"
    "s_waitcnt lgkmcnt(4)\n\t"
    "v_mfma_f32_16x16x32_bf16 v[156:159], v[20:23], v[68:71], v[156:159]\n\t"
    "ds_read_b64_tr_b16 v[16:17], v1 offset:8192\n\t"
    "v_cvt_pk_bf16_f32 v33, v58, v59\n\t"
    "v_mfma_f32_16x16x32_bf16 v[124:127], v[20:23], v[64:67], v[124:127]\n\t"
    "ds_read_b64_tr_b16 v[18:19], v1 offset:10240\n\t"
    "s_waitcnt lgkmcnt(4)\n\t"
    "v_mfma_f32_16x16x32_bf16 v[136:139], v[24:27], v[64:67], v[136:139]\n\t"
    "ds_read_b64_tr_b16 v[20:21], v2 offset:8192\n\t"
    "v_cvt_pk_bf16_f32 v34, v60, v61\n\t"
    "v_mfma_f32_16x16x32_bf16 v[168:171], v[24:27], v[68:71], v[168:171]\n\t"
    "ds_read_b64_tr_b16 v[22:23], v2 offset:10240\n\t"
    "s_waitcnt lgkmcnt(4)\n\t"
    "v_mfma_f32_16x16x32_bf16 v[172:175], v[28:31], v[68:71], v[172:175]\n\t"
    "ds_read_b64_tr_b16 v[24:25], v3 offset:8192\n\t"
    "v_cvt_pk_bf16_f32 v35, v62, v63\n\t"
    "v_mfma_f32_16x16x32_bf16 v[140:143], v[28:31], v[64:67], v[140:143]\n\t"
    "ds_read_b64_tr_b16 v[26:27], v3 offset:10240\n\t"
    "s_waitcnt lgkmcnt(4)\n\t"
    "v_mfma_f32_16x16x32_bf16 v[120:123], v[16:19], v[80:83], v[120:123]\n\t"
    "ds_read_b64_tr_b16 v[28:29], v4 offset:8192\n\t"
    "buffer_store_dwordx4 v[32:35], v0, s[36:39], s72 offen offset:0 sc1\n\t"
    "v_mfma_f32_16x16x32_bf16 v[152:155], v[16:19], v[84:87], v[152:155]\n\t"
    "ds_read_b64_tr_b16 v[30:31], v4 offset:10240\n\t"
    "s_waitcnt lgkmcnt(4)\n\t"
    "v_mfma_f32_16x16x32_bf16 v[156:159], v[20:23], v[84:87], v[156:159]\n\t"
    "ds_read_b64_tr_b16 v[16:17], v1 offset:12288\n\t"
    "v_cvt_pk_bf16_f32 v36, v72, v73\n\t"
    "v_mfma_f32_16x16x32_bf16 v[124:127], v[20:23], v[80:83], v[124:127]\n\t"
    "ds_read_b64_tr_b16 v[18:19], v1 offset:14336\n\t"
    "s_waitcnt lgkmcnt(4)\n\t"
    "v_mfma_f32_16x16x32_bf16 v[136:139], v[24:27], v[80:83], v[136:139]\n\t"
    "ds_read_b64_tr_b16 v[20:21], v2 offset:12288\n\t"
    "v_cvt_pk_bf16_f32 v37, v74, v75\n\t"
    "v_mfma_f32_16x16x32_bf16 v[168:171], v[24:27], v[84:87], v[168:171]\n\t"
    "ds_read_b64_tr_b16 v[22:23], v2 offset:14336\n\t"
    "s_waitcnt lgkmcnt(4)\n\t"
    "v_mfma_f32_16x16x32_bf16 v[172:175], v[28:31], v[84:87], v[172:175]\n\t"
    "ds_read_b64_tr_b16 v[24:25], v3 offset:12288\n\t"
    "v_cvt_pk_bf16_f32 v38, v76, v77\n\t"
    "v_mfma_f32_16x16x32_bf16 v[140:143], v[28:31], v[80:83], v[140:143]\n\t"
    "ds_read_b64_tr_b16 v[26:27], v3 offset:14336\n\t"
    "s_waitcnt lgkmcnt(4)\n\t"
    "v_mfma_f32_16x16x32_bf16 v[120:123], v[16:19], v[96:99], v[120:123]\n\t"
    "ds_read_b64_tr_b16 v[28:29], v4 offset:12288\n\t"
    "v_cvt_pk_bf16_f32 v39, v78, v79\n\t"
    "v_mfma_f32_16x16x32_bf16 v[152:155], v[16:19], v[100:103], v[152:155]\n\t"
    "ds_read_b64_tr_b16 v[30:31], v4 offset:14336\n\t"
    "s_waitcnt lgkmcnt(4)\n\t"
    "v_mfma_f32_16x16x32_bf16 v[156:159], v[20:23], v[100:103], v[156:159]\n\t"
    "ds_read_b64_tr_b16 v[16:17], v1 offset:16384\n\t"
    "buffer_store_dwordx4 v[36:39], v0, s[36:39], s72 offen offset:1024 sc1\n\t"
    "v_mfma_f32_16x16x32_bf16 v[124:127], v[20:23], v[96:99], v[124:127]\n\t"
    "ds_read_b64_tr_b16 v[18:19], v1 offset:18432\n\t"
    "s_waitcnt lgkmcnt(4)\n\t"
    "v_mfma_f32_16x16x32_bf16 v[136:139], v[24:27], v[96:99], v[136:139]\n\t"
    "ds_read_b64_tr_b16 v[20:21], v2 offset:16384\n\t"
    "v_cvt_pk_bf16_f32 v40, v88, v89\n\t"
    "v_mfma_f32_16x16x32_bf16 v[168:171], v[24:27], v[100:103], v[168:171]\n\t"
    "ds_read_b64_tr_b16 v[22:23], v2 offset:18432\n\t"
    "s_waitcnt lgkmcnt(4)\n\t"
    "v_mfma_f32_16x16x32_bf16 v[172:175], v[28:31], v[100:103], v[172:175]\n\t"
    "ds_read_b64_tr_b16 v[24:25], v3 offset:16384\n\t"
    "v_cvt_pk_bf16_f32 v41, v90, v91\n\t"
    "v_mfma_f32_16x16x32_bf16 v[140:143], v[28:31], v[96:99], v[140:143]\n\t"
    "ds_read_b64_tr_b16 v[26:27], v3 offset:18432\n\t"
    "s_waitcnt lgkmcnt(4)\n\t"
    "v_mfma_f32_16x16x32_bf16 v[120:123], v[16:19], v[112:115], v[120:123]\n\t"
    "ds_read_b64_tr_b16 v[28:29], v4 offset:16384\n\t"
    "v_cvt_pk_bf16_f32 v42, v92, v93\n\t"
    "v_mfma_f32_16x16x32_bf16 v[152:155], v[16:19], v[116:119], v[152:155]\n\t"
    "ds_read_b64_tr_b16 v[30:31], v4 offset:18432\n\t"
    "s_waitcnt lgkmcnt(4)\n\t"
    "v_mfma_f32_16x16x32_bf16 v[156:159], v[20:23], v[116:119], v[156:159]\n\t"
    "ds_read_b64_tr_b16 v[16:17], v1 offset:20480\n\t"
    "v_cvt_pk_bf16_f32 v43, v94, v95\n\t"
    "v_mfma_f32_16x16x32_bf16 v[124:127], v[20:23], v[112:115], v[124:127]\n\t"
    "ds_read_b64_tr_b16 v[18:19], v1 offset:22528\n\t"
    "s_waitcnt lgkmcnt(4)\n\t"
    "v_mfma_f32_16x16x32_bf16 v[136:139], v[24:27], v[112:115], v[136:139]\n\t"
    "ds_read_b64_tr_b16 v[20:21], v2 offset:20480\n\t"
    "buffer_store_dwordx4 v[40:43], v0, s[36:39], s72 offen offset:2048 sc1\n\t"
    "v_mfma_f32_16x16x32_bf16 v[168:171], v[24:27], v[116:119], v[168:171]\n\t"
    "ds_read_b64_tr_b16 v[22:23], v2 offset:22528\n\t"
    "s_waitcnt lgkmcnt(4)\n\t"
    "v_mfma_f32_16x16x32_bf16 v[172:175], v[28:31], v[116:119], v[172:175]\n\t"
    "ds_read_b64_tr_b16 v[24:25], v3 offset:20480\n\t"
    "v_cvt_pk_bf16_f32 v44, v104, v105\n\t"
    "v_mfma_f32_16x16x32_bf16 v[140:143], v[28:31], v[112:115], v[140:143]\n\t"
    "ds_read_b64_tr_b16 v[26:27], v3 offset:22528\n\t"
    "s_waitcnt lgkmcnt(4)\n\t"
    "v_mfma_f32_16x16x32_bf16 v[120:123], v[16:19], v[128:131], v[120:123]\n\t"
    "ds_read_b64_tr_b16 v[28:29], v4 offset:20480\n\t"
    "v_cvt_pk_bf16_f32 v45, v106, v107\n\t"
    "v_mfma_f32_16x16x32_bf16 v[152:155], v[16:19], v[132:135], v[152:155]\n\t"
    "ds_read_b64_tr_b16 v[30:31], v4 offset:22528\n\t"
    "s_waitcnt lgkmcnt(4)\n\t"
    "v_mfma_f32_16x16x32_bf16 v[156:159], v[20:23], v[132:135], v[156:159]\n\t"
    "ds_read_b64_tr_b16 v[16:17], v1 offset:24576\n\t"
    "v_cvt_pk_bf16_f32 v46, v108, v109\n\t"
    "v_mfma_f32_16x16x32_bf16 v[124:127], v[20:23], v[128:131], v[124:127]\n\t"
    "ds_read_b64_tr_b16 v[18:19], v1 offset:26624\n\t"
    "s_waitcnt lgkmcnt(4)\n\t"
    "v_mfma_f32_16x16x32_bf16 v[136:139], v[24:27], v[128:131], v[136:139]\n\t"
    "ds_read_b64_tr_b16 v[20:21], v2 offset:24576\n\t"
    "v_cvt_pk_bf16_f32 v47, v110, v111\n\t"
    "v_mfma_f32_16x16x32_bf16 v[168:171], v[24:27], v[132:135], v[168:171]\n\t"
    "ds_read_b64_tr_b16 v[22:23], v2 offset:26624\n\t"
    "s_waitcnt lgkmcnt(4)\n\t"
    "v_mfma_f32_16x16x32_bf16 v[172:175], v[28:31], v[132:135], v[172:175]\n\t"
    "ds_read_b64_tr_b16 v[24:25], v3 offset:24576\n\t"
    "buffer_store_dwordx4 v[44:47], v0, s[36:39], s72 offen offset:3072 sc1\n\t"
    "v_mfma_f32_16x16x32_bf16 v[140:143], v[28:31], v[128:131], v[140:143]\n\t"
    "ds_read_b64_tr_b16 v[26:27], v3 offset:26624\n\t"
    "s_waitcnt lgkmcnt(4)\n\t"
    "v_mfma_f32_16x16x32_bf16 v[120:123], v[16:19], v[144:147], v[120:123]\n\t"
    "ds_read_b64_tr_b16 v[28:29], v4 offset:24576\n\t"
    "s_add_u32 s75, s75, 1\n\t"
    "s_cmp_eq_u32 s75, 9\n\t"
    "s_cselect_b32 s75, 0, s75\n\t"
    "v_mfma_f32_16x16x32_bf16 v[152:155], v[16:19], v[148:151], v[152:155]\n\t"
    "ds_read_b64_tr_b16 v[30:31], v4 offset:26624\n\t"
    "s_waitcnt lgkmcnt(4)\n\t"
    "v_mfma_f32_16x16x32_bf16 v[156:159], v[20:23], v[148:151], v[156:159]\n\t"
    "ds_read_b64_tr_b16 v[16:17], v1 offset:28672\n\t"
    "s_lshl_b32 s72, s75, 18\n\t"
    "s_add_u32 s72, s72, s74\n\t"
    "v_mfma_f32_16x16x32_bf16 v[124:127], v[20:23], v[144:147], v[124:127]\n\t"
    "ds_read_b64_tr_b16 v[18:19], v1 offset:30720\n\t"
    "s_waitcnt lgkmcnt(4)\n\t"
    "v_mfma_f32_16x16x32_bf16 v[136:139], v[24:27], v[144:147], v[136:139]\n\t"
    "ds_read_b64_tr_b16 v[20:21], v2 offset:28672\n\t"
    "v_mfma_f32_16x16x32_bf16 v[168:171], v[24:27], v[148:151], v[168:171]\n\t"
    "ds_read_b64_tr_b16 v[22:23], v2 offset:30720\n\t"
    "s_waitcnt lgkmcnt(4)\n\t"
    "v_mfma_f32_16x16x32_bf16 v[172:175], v[28:31], v[148:151], v[172:175]\n\t"
    "ds_read_b64_tr_b16 v[24:25], v3 offset:28672\n\t"
    "v_mfma_f32_16x16x32_bf16 v[140:143], v[28:31], v[144:147], v[140:143]\n\t"
    "ds_read_b64_tr_b16 v[26:27], v3 offset:30720\n\t"
    "s_waitcnt lgkmcnt(4)\n\t"
    "v_mfma_f32_16x16x32_bf16 v[120:123], v[16:19], v[160:163], v[120:123]\n\t"
    "ds_read_b64_tr_b16 v[28:29], v4 offset:28672\n\t"
    "v_mfma_f32_16x16x32_bf16 v[152:155], v[16:19], v[164:167], v[152:155]\n\t"
    "ds_read_b64_tr_b16 v[30:31], v4 offset:30720\n\t"
    "s_waitcnt lgkmcnt(4)\n\t"
    "v_mfma_f32_16x16x32_bf16 v[156:159], v[20:23], v[164:167], v[156:159]\n\t"
    "ds_read_b64_tr_b16 v[16:17], v1 offset:32768\n\t"
    "v_mfma_f32_16x16x32_bf16 v[124:127], v[20:23], v[160:163], v[124:127]\n\t"
    "ds_read_b64_tr_b16 v[18:19], v1 offset:34816\n\t"
    "s_waitcnt lgkmcnt(4)\n\t"
    "v_mfma_f32_16x16x32_bf16 v[136:139], v[24:27], v[160:163], v[136:139]\n\t"
    "ds_read_b64_tr_b16 v[20:21], v2 offset:32768\n\t"
    "v_mfma_f32_16x16x32_bf16 v[168:171], v[24:27], v[164:167], v[168:171]\n\t"
    "ds_read_b64_tr_b16 v[22:23], v2 offset:34816\n\t"
    "s_waitcnt lgkmcnt(4)\n\t"
    "v_mfma_f32_16x16x32_bf16 v[172:175], v[28:31], v[164:167], v[172:175]\n\t"
    "ds_read_b64_tr_b16 v[24:25], v3 offset:32768\n\t"
    "v_mfma_f32_16x16x32_bf16 v[140:143], v[28:31], v[160:163], v[140:143]\n\t"
    "ds_read_b64_tr_b16 v[26:27], v3 offset:34816\n\t"
    "s_waitcnt lgkmcnt(4)\n\t"
    "v_mfma_f32_16x16x32_bf16 v[120:123], v[16:19], v[176:179], v[120:123]\n\t"
    "ds_read_b64_tr_b16 v[28:29], v4 offset:32768\n\t"
    "v_mfma_f32_16x16x32_bf16 v[152:155], v[16:19], v[180:183], v[152:155]\n\t"
    "ds_read_b64_tr_b16 v[30:31], v4 offset:34816\n\t"
    "s_waitcnt lgkmcnt(4)\n\t"
    "v_mfma_f32_16x16x32_bf16 v[156:159], v[20:23], v[180:183], v[156:159]\n\t"
    "v_mfma_f32_16x16x32_bf16 v[124:127], v[20:23], v[176:179], v[124:127]\n\t"
    "s_waitcnt lgkmcnt(0)\n\t"
    "s_barrier\n\t"
    "v_add_u32 v1, s68, v1\n\t"
    "v_add_u32 v2, s68, v2\n\t"
    "v_add_u32 v3, s68, v3\n\t"
    "v_add_u32 v4, s68, v4\n\t"
    "v_mfma_f32_16x16x32_bf16 v[136:139], v[24:27], v[176:179], v[136:139]\n\t"
    "ds_read_b128 v[16:19], v5 offset:0\n\t"
    "v_mfma_f32_16x16x32_bf16 v[168:171], v[24:27], v[180:183], v[168:171]\n\t"
    "ds_read_b128 v[20:23], v5 offset:1024\n\t"
    "v_mfma_f32_16x16x32_bf16 v[172:175], v[28:31], v[180:183], v[172:175]\n\t"
    "v_mfma_f32_16x16x32_bf16 v[140:143], v[28:31], v[176:179], v[140:143]\n\t"
    "s_waitcnt lgkmcnt(1)\n\t"
    "v_mfma_f32_16x16x32_bf16 v[120:123], v[16:19], v[192:195], v[120:123]\n\t"
    "ds_read_b128 v[24:27], v5 offset:2048\n\t"
    "v_mfma_f32_16x16x32_bf16 v[152:155], v[16:19], v[196:199], v[152:155]\n\t"
    "ds_read_b128 v[28:31], v5 offset:3072\n\t"
    "s_waitcnt lgkmcnt(2)\n\t"
    "v_mfma_f32_16x16x32_bf16 v[156:159], v[20:23], v[196:199], v[156:159]\n\t"
    "ds_read_b128 v[16:19], v5 offset:4096\n\t"
    "v_mfma_f32_16x16x32_bf16 v[124:127], v[20:23], v[192:195], v[124:127]\n\t"
    "s_waitcnt lgkmcnt(2)\n\t"
    "v_mfma_f32_16x16x32_bf16 v[136:139], v[24:27], v[192:195], v[136:139]\n\t"
    "ds_read_b128 v[20:23], v5 offset:5120\n\t"
    "v_mfma_f32_16x16x32_bf16 v[168:171], v[24:27], v[196:199], v[168:171]\n\t"
    "s_waitcnt lgkmcnt(2)\n\t"
    "v_mfma_f32_16x16x32_bf16 v[172:175], v[28:31], v[196:199], v[172:175]\n\t"
    "ds_read_b128 v[24:27], v5 offset:6144\n\t"
    "v_mfma_f32_16x16x32_bf16 v[140:143], v[28:31], v[192:195], v[140:143]\n\t"
    "s_waitcnt lgkmcnt(2)\n\t"
    "v_mfma_f32_16x16x32_bf16 v[120:123], v[16:19], v[200:203], v[120:123]\n\t"
    "ds_read_b128 v[28:31], v5 offset:7168\n\t"
    "s_waitcnt vmcnt(0)\n\t"
    "s_mov_b64 exec, 1\n\t"
    "buffer_store_dword v6, v14, s[40:43], s85 offen sc1\n\t"
    "s_mov_b64 exec, -1\n\t"
    "s_add_u32 s85, s85, 256\n\t"
    "s_nop 0\n\t"
    "v_mfma_f32_16x16x32_bf16 v[152:155], v[16:19], v[204:207], v[152:155]\n\t"
    "s_waitcnt lgkmcnt(2)\n\t"
    "v_mfma_f32_16x16x32_bf16 v[156:159], v[20:23], v[204:207], v[156:159]\n\t"
    "ds_read_b128 v[16:19], v5 offset:8192\n\t"
    "v_mfma_f32_16x16x32_bf16 v[124:127], v[20:23], v[200:203], v[124:127]\n\t"
    "s_waitcnt lgkmcnt(2)\n\t"
    "v_mfma_f32_16x16x32_bf16 v[136:139], v[24:27], v[200:203], v[136:139]\n\t"
    "ds_read_b128 v[20:23], v5 offset:9216\n\t"
    "v_mfma_f32_16x16x32_bf16 v[168:171], v[24:27], v[204:207], v[168:171]\n\t"
    "s_waitcnt lgkmcnt(2)\n\t"
    "v_mfma_f32_16x16x32_bf16 v[172:175], v[28:31], v[204:207], v[172:175]\n\t"
    "ds_read_b128 v[24:27], v5 offset:10240\n\t"
    "v_mfma_f32_16x16x32_bf16 v[140:143], v[28:31], v[200:203], v[140:143]\n\t"
    "s_waitcnt lgkmcnt(2)\n\t"
    "v_mfma_f32_16x16x32_bf16 v[120:123], v[16:19], v[208:211], v[120:123]\n\t"
    "ds_read_b128 v[28:31], v5 offset:11264\n\t"
    "v_mfma_f32_16x16x32_bf16 v[152:155], v[16:19], v[212:215], v[152:155]\n\t"
    "s_waitcnt lgkmcnt(2)\n\t"
    "v_mfma_f32_16x16x32_bf16 v[156:159], v[20:23], v[212:215], v[156:159]\n\t"
    "ds_read_b128 v[16:19], v5 offset:12288\n\t"
    "v_mfma_f32_16x16x32_bf16 v[124:127], v[20:23], v[208:211], v[124:127]\n\t"
    "s_waitcnt lgkmcnt(2)\n\t"
    "v_mfma_f32_16x16x32_bf16 v[136:139], v[24:27], v[208:211], v[136:139]\n\t"
    "ds_read_b128 v[20:23], v5 offset:13312\n\t"
    "v_mfma_f32_16x16x32_bf16 v[168:171], v[24:27], v[212:215], v[168:171]\n\t"
    "s_waitcnt lgkmcnt(2)\n\t"
    "v_mfma_f32_16x16x32_bf16 v[172:175], v[28:31], v[212:215], v[172:175]\n\t"
    "ds_read_b128 v[24:27], v5 offset:14336\n\t"
    "v_mfma_f32_16x16x32_bf16 v[140:143], v[28:31], v[208:211], v[140:143]\n\t"
    "s_waitcnt lgkmcnt(2)\n\t"
    "v_mfma_f32_16x16x32_bf16 v[120:123], v[16:19], v[216:219], v[120:123]\n\t"
    "ds_read_b128 v[28:31], v5 offset:15360\n\t"
    "v_mfma_f32_16x16x32_bf16 v[152:155], v[16:19], v[220:223], v[152:155]\n\t"
    "s_waitcnt lgkmcnt(2)\n\t"
    "v_mfma_f32_16x16x32_bf16 v[156:159], v[20:23], v[220:223], v[156:159]\n\t"
    "ds_read_b128 v[16:19], v5 offset:16384\n\t"
    "v_mfma_f32_16x16x32_bf16 v[124:127], v[20:23], v[216:219], v[124:127]\n\t"
    "s_waitcnt lgkmcnt(2)\n\t"
    "v_mfma_f32_16x16x32_bf16 v[136:139], v[24:27], v[216:219], v[136:139]\n\t"
    "ds_read_b128 v[20:23], v5 offset:17408\n\t"
    "v_mfma_f32_16x16x32_bf16 v[168:171], v[24:27], v[220:223], v[168:171]\n\t"
    "s_waitcnt lgkmcnt(2)\n\t"
    "v_mfma_f32_16x16x32_bf16 v[172:175], v[28:31], v[220:223], v[172:175]\n\t"
    "ds_read_b128 v[24:27], v5 offset:18432\n\t"
    "v_mfma_f32_16x16x32_bf16 v[140:143], v[28:31], v[216:219], v[140:143]\n\t"
    "s_waitcnt lgkmcnt(2)\n\t"
    "v_mfma_f32_16x16x32_bf16 v[120:123], v[16:19], v[224:227], v[120:123]\n\t"
    "ds_read_b128 v[28:31], v5 offset:19456\n\t"
    "v_mfma_f32_16x16x32_bf16 v[152:155], v[16:19], v[228:231], v[152:155]\n\t"
    "s_waitcnt lgkmcnt(2)\n\t"
    "v_mfma_f32_16x16x32_bf16 v[156:159], v[20:23], v[228:231], v[156:159]\n\t"
    "ds_read_b128 v[16:19], v5 offset:20480\n\t"
    "v_mfma_f32_16x16x32_bf16 v[124:127], v[20:23], v[224:227], v[124:127]\n\t"
    "s_waitcnt lgkmcnt(2)\n\t"
    "v_mfma_f32_16x16x32_bf16 v[136:139], v[24:27], v[224:227], v[136:139]\n\t"
    "ds_read_b128 v[20:23], v5 offset:21504\n\t"
    "v_mfma_f32_16x16x32_bf16 v[168:171], v[24:27], v[228:231], v[168:171]\n\t"
    "s_waitcnt lgkmcnt(2)\n\t"
    "v_mfma_f32_16x16x32_bf16 v[172:175], v[28:31], v[228:231], v[172:175]\n\t"
    "ds_read_b128 v[24:27], v5 offset:22528\n\t"
    "v_mfma_f32_16x16x32_bf16 v[140:143], v[28:31], v[224:227], v[140:143]\n\t"
    "s_waitcnt lgkmcnt(2)\n\t"
    "v_mfma_f32_16x16x32_bf16 v[120:123], v[16:19], v[232:235], v[120:123]\n\t"
    "ds_read_b128 v[28:31], v5 offset:23552\n\t"
    "v_mfma_f32_16x16x32_bf16 v[152:155], v[16:19], v[236:239], v[152:155]\n\t"
    "s_waitcnt lgkmcnt(2)\n\t"
    "v_mfma_f32_16x16x32_bf16 v[156:159], v[20:23], v[236:239], v[156:159]\n\t"
    "ds_read_b128 v[16:19], v5 offset:24576\n\t"
    "v_mfma_f32_16x16x32_bf16 v[124:127], v[20:23], v[232:235], v[124:127]\n\t"
    "s_waitcnt lgkmcnt(2)\n\t"
    "v_mfma_f32_16x16x32_bf16 v[136:139], v[24:27], v[232:235], v[136:139]\n\t"
    "ds_read_b128 v[20:23], v5 offset:25600\n\t"
    "v_mfma_f32_16x16x32_bf16 v[168:171], v[24:27], v[236:239], v[168:171]\n\t"
    "s_waitcnt lgkmcnt(2)\n\t"
    "v_mfma_f32_16x16x32_bf16 v[172:175], v[28:31], v[236:239], v[172:175]\n\t"
    "ds_read_b128 v[24:27], v5 offset:26624\n\t"
    "v_mfma_f32_16x16x32_bf16 v[140:143], v[28:31], v[232:235], v[140:143]\n\t"
    "s_waitcnt lgkmcnt(2)\n\t"
    "v_mfma_f32_16x16x32_bf16 v[120:123], v[16:19], v[240:243], v[120:123]\n\t"
    "ds_read_b128 v[28:31], v5 offset:27648\n\t"
    "v_mfma_f32_16x16x32_bf16 v[152:155], v[16:19], v[244:247], v[152:155]\n\t"
    "s_waitcnt lgkmcnt(2)\n\t"
    "v_mfma_f32_16x16x32_bf16 v[156:159], v[20:23], v[244:247], v[156:159]\n\t"
    "ds_read_b128 v[16:19], v5 offset:28672\n\t"
    "v_mfma_f32_16x16x32_bf16 v[124:127], v[20:23], v[240:243], v[124:127]\n\t"
    "s_waitcnt lgkmcnt(2)\n\t"
    "v_mfma_f32_16x16x32_bf16 v[136:139], v[24:27], v[240:243], v[136:139]\n\t"
    "ds_read_b128 v[20:23], v5 offset:29696\n\t"
    "v_mfma_f32_16x16x32_bf16 v[168:171], v[24:27], v[244:247], v[168:171]\n\t"
    "s_waitcnt lgkmcnt(2)\n\t"
    "v_mfma_f32_16x16x32_bf16 v[172:175], v[28:31], v[244:247], v[172:175]\n\t"
    "ds_read_b128 v[24:27], v5 offset:30720\n\t"
    "v_mfma_f32_16x16x32_bf16 v[140:143], v[28:31], v[240:243], v[140:143]\n\t"
    "s_waitcnt lgkmcnt(2)\n\t"
    "v_mfma_f32_16x16x32_bf16 v[120:123], v[16:19], v[248:251], v[120:123]\n\t"
    "ds_read_b128 v[28:31], v5 offset:31744\n\t"
    "v_mfma_f32_16x16x32_bf16 v[152:155], v[16:19], v[252:255], v[152:155]\n\t"
    "s_waitcnt lgkmcnt(2)\n\t"
    "v_mfma_f32_16x16x32_bf16 v[156:159], v[20:23], v[252:255], v[156:159]\n\t"
    "v_mfma_f32_16x16x32_bf16 v[124:127], v[20:23], v[248:251], v[124:127]\n\t"
    "s_waitcnt lgkmcnt(0)\n\t"
    "s_barrier\n\t"
    "v_add_u32 v5, s69, v5\n\t"
    "s_sub_u32 s68, 0, s68\n\t"
    "s_sub_u32 s69, 0, s69\n\t"
    "v_mfma_f32_16x16x32_bf16 v[136:139], v[24:27], v[248:251], v[136:139]\n\t"
    "ds_read_b64_tr_b16 v[16:17], v1 offset:0\n\t"
    "ds_read_b64_tr_b16 v[18:19], v1 offset:2048\n\t"
    "v_mfma_f32_16x16x32_bf16 v[168:171], v[24:27], v[252:255], v[168:171]\n\t"
    "ds_read_b64_tr_b16 v[20:21], v2 offset:0\n\t"
    "ds_read_b64_tr_b16 v[22:23], v2 offset:2048\n\t"
    "v_mfma_f32_16x16x32_bf16 v[172:175], v[28:31], v[252:255], v[172:175]\n\t"
    "v_mfma_f32_16x16x32_bf16 v[140:143], v[28:31], v[248:251], v[140:143]\n\t"
    "s_add_u32 s66, s66, 1\n\t"
    "s_cmp_eq_u32 s66, 4\n\t"
    "s_cbranch_scc1 LCE_%=\n\t"
    "s_waitcnt lgkmcnt(2)\n\t"
    "v_mfma_f32_16x16x32_bf16 v[56:59], v[16:19], v[48:51], 0\n\t"
    "ds_read_b64_tr_b16 v[24:25], v3 offset:0\n\t"
    "ds_read_b64_tr_b16 v[26:27], v3 offset:2048\n\t"
    "v_mfma_f32_16x16x32_bf16 v[88:91], v[16:19], v[52:55], 0\n\t"
    "ds_read_b64_tr_b16 v[28:29], v4 offset:0\n\t"
    "ds_read_b64_tr_b16 v[30:31], v4 offset:2048\n\t"
    "s_waitcnt lgkmcnt(4)\n\t"
    "v_mfma_f32_16x16x32_bf16 v[92:95], v[20:23], v[52:55], 0\n\t"
    "ds_read_b64_tr_b16 v[16:17], v1 offset:4096\n\t"
    "v_mfma_f32_16x16x32_bf16 v[60:63], v[20:23], v[48:51], 0\n\t"
    "ds_read_b64_tr_b16 v[18:19], v1 offset:6144\n\t"
    "s_waitcnt lgkmcnt(4)\n\t"
    "v_mfma_f32_16x16x32_bf16 v[72:75], v[24:27], v[48:51], 0\n\t"
    "ds_read_b64_tr_b16 v[20:21], v2 offset:4096\n\t"
    "v_mfma_f32_16x16x32_bf16 v[104:107], v[24:27], v[52:55], 0\n\t"
    "ds_read_b64_tr_b16 v[22:23], v2 offset:6144\n\t"
    "s_waitcnt lgkmcnt(4)\n\t"
    "v_mfma_f32_16x16x32_bf16 v[108:111], v[28:31], v[52:55], 0\n\t"
    "ds_read_b64_tr_b16 v[24:25], v3 offset:4096\n\t"
    "v_mfma_f32_16x16x32_bf16 v[76:79], v[28:31], v[48:51], 0\n\t"
    "ds_read_b64_tr_b16 v[26:27], v3 offset:6144\n\t"
    "s_waitcnt lgkmcnt(4)\n\t"
    "v_mfma_f32_16x16x32_bf16 v[56:59], v[16:19], v[64:67], v[56:59]\n\t"
    "ds_read_b64_tr_b16 v[28:29], v4 offset:4096\n\t"
    "v_cvt_pk_bf16_f32 v32, v120, v121\n\t"
    "v_mfma_f32_16x16x32_bf16 v[88:91], v[16:19], v[68:71], v[88:91]\n\t"
    "ds_read_b64_tr_b16 v[30:31], v4 offset:6144\n\t"
    "s_waitcnt lgkmcnt(4)\n\t"
    "v_mfma_f32_16x16x32_bf16 v[92:95], v[20:23], v[68:71], v[92:95]\n\t"
    "ds_read_b64_tr_b16 v[16:17], v1 offset:8192\n\t"
    "v_cvt_pk_bf16_f32 v33, v122, v123\n\t"
    "v_mfma_f32_16x16x32_bf16 v[60:63], v[20:23], v[64:67], v[60:63]\n\t"
    "ds_read_b64_tr_b16 v[18:19], v1 offset:10240\n\t"
    "s_waitcnt lgkmcnt(4)\n\t"
    "v_mfma_f32_16x16x32_bf16 v[72:75], v[24:27], v[64:67], v[72:75]\n\t"
    "ds_read_b64_tr_b16 v[20:21], v2 offset:8192\n\t"
    "v_cvt_pk_bf16_f32 v34, v124, v125\n\t"
    "v_mfma_f32_16x16x32_bf16 v[104:107], v[24:27], v[68:71], v[104:107]\n\t"
    "ds_read_b64_tr_b16 v[22:23], v2 offset:10240\n\t"
    "s_waitcnt lgkmcnt(4)\n\t"
    "v_mfma_f32_16x16x32_bf16 v[108:111], v[28:31], v[68:71], v[108:111]\n\t"
    "ds_read_b64_tr_b16 v[24:25], v3 offset:8192\n\t"
    "v_cvt_pk_bf16_f32 v35, v126, v127\n\t"
    "v_mfma_f32_16x16x32_bf16 v[76:79], v[28:31], v[64:67], v[76:79]\n\t"
    "ds_read_b64_tr_b16 v[26:27], v3 offset:10240\n\t"
    "s_waitcnt lgkmcnt(4)\n\t"
    "v_mfma_f32_16x16x32_bf16 v[56:59], v[16:19], v[80:83], v[56:59]\n\t"
    "ds_read_b64_tr_b16 v[28:29], v4 offset:8192\n\t"
    "buffer_store_dwordx4 v[32:35], v0, s[36:39], s72 offen offset:0 sc1\n\t"
    "v_mfma_f32_16x16x32_bf16 v[88:91], v[16:19], v[84:87], v[88:91]\n\t"
    "ds_read_b64_tr_b16 v[30:31], v4 offset:10240\n\t"
    "s_waitcnt lgkmcnt(4)\n\t"
    "v_mfma_f32_16x16x32_bf16 v[92:95], v[20:23], v[84:87], v[92:95]\n\t"
    "ds_read_b64_tr_b16 v[16:17], v1 offset:12288\n\t"
    "v_cvt_pk_bf16_f32 v36, v136, v137\n\t"
    "v_mfma_f32_16x16x32_bf16 v[60:63], v[20:23], v[80:83], v[60:63]\n\t"
    "ds_read_b64_tr_b16 v[18:19], v1 offset:14336\n\t"
    "s_waitcnt lgkmcnt(4)\n\t"
    "v_mfma_f32_16x16x32_bf16 v[72:75], v[24:27], v[80:83], v[72:75]\n\t"
    "ds_read_b64_tr_b16 v[20:21], v2 offset:12288\n\t"
    "v_cvt_pk_bf16_f32 v37, v138, v139\n\t"
    "v_mfma_f32_16x16x32_bf16 v[104:107], v[24:27], v[84:87], v[104:107]\n\t"
    "ds_read_b64_tr_b16 v[22:23], v2 offset:14336\n\t"
    "s_waitcnt lgkmcnt(4)\n\t"
    "v_mfma_f32_16x16x32_bf16 v[108:111], v[28:31], v[84:87], v[108:111]\n\t"
    "ds_read_b64_tr_b16 v[24:25], v3 offset:12288\n\t"
    "v_cvt_pk_bf16_f32 v38, v140, v141\n\t"
    "v_mfma_f32_16x16x32_bf16 v[76:79], v[28:31], v[80:83], v[76:79]\n\t"
    "ds_read_b64_tr_b16 v[26:27], v3 offset:14336\n\t"
    "s_waitcnt lgkmcnt(4)\n\t"
    "v_mfma_f32_16x16x32_bf16 v[56:59], v[16:19], v[96:99], v[56:59]\n\t"
    "ds_read_b64_tr_b16 v[28:29], v4 offset:12288\n\t"
    "v_cvt_pk_bf16_f32 v39, v142, v143\n\t"
    "v_mfma_f32_16x16x32_bf16 v[88:91], v[16:19], v[100:103], v[88:91]\n\t"
    "ds_read_b64_tr_b16 v[30:31], v4 offset:14336\n\t"
    "s_waitcnt lgkmcnt(4)\n\t"
    "v_mfma_f32_16x16x32_bf16 v[92:95], v[20:23], v[100:103], v[92:95]\n\t"
    "ds_read_b64_tr_b16 v[16:17], v1 offset:16384\n\t"
    "buffer_store_dwordx4 v[36:39], v0, s[36:39], s72 offen offset:1024 sc1\n\t"
    "v_mfma_f32_16x16x32_bf16 v[60:63], v[20:23], v[96:99], v[60:63]\n\t"
    "ds_read_b64_tr_b16 v[18:19], v1 offset:18432\n\t"
    "s_waitcnt lgkmcnt(4)\n\t"
    "v_mfma_f32_16x16x32_bf16 v[72:75], v[24:27], v[96:99], v[72:75]\n\t"
    "ds_read_b64_tr_b16 v[20:21], v2 offset:16384\n\t"
    "v_cvt_pk_bf16_f32 v40, v152, v153\n\t"
    "v_mfma_f32_16x16x32_bf16 v[104:107], v[24:27], v[100:103], v[104:107]\n\t"
    "ds_read_b64_tr_b16 v[22:23], v2 offset:18432\n\t"
    "s_waitcnt lgkmcnt(4)\n\t"
    "v_mfma_f32_16x16x32_bf16 v[108:111], v[28:31], v[100:103], v[108:111]\n\t"
    "ds_read_b64_tr_b16 v[24:25], v3 offset:16384\n\t"
    "v_cvt_pk_bf16_f32 v41, v154, v155\n\t"
    "v_mfma_f32_16x16x32_bf16 v[76:79], v[28:31], v[96:99], v[76:79]\n\t"
    "ds_read_b64_tr_b16 v[26:27], v3 offset:18432\n\t"
    "s_waitcnt lgkmcnt(4)\n\t"
    "v_mfma_f32_16x16x32_bf16 v[56:59], v[16:19], v[112:115], v[56:59]\n\t"
    "ds_read_b64_tr_b16 v[28:29], v4 offset:16384\n\t"
    "v_cvt_pk_bf16_f32 v42, v156, v157\n\t"
    "v_mfma_f32_16x16x32_bf16 v[88:91], v[16:19], v[116:119], v[88:91]\n\t"
    "ds_read_b64_tr_b16 v[30:31], v4 offset:18432\n\t"
    "s_waitcnt lgkmcnt(4)\n\t"
    "v_mfma_f32_16x16x32_bf16 v[92:95], v[20:23], v[116:119], v[92:95]\n\t"
    "ds_read_b64_tr_b16 v[16:17], v1 offset:20480\n\t"
    "v_cvt_pk_bf16_f32 v43, v158, v159\n\t"
    "v_mfma_f32_16x16x32_bf16 v[60:63], v[20:23], v[112:115], v[60:63]\n\t"
    "ds_read_b64_tr_b16 v[18:19], v1 offset:22528\n\t"
    "s_waitcnt lgkmcnt(4)\n\t"
    "v_mfma_f32_16x16x32_bf16 v[72:75], v[24:27], v[112:115], v[72:75]\n\t"
    "ds_read_b64_tr_b16 v[20:21], v2 offset:20480\n\t"
    "buffer_store_dwordx4 v[40:43], v0, s[36:39], s72 offen offset:2048 sc1\n\t"
    "v_mfma_f32_16x16x32_bf16 v[104:107], v[24:27], v[116:119], v[104:107]\n\t"
    "ds_read_b64_tr_b16 v[22:23], v2 offset:22528\n\t"
    "s_waitcnt lgkmcnt(4)\n\t"
    "v_mfma_f32_16x16x32_bf16 v[108:111], v[28:31], v[116:119], v[108:111]\n\t"
    "ds_read_b64_tr_b16 v[24:25], v3 offset:20480\n\t"
    "v_cvt_pk_bf16_f32 v44, v168, v169\n\t"
    "v_mfma_f32_16x16x32_bf16 v[76:79], v[28:31], v[112:115], v[76:79]\n\t"
    "ds_read_b64_tr_b16 v[26:27], v3 offset:22528\n\t"
    "s_waitcnt lgkmcnt(4)\n\t"
    "v_mfma_f32_16x16x32_bf16 v[56:59], v[16:19], v[128:131], v[56:59]\n\t"
    "ds_read_b64_tr_b16 v[28:29], v4 offset:20480\n\t"
    "v_cvt_pk_bf16_f32 v45, v170, v171\n\t"
    "v_mfma_f32_16x16x32_bf16 v[88:91], v[16:19], v[132:135], v[88:91]\n\t"
    "ds_read_b64_tr_b16 v[30:31], v4 offset:22528\n\t"
    "s_waitcnt lgkmcnt(4)\n\t"
    "v_mfma_f32_16x16x32_bf16 v[92:95], v[20:23], v[132:135], v[92:95]\n\t"
    "ds_read_b64_tr_b16 v[16:17], v1 offset:24576\n\t"
    "v_cvt_pk_bf16_f32 v46, v172, v173\n\t"
    "v_mfma_f32_16x16x32_bf16 v[60:63], v[20:23], v[128:131], v[60:63]\n\t"
    "ds_read_b64_tr_b16 v[18:19], v1 offset:26624\n\t"
    "s_waitcnt lgkmcnt(4)\n\t"
    "v_mfma_f32_16x16x32_bf16 v[72:75], v[24:27], v[128:131], v[72:75]\n\t"
    "ds_read_b64_tr_b16 v[20:21], v2 offset:24576\n\t"
    "v_cvt_pk_bf16_f32 v47, v174, v175\n\t"
    "v_mfma_f32_16x16x32_bf16 v[104:107], v[24:27], v[132:135], v[104:107]\n\t"
    "ds_read_b64_tr_b16 v[22:23], v2 offset:26624\n\t"
    "s_waitcnt lgkmcnt(4)\n\t"
    "v_mfma_f32_16x16x32_bf16 v[108:111], v[28:31], v[132:135], v[108:111]\n\t"
    "ds_read_b64_tr_b16 v[24:25], v3 offset:24576\n\t"
    "buffer_store_dwordx4 v[44:47], v0, s[36:39], s72 offen offset:3072 sc1\n\t"
    "v_mfma_f32_16x16x32_bf16 v[76:79], v[28:31], v[128:131], v[76:79]\n\t"
    "ds_read_b64_tr_b16 v[26:27], v3 offset:26624\n\t"
    "s_waitcnt lgkmcnt(4)\n\t"
    "v_mfma_f32_16x16x32_bf16 v[56:59], v[16:19], v[144:147], v[56:59]\n\t"
    "ds_read_b64_tr_b16 v[28:29], v4 offset:24576\n\t"
    "s_add_u32 s75, s75, 1\n\t"
    "s_cmp_eq_u32 s75, 9\n\t"
    "s_cselect_b32 s75, 0, s75\n\t"
    "v_mfma_f32_16x16x32_bf16 v[88:91], v[16:19], v[148:151], v[88:91]\n\t"
    "ds_read_b64_tr_b16 v[30:31], v4 offset:26624\n\t"
    "s_waitcnt lgkmcnt(4)\n\t"
    "v_mfma_f32_16x16x32_bf16 v[92:95], v[20:23], v[148:151], v[92:95]\n\t"
    "ds_read_b64_tr_b16 v[16:17], v1 offset:28672\n\t"
    "s_lshl_b32 s72, s75, 18\n\t"
    "s_add_u32 s72, s72, s74\n\t"
    "v_mfma_f32_16x16x32_bf16 v[60:63], v[20:23], v[144:147], v[60:63]\n\t"
    "ds_read_b64_tr_b16 v[18:19], v1 offset:30720\n\t"
    "s_waitcnt lgkmcnt(4)\n\t"
    "v_mfma_f32_16x16x32_bf16 v[72:75], v[24:27], v[144:147], v[72:75]\n\t"
    "ds_read_b64_tr_b16 v[20:21], v2 offset:28672\n\t"
    "v_mfma_f32_16x16x32_bf16 v[104:107], v[24:27], v[148:151], v[104:107]\n\t"
    "ds_read_b64_tr_b16 v[22:23], v2 offset:30720\n\t"
    "s_waitcnt lgkmcnt(4)\n\t"
    "v_mfma_f32_16x16x32_bf16 v[108:111], v[28:31], v[148:151], v[108:111]\n\t"
    "ds_read_b64_tr_b16 v[24:25], v3 offset:28672\n\t"
    "v_mfma_f32_16x16x32_bf16 v[76:79], v[28:31], v[144:147], v[76:79]\n\t"
    "ds_read_b64_tr_b16 v[26:27], v3 offset:30720\n\t"
    "s_waitcnt lgkmcnt(4)\n\t"
    "v_mfma_f32_16x16x32_bf16 v[56:59], v[16:19], v[160:163], v[56:59]\n\t"
    "ds_read_b64_tr_b16 v[28:29], v4 offset:28672\n\t"
    "v_mfma_f32_16x16x32_bf16 v[88:91], v[16:19], v[164:167], v[88:91]\n\t"
    "ds_read_b64_tr_b16 v[30:31], v4 offset:30720\n\t"
    "s_waitcnt lgkmcnt(4)\n\t"
    "v_mfma_f32_16x16x32_bf16 v[92:95], v[20:23], v[164:167], v[92:95]\n\t"
    "ds_read_b64_tr_b16 v[16:17], v1 offset:32768\n\t"
    "v_mfma_f32_16x16x32_bf16 v[60:63], v[20:23], v[160:163], v[60:63]\n\t"
    "ds_read_b64_tr_b16 v[18:19], v1 offset:34816\n\t"
    "s_waitcnt lgkmcnt(4)\n\t"
    "v_mfma_f32_16x16x32_bf16 v[72:75], v[24:27], v[160:163], v[72:75]\n\t"
    "ds_read_b64_tr_b16 v[20:21], v2 offset:32768\n\t"
    "v_mfma_f32_16x16x32_bf16 v[104:107], v[24:27], v[164:167], v[104:107]\n\t"
    "ds_read_b64_tr_b16 v[22:23], v2 offset:34816\n\t"
    "s_waitcnt lgkmcnt(4)\n\t"
    "v_mfma_f32_16x16x32_bf16 v[108:111], v[28:31], v[164:167], v[108:111]\n\t"
    "ds_read_b64_tr_b16 v[24:25], v3 offset:32768\n\t"
    "v_mfma_f32_16x16x32_bf16 v[76:79], v[28:31], v[160:163], v[76:79]\n\t"
    "ds_read_b64_tr_b16 v[26:27], v3 offset:34816\n\t"
    "s_waitcnt lgkmcnt(4)\n\t"
    "v_mfma_f32_16x16x32_bf16 v[56:59], v[16:19], v[176:179], v[56:59]\n\t"
    "ds_read_b64_tr_b16 v[28:29], v4 offset:32768\n\t"
    "v_mfma_f32_16x16x32_bf16 v[88:91], v[16:19], v[180:183], v[88:91]\n\t"
    "ds_read_b64_tr_b16 v[30:31], v4 offset:34816\n\t"
    "s_waitcnt lgkmcnt(4)\n\t"
    "v_mfma_f32_16x16x32_bf16 v[92:95], v[20:23], v[180:183], v[92:95]\n\t"
    "v_mfma_f32_16x16x32_bf16 v[60:63], v[20:23], v[176:179], v[60:63]\n\t"
    "s_waitcnt lgkmcnt(0)\n\t"
    "s_barrier\n\t"
    "v_add_u32 v1, s68, v1\n\t"
    "v_add_u32 v2, s68, v2\n\t"
    "v_add_u32 v3, s68, v3\n\t"
    "v_add_u32 v4, s68, v4\n\t"
    "v_mfma_f32_16x16x32_bf16 v[72:75], v[24:27], v[176:179], v[72:75]\n\t"
    "ds_read_b128 v[16:19], v5 offset:0\n\t"
    "v_mfma_f32_16x16x32_bf16 v[104:107], v[24:27], v[180:183], v[104:107]\n\t"
    "ds_read_b128 v[20:23], v5 offset:1024\n\t"
    "v_mfma_f32_16x16x32_bf16 v[108:111], v[28:31], v[180:183], v[108:111]\n\t"
    "v_mfma_f32_16x16x32_bf16 v[76:79], v[28:31], v[176:179], v[76:79]\n\t"
    "s_waitcnt lgkmcnt(1)\n\t"
    "v_mfma_f32_16x16x32_bf16 v[56:59], v[16:19], v[192:195], v[56:59]\n\t"
    "ds_read_b128 v[24:27], v5 offset:2048\n\t"
    "v_mfma_f32_16x16x32_bf16 v[88:91], v[16:19], v[196:199], v[88:91]\n\t"
    "ds_read_b128 v[28:31], v5 offset:3072\n\t"
    "s_waitcnt lgkmcnt(2)\n\t"
    "v_mfma_f32_16x16x32_bf16 v[92:95], v[20:23], v[196:199], v[92:95]\n\t"
    "ds_read_b128 v[16:19], v5 offset:4096\n\t"
    "v_mfma_f32_16x16x32_bf16 v[60:63], v[20:23], v[192:195], v[60:63]\n\t"
    "s_waitcnt lgkmcnt(2)\n\t"
    "v_mfma_f32_16x16x32_bf16 v[72:75], v[24:27], v[192:195], v[72:75]\n\t"
    "ds_read_b128 v[20:23], v5 offset:5120\n\t"
    "v_mfma_f32_16x16x32_bf16 v[104:107], v[24:27], v[196:199], v[104:107]\n\t"
    "s_waitcnt lgkmcnt(2)\n\t"
    "v_mfma_f32_16x16x32_bf16 v[108:111], v[28:31], v[196:199], v[108:111]\n\t"
    "ds_read_b128 v[24:27], v5 offset:6144\n\t"
    "v_mfma_f32_16x16x32_bf16 v[76:79], v[28:31], v[192:195], v[76:79]\n\t"
    "s_waitcnt lgkmcnt(2)\n\t"
    "v_mfma_f32_16x16x32_bf16 v[56:59], v[16:19], v[200:203], v[56:59]\n\t"
    "ds_read_b128 v[28:31], v5 offset:7168\n\t"
    "s_waitcnt vmcnt(0)\n\t"
    "s_mov_b64 exec, 1\n\t"
    "buffer_store_dword v6, v14, s[40:43], s85 offen sc1\n\t"
    "s_mov_b64 exec, -1\n\t"
    "s_add_u32 s85, s85, 256\n\t"
    "s_nop 0\n\t"
    "v_mfma_f32_16x16x32_bf16 v[88:91], v[16:19], v[204:207], v[88:91]\n\t"
    "s_waitcnt lgkmcnt(2)\n\t"
    "v_mfma_f32_16x16x32_bf16 v[92:95], v[20:23], v[204:207], v[92:95]\n\t"
    "ds_read_b128 v[16:19], v5 offset:8192\n\t"
    "v_mfma_f32_16x16x32_bf16 v[60:63], v[20:23], v[200:203], v[60:63]\n\t"
    "s_waitcnt lgkmcnt(2)\n\t"
    "v_mfma_f32_16x16x32_bf16 v[72:75], v[24:27], v[200:203], v[72:75]\n\t"
    "ds_read_b128 v[20:23], v5 offset:9216\n\t"
    "v_mfma_f32_16x16x32_bf16 v[104:107], v[24:27], v[204:207], v[104:107]\n\t"
    "s_waitcnt lgkmcnt(2)\n\t"
    "v_mfma_f32_16x16x32_bf16 v[108:111], v[28:31], v[204:207], v[108:111]\n\t"
    "ds_read_b128 v[24:27], v5 offset:10240\n\t"
    "v_mfma_f32_16x16x32_bf16 v[76:79], v[28:31], v[200:203], v[76:79]\n\t"
    "s_waitcnt lgkmcnt(2)\n\t"
    "v_mfma_f32_16x16x32_bf16 v[56:59], v[16:19], v[208:211], v[56:59]\n\t"
    "ds_read_b128 v[28:31], v5 offset:11264\n\t"
    "v_mfma_f32_16x16x32_bf16 v[88:91], v[16:19], v[212:215], v[88:91]\n\t"
    "s_waitcnt lgkmcnt(2)\n\t"
    "v_mfma_f32_16x16x32_bf16 v[92:95], v[20:23], v[212:215], v[92:95]\n\t"
    "ds_read_b128 v[16:19], v5 offset:12288\n\t"
    "v_mfma_f32_16x16x32_bf16 v[60:63], v[20:23], v[208:211], v[60:63]\n\t"
    "s_waitcnt lgkmcnt(2)\n\t"
    "v_mfma_f32_16x16x32_bf16 v[72:75], v[24:27], v[208:211], v[72:75]\n\t"
    "ds_read_b128 v[20:23], v5 offset:13312\n\t"
    "v_mfma_f32_16x16x32_bf16 v[104:107], v[24:27], v[212:215], v[104:107]\n\t"
    "s_waitcnt lgkmcnt(2)\n\t"
    "v_mfma_f32_16x16x32_bf16 v[108:111], v[28:31], v[212:215], v[108:111]\n\t"
    "ds_read_b128 v[24:27], v5 offset:14336\n\t"
    "v_mfma_f32_16x16x32_bf16 v[76:79], v[28:31], v[208:211], v[76:79]\n\t"
    "s_waitcnt lgkmcnt(2)\n\t"
    "v_mfma_f32_16x16x32_bf16 v[56:59], v[16:19], v[216:219], v[56:59]\n\t"
    "ds_read_b128 v[28:31], v5 offset:15360\n\t"
    "v_mfma_f32_16x16x32_bf16 v[88:91], v[16:19], v[220:223], v[88:91]\n\t"
    "s_waitcnt lgkmcnt(2)\n\t"
    "v_mfma_f32_16x16x32_bf16 v[92:95], v[20:23], v[220:223], v[92:95]\n\t"
    "ds_read_b128 v[16:19], v5 offset:16384\n\t"
    "v_mfma_f32_16x16x32_bf16 v[60:63], v[20:23], v[216:219], v[60:63]\n\t"
    "s_waitcnt lgkmcnt(2)\n\t"
    "v_mfma_f32_16x16x32_bf16 v[72:75], v[24:27], v[216:219], v[72:75]\n\t"
    "ds_read_b128 v[20:23], v5 offset:17408\n\t"
    "v_mfma_f32_16x16x32_bf16 v[104:107], v[24:27], v[220:223], v[104:107]\n\t"
    "s_waitcnt lgkmcnt(2)\n\t"
    "v_mfma_f32_16x16x32_bf16 v[108:111], v[28:31], v[220:223], v[108:111]\n\t"
    "ds_read_b128 v[24:27], v5 offset:18432\n\t"
    "v_mfma_f32_16x16x32_bf16 v[76:79], v[28:31], v[216:219], v[76:79]\n\t"
    "s_waitcnt lgkmcnt(2)\n\t"
    "v_mfma_f32_16x16x32_bf16 v[56:59], v[16:19], v[224:227], v[56:59]\n\t"
    "ds_read_b128 v[28:31], v5 offset:19456\n\t"
    "v_mfma_f32_16x16x32_bf16 v[88:91], v[16:19], v[228:231], v[88:91]\n\t"
    "s_waitcnt lgkmcnt(2)\n\t"
    "v_mfma_f32_16x16x32_bf16 v[92:95], v[20:23], v[228:231], v[92:95]\n\t"
    "ds_read_b128 v[16:19], v5 offset:20480\n\t"
    "v_mfma_f32_16x16x32_bf16 v[60:63], v[20:23], v[224:227], v[60:63]\n\t"
    "s_waitcnt lgkmcnt(2)\n\t"
    "v_mfma_f32_16x16x32_bf16 v[72:75], v[24:27], v[224:227], v[72:75]\n\t"
    "ds_read_b128 v[20:23], v5 offset:21504\n\t"
    "v_mfma_f32_16x16x32_bf16 v[104:107], v[24:27], v[228:231], v[104:107]\n\t"
    "s_waitcnt lgkmcnt(2)\n\t"
    "v_mfma_f32_16x16x32_bf16 v[108:111], v[28:31], v[228:231], v[108:111]\n\t"
    "ds_read_b128 v[24:27], v5 offset:22528\n\t"
    "v_mfma_f32_16x16x32_bf16 v[76:79], v[28:31], v[224:227], v[76:79]\n\t"
    "s_waitcnt lgkmcnt(2)\n\t"
    "v_mfma_f32_16x16x32_bf16 v[56:59], v[16:19], v[232:235], v[56:59]\n\t"
    "ds_read_b128 v[28:31], v5 offset:23552\n\t"
    "v_mfma_f32_16x16x32_bf16 v[88:91], v[16:19], v[236:239], v[88:91]\n\t"
    "s_waitcnt lgkmcnt(2)\n\t"
    "v_mfma_f32_16x16x32_bf16 v[92:95], v[20:23], v[236:239], v[92:95]\n\t"
    "ds_read_b128 v[16:19], v5 offset:24576\n\t"
    "v_mfma_f32_16x16x32_bf16 v[60:63], v[20:23], v[232:235], v[60:63]\n\t"
    "s_waitcnt lgkmcnt(2)\n\t"
    "v_mfma_f32_16x16x32_bf16 v[72:75], v[24:27], v[232:235], v[72:75]\n\t"
    "ds_read_b128 v[20:23], v5 offset:25600\n\t"
    "v_mfma_f32_16x16x32_bf16 v[104:107], v[24:27], v[236:239], v[104:107]\n\t"
    "s_waitcnt lgkmcnt(2)\n\t"
    "v_mfma_f32_16x16x32_bf16 v[108:111], v[28:31], v[236:239], v[108:111]\n\t"
    "ds_read_b128 v[24:27], v5 offset:26624\n\t"
    "v_mfma_f32_16x16x32_bf16 v[76:79], v[28:31], v[232:235], v[76:79]\n\t"
    "s_waitcnt lgkmcnt(2)\n\t"
    "v_mfma_f32_16x16x32_bf16 v[56:59], v[16:19], v[240:243], v[56:59]\n\t"
    "ds_read_b128 v[28:31], v5 offset:27648\n\t"
    "v_mfma_f32_16x16x32_bf16 v[88:91], v[16:19], v[244:247], v[88:91]\n\t"
    "s_waitcnt lgkmcnt(2)\n\t"
    "v_mfma_f32_16x16x32_bf16 v[92:95], v[20:23], v[244:247], v[92:95]\n\t"
    "ds_read_b128 v[16:19], v5 offset:28672\n\t"
    "v_mfma_f32_16x16x32_bf16 v[60:63], v[20:23], v[240:243], v[60:63]\n\t"
    "s_waitcnt lgkmcnt(2)\n\t"
    "v_mfma_f32_16x16x32_bf16 v[72:75], v[24:27], v[240:243], v[72:75]\n\t"
    "ds_read_b128 v[20:23], v5 offset:29696\n\t"
    "v_mfma_f32_16x16x32_bf16 v[104:107], v[24:27], v[244:247], v[104:107]\n\t"
    "s_waitcnt lgkmcnt(2)\n\t"
    "v_mfma_f32_16x16x32_bf16 v[108:111], v[28:31], v[244:247], v[108:111]\n\t"
    "ds_read_b128 v[24:27], v5 offset:30720\n\t"
    "v_mfma_f32_16x16x32_bf16 v[76:79], v[28:31], v[240:243], v[76:79]\n\t"
    "s_waitcnt lgkmcnt(2)\n\t"
    "v_mfma_f32_16x16x32_bf16 v[56:59], v[16:19], v[248:251], v[56:59]\n\t"
    "ds_read_b128 v[28:31], v5 offset:31744\n\t"
    "v_mfma_f32_16x16x32_bf16 v[88:91], v[16:19], v[252:255], v[88:91]\n\t"
    "s_waitcnt lgkmcnt(2)\n\t"
    "v_mfma_f32_16x16x32_bf16 v[92:95], v[20:23], v[252:255], v[92:95]\n\t"
    "v_mfma_f32_16x16x32_bf16 v[60:63], v[20:23], v[248:251], v[60:63]\n\t"
    "s_waitcnt lgkmcnt(0)\n\t"
    "s_barrier\n\t"
    "v_add_u32 v5, s69, v5\n\t"
    "s_sub_u32 s68, 0, s68\n\t"
    "s_sub_u32 s69, 0, s69\n\t"
    "v_mfma_f32_16x16x32_bf16 v[72:75], v[24:27], v[248:251], v[72:75]\n\t"
    "ds_read_b64_tr_b16 v[16:17], v1 offset:0\n\t"
    "ds_read_b64_tr_b16 v[18:19], v1 offset:2048\n\t"
    "v_mfma_f32_16x16x32_bf16 v[104:107], v[24:27], v[252:255], v[104:107]\n\t"
    "ds_read_b64_tr_b16 v[20:21], v2 offset:0\n\t"
    "ds_read_b64_tr_b16 v[22:23], v2 offset:2048\n\t"
    "v_mfma_f32_16x16x32_bf16 v[108:111], v[28:31], v[252:255], v[108:111]\n\t"
    "v_mfma_f32_16x16x32_bf16 v[76:79], v[28:31], v[248:251], v[76:79]\n\t"
    "s_branch LC_%=\n\t"
    "LCE_%=:\n\t"
    "s_waitcnt lgkmcnt(2)\n\t"
    "v_mfma_f32_16x16x32_bf16 v[56:59], v[16:19], v[48:51], 0\n\t"
    "ds_read_b64_tr_b16 v[24:25], v3 offset:0\n\t"
    "ds_read_b64_tr_b16 v[26:27], v3 offset:2048\n\t"
    "v_mfma_f32_16x16x32_bf16 v[88:91], v[16:19], v[52:55], 0\n\t"
    "ds_read_b64_tr_b16 v[28:29], v4 offset:0\n\t"
    "ds_read_b64_tr_b16 v[30:31], v4 offset:2048\n\t"
    "s_waitcnt lgkmcnt(4)\n\t"
    "v_mfma_f32_16x16x32_bf16 v[92:95], v[20:23], v[52:55], 0\n\t"
    "ds_read_b64_tr_b16 v[16:17], v1 offset:4096\n\t"
    "v_mfma_f32_16x16x32_bf16 v[60:63], v[20:23], v[48:51], 0\n\t"
    "ds_read_b64_tr_b16 v[18:19], v1 offset:6144\n\t"
    "s_waitcnt lgkmcnt(4)\n\t"
    "v_mfma_f32_16x16x32_bf16 v[72:75], v[24:27], v[48:51], 0\n\t"
    "ds_read_b64_tr_b16 v[20:21], v2 offset:4096\n\t"
    "v_mfma_f32_16x16x32_bf16 v[104:107], v[24:27], v[52:55], 0\n\t"
    "ds_read_b64_tr_b16 v[22:23], v2 offset:6144\n\t"
    "s_waitcnt lgkmcnt(4)\n\t"
    "v_mfma_f32_16x16x32_bf16 v[108:111], v[28:31], v[52:55], 0\n\t"
    "ds_read_b64_tr_b16 v[24:25], v3 offset:4096\n\t"
    "v_mfma_f32_16x16x32_bf16 v[76:79], v[28:31], v[48:51], 0\n\t"
    "ds_read_b64_tr_b16 v[26:27], v3 offset:6144\n\t"
    "s_waitcnt lgkmcnt(4)\n\t"
    "v_mfma_f32_16x16x32_bf16 v[56:59], v[16:19], v[64:67], v[56:59]\n\t"
    "ds_read_b64_tr_b16 v[28:29], v4 offset:4096\n\t"
    "v_cvt_pk_bf16_f32 v32, v120, v121\n\t"
    "v_mfma_f32_16x16x32_bf16 v[88:91], v[16:19], v[68:71], v[88:91]\n\t"
    "ds_read_b64_tr_b16 v[30:31], v4 offset:6144\n\t"
    "s_waitcnt lgkmcnt(4)\n\t"
    "v_mfma_f32_16x16x32_bf16 v[92:95], v[20:23], v[68:71], v[92:95]\n\t"
    "ds_read_b64_tr_b16 v[16:17], v1 offset:8192\n\t"
    "v_cvt_pk_bf16_f32 v33, v122, v123\n\t"
    "v_mfma_f32_16x16x32_bf16 v[60:63], v[20:23], v[64:67], v[60:63]\n\t"
    "ds_read_b64_tr_b16 v[18:19], v1 offset:10240\n\t"
    "s_waitcnt lgkmcnt(4)\n\t"
    "v_mfma_f32_16x16x32_bf16 v[72:75], v[24:27], v[64:67], v[72:75]\n\t"
    "ds_read_b64_tr_b16 v[20:21], v2 offset:8192\n\t"
    "v_cvt_pk_bf16_f32 v34, v124, v125\n\t"
    "v_mfma_f32_16x16x32_bf16 v[104:107], v[24:27], v[68:71], v[104:107]\n\t"
    "ds_read_b64_tr_b16 v[22:23], v2 offset:10240\n\t"
    "s_waitcnt lgkmcnt(4)\n\t"
    "v_mfma_f32_16x16x32_bf16 v[108:111], v[28:31], v[68:71], v[108:111]\n\t"
    "ds_read_b64_tr_b16 v[24:25], v3 offset:8192\n\t"
    "v_cvt_pk_bf16_f32 v35, v126, v127\n\t"
    "v_mfma_f32_16x16x32_bf16 v[76:79], v[28:31], v[64:67], v[76:79]\n\t"
    "ds_read_b64_tr_b16 v[26:27], v3 offset:10240\n\t"
    "s_waitcnt lgkmcnt(4)\n\t"
    "v_mfma_f32_16x16x32_bf16 v[56:59], v[16:19], v[80:83], v[56:59]\n\t"
    "ds_read_b64_tr_b16 v[28:29], v4 offset:8192\n\t"
    "buffer_store_dwordx4 v[32:35], v0, s[36:39], s72 offen offset:0 sc1\n\t"
    "v_mfma_f32_16x16x32_bf16 v[88:91], v[16:19], v[84:87], v[88:91]\n\t"
    "ds_read_b64_tr_b16 v[30:31], v4 offset:10240\n\t"
    "s_waitcnt lgkmcnt(4)\n\t"
    "v_mfma_f32_16x16x32_bf16 v[92:95], v[20:23], v[84:87], v[92:95]\n\t"
    "ds_read_b64_tr_b16 v[16:17], v1 offset:12288\n\t"
    "v_cvt_pk_bf16_f32 v36, v136, v137\n\t"
    "v_mfma_f32_16x16x32_bf16 v[60:63], v[20:23], v[80:83], v[60:63]\n\t"
    "ds_read_b64_tr_b16 v[18:19], v1 offset:14336\n\t"
    "s_waitcnt lgkmcnt(4)\n\t"
    "v_mfma_f32_16x16x32_bf16 v[72:75], v[24:27], v[80:83], v[72:75]\n\t"
    "ds_read_b64_tr_b16 v[20:21], v2 offset:12288\n\t"
    "v_cvt_pk_bf16_f32 v37, v138, v139\n\t"
    "v_mfma_f32_16x16x32_bf16 v[104:107], v[24:27], v[84:87], v[104:107]\n\t"
    "ds_read_b64_tr_b16 v[22:23], v2 offset:14336\n\t"
    "s_waitcnt lgkmcnt(4)\n\t"
    "v_mfma_f32_16x16x32_bf16 v[108:111], v[28:31], v[84:87], v[108:111]\n\t"
    "ds_read_b64_tr_b16 v[24:25], v3 offset:12288\n\t"
    "v_cvt_pk_bf16_f32 v38, v140, v141\n\t"
    "v_mfma_f32_16x16x32_bf16 v[76:79], v[28:31], v[80:83], v[76:79]\n\t"
    "ds_read_b64_tr_b16 v[26:27], v3 offset:14336\n\t"
    "s_waitcnt lgkmcnt(4)\n\t"
    "v_mfma_f32_16x16x32_bf16 v[56:59], v[16:19], v[96:99], v[56:59]\n\t"
    "ds_read_b64_tr_b16 v[28:29], v4 offset:12288\n\t"
    "v_cvt_pk_bf16_f32 v39, v142, v143\n\t"
    "v_mfma_f32_16x16x32_bf16 v[88:91], v[16:19], v[100:103], v[88:91]\n\t"
    "ds_read_b64_tr_b16 v[30:31], v4 offset:14336\n\t"
    "s_waitcnt lgkmcnt(4)\n\t"
    "v_mfma_f32_16x16x32_bf16 v[92:95], v[20:23], v[100:103], v[92:95]\n\t"
    "ds_read_b64_tr_b16 v[16:17], v1 offset:16384\n\t"
    "buffer_store_dwordx4 v[36:39], v0, s[36:39], s72 offen offset:1024 sc1\n\t"
    "v_mfma_f32_16x16x32_bf16 v[60:63], v[20:23], v[96:99], v[60:63]\n\t"
    "ds_read_b64_tr_b16 v[18:19], v1 offset:18432\n\t"
    "s_waitcnt lgkmcnt(4)\n\t"
    "v_mfma_f32_16x16x32_bf16 v[72:75], v[24:27], v[96:99], v[72:75]\n\t"
    "ds_read_b64_tr_b16 v[20:21], v2 offset:16384\n\t"
    "v_cvt_pk_bf16_f32 v40, v152, v153\n\t"
    "v_mfma_f32_16x16x32_bf16 v[104:107], v[24:27], v[100:103], v[104:107]\n\t"
    "ds_read_b64_tr_b16 v[22:23], v2 offset:18432\n\t"
    "s_waitcnt lgkmcnt(4)\n\t"
    "v_mfma_f32_16x16x32_bf16 v[108:111], v[28:31], v[100:103], v[108:111]\n\t"
    "ds_read_b64_tr_b16 v[24:25], v3 offset:16384\n\t"
    "v_cvt_pk_bf16_f32 v41, v154, v155\n\t"
    "v_mfma_f32_16x16x32_bf16 v[76:79], v[28:31], v[96:99], v[76:79]\n\t"
    "ds_read_b64_tr_b16 v[26:27], v3 offset:18432\n\t"
    "s_waitcnt lgkmcnt(4)\n\t"
    "v_mfma_f32_16x16x32_bf16 v[56:59], v[16:19], v[112:115], v[56:59]\n\t"
    "ds_read_b64_tr_b16 v[28:29], v4 offset:16384\n\t"
    "v_cvt_pk_bf16_f32 v42, v156, v157\n\t"
    "v_mfma_f32_16x16x32_bf16 v[88:91], v[16:19], v[116:119], v[88:91]\n\t"
    "ds_read_b64_tr_b16 v[30:31], v4 offset:18432\n\t"
    "s_waitcnt lgkmcnt(4)\n\t"
    "v_mfma_f32_16x16x32_bf16 v[92:95], v[20:23], v[116:119], v[92:95]\n\t"
    "ds_read_b64_tr_b16 v[16:17], v1 offset:20480\n\t"
    "v_cvt_pk_bf16_f32 v43, v158, v159\n\t"
    "v_mfma_f32_16x16x32_bf16 v[60:63], v[20:23], v[112:115], v[60:63]\n\t"
    "ds_read_b64_tr_b16 v[18:19], v1 offset:22528\n\t"
    "s_waitcnt lgkmcnt(4)\n\t"
    "v_mfma_f32_16x16x32_bf16 v[72:75], v[24:27], v[112:115], v[72:75]\n\t"
    "ds_read_b64_tr_b16 v[20:21], v2 offset:20480\n\t"
    "buffer_store_dwordx4 v[40:43], v0, s[36:39], s72 offen offset:2048 sc1\n\t"
    "v_mfma_f32_16x16x32_bf16 v[104:107], v[24:27], v[116:119], v[104:107]\n\t"
    "ds_read_b64_tr_b16 v[22:23], v2 offset:22528\n\t"
    "s_waitcnt lgkmcnt(4)\n\t"
    "v_mfma_f32_16x16x32_bf16 v[108:111], v[28:31], v[116:119], v[108:111]\n\t"
    "ds_read_b64_tr_b16 v[24:25], v3 offset:20480\n\t"
    "v_cvt_pk_bf16_f32 v44, v168, v169\n\t"
    "v_mfma_f32_16x16x32_bf16 v[76:79], v[28:31], v[112:115], v[76:79]\n\t"
    "ds_read_b64_tr_b16 v[26:27], v3 offset:22528\n\t"
    "s_waitcnt lgkmcnt(4)\n\t"
    "v_mfma_f32_16x16x32_bf16 v[56:59], v[16:19], v[128:131], v[56:59]\n\t"
    "ds_read_b64_tr_b16 v[28:29], v4 offset:20480\n\t"
    "v_cvt_pk_bf16_f32 v45, v170, v171\n\t"
    "v_mfma_f32_16x16x32_bf16 v[88:91], v[16:19], v[132:135], v[88:91]\n\t"
    "ds_read_b64_tr_b16 v[30:31], v4 offset:22528\n\t"
    "s_waitcnt lgkmcnt(4)\n\t"
    "v_mfma_f32_16x16x32_bf16 v[92:95], v[20:23], v[132:135], v[92:95]\n\t"
    "ds_read_b64_tr_b16 v[16:17], v1 offset:24576\n\t"
    "v_cvt_pk_bf16_f32 v46, v172, v173\n\t"
    "v_mfma_f32_16x16x32_bf16 v[60:63], v[20:23], v[128:131], v[60:63]\n\t"
    "ds_read_b64_tr_b16 v[18:19], v1 offset:26624\n\t"
    "s_waitcnt lgkmcnt(4)\n\t"
    "v_mfma_f32_16x16x32_bf16 v[72:75], v[24:27], v[128:131], v[72:75]\n\t"
    "ds_read_b64_tr_b16 v[20:21], v2 offset:24576\n\t"
    "v_cvt_pk_bf16_f32 v47, v174, v175\n\t"
    "v_mfma_f32_16x16x32_bf16 v[104:107], v[24:27], v[132:135], v[104:107]\n\t"
    "ds_read_b64_tr_b16 v[22:23], v2 offset:26624\n\t"
    "s_waitcnt lgkmcnt(4)\n\t"
    "v_mfma_f32_16x16x32_bf16 v[108:111], v[28:31], v[132:135], v[108:111]\n\t"
    "ds_read_b64_tr_b16 v[24:25], v3 offset:24576\n\t"
    "buffer_store_dwordx4 v[44:47], v0, s[36:39], s72 offen offset:3072 sc1\n\t"
    "v_mfma_f32_16x16x32_bf16 v[76:79], v[28:31], v[128:131], v[76:79]\n\t"
    "ds_read_b64_tr_b16 v[26:27], v3 offset:26624\n\t"
    "s_waitcnt lgkmcnt(4)\n\t"
    "v_mfma_f32_16x16x32_bf16 v[56:59], v[16:19], v[144:147], v[56:59]\n\t"
    "ds_read_b64_tr_b16 v[28:29], v4 offset:24576\n\t"
    "s_add_u32 s75, s75, 1\n\t"
    "s_cmp_eq_u32 s75, 9\n\t"
    "s_cselect_b32 s75, 0, s75\n\t"
    "v_mfma_f32_16x16x32_bf16 v[88:91], v[16:19], v[148:151], v[88:91]\n\t"
    "ds_read_b64_tr_b16 v[30:31], v4 offset:26624\n\t"
    "s_waitcnt lgkmcnt(4)\n\t"
    "v_mfma_f32_16x16x32_bf16 v[92:95], v[20:23], v[148:151], v[92:95]\n\t"
    "ds_read_b64_tr_b16 v[16:17], v1 offset:28672\n\t"
    "s_lshl_b32 s72, s75, 18\n\t"
    "s_add_u32 s72, s72, s74\n\t"
    "v_mfma_f32_16x16x32_bf16 v[60:63], v[20:23], v[144:147], v[60:63]\n\t"
    "ds_read_b64_tr_b16 v[18:19], v1 offset:30720\n\t"
    "s_waitcnt lgkmcnt(4)\n\t"
    "v_mfma_f32_16x16x32_bf16 v[72:75], v[24:27], v[144:147], v[72:75]\n\t"
    "ds_read_b64_tr_b16 v[20:21], v2 offset:28672\n\t"
    "v_mfma_f32_16x16x32_bf16 v[104:107], v[24:27], v[148:151], v[104:107]\n\t"
    "ds_read_b64_tr_b16 v[22:23], v2 offset:30720\n\t"
    "s_waitcnt lgkmcnt(4)\n\t"
    "v_mfma_f32_16x16x32_bf16 v[108:111], v[28:31], v[148:151], v[108:111]\n\t"
    "ds_read_b64_tr_b16 v[24:25], v3 offset:28672\n\t"
    "v_mfma_f32_16x16x32_bf16 v[76:79], v[28:31], v[144:147], v[76:79]\n\t"
    "ds_read_b64_tr_b16 v[26:27], v3 offset:30720\n\t"
    "s_waitcnt lgkmcnt(4)\n\t"
    "v_mfma_f32_16x16x32_bf16 v[56:59], v[16:19], v[160:163], v[56:59]\n\t"
    "ds_read_b64_tr_b16 v[28:29], v4 offset:28672\n\t"
    "v_mfma_f32_16x16x32_bf16 v[88:91], v[16:19], v[164:167], v[88:91]\n\t"
    "ds_read_b64_tr_b16 v[30:31], v4 offset:30720\n\t"
    "s_waitcnt lgkmcnt(4)\n\t"
    "v_mfma_f32_16x16x32_bf16 v[92:95], v[20:23], v[164:167], v[92:95]\n\t"
    "ds_read_b64_tr_b16 v[16:17], v1 offset:32768\n\t"
    "v_mfma_f32_16x16x32_bf16 v[60:63], v[20:23], v[160:163], v[60:63]\n\t"
    "ds_read_b64_tr_b16 v[18:19], v1 offset:34816\n\t"
    "s_waitcnt lgkmcnt(4)\n\t"
    "v_mfma_f32_16x16x32_bf16 v[72:75], v[24:27], v[160:163], v[72:75]\n\t"
    "ds_read_b64_tr_b16 v[20:21], v2 offset:32768\n\t"
    "v_mfma_f32_16x16x32_bf16 v[104:107], v[24:27], v[164:167], v[104:107]\n\t"
    "ds_read_b64_tr_b16 v[22:23], v2 offset:34816\n\t"
    "s_waitcnt lgkmcnt(4)\n\t"
    "v_mfma_f32_16x16x32_bf16 v[108:111], v[28:31], v[164:167], v[108:111]\n\t"
    "ds_read_b64_tr_b16 v[24:25], v3 offset:32768\n\t"
    "v_mfma_f32_16x16x32_bf16 v[76:79], v[28:31], v[160:163], v[76:79]\n\t"
    "ds_read_b64_tr_b16 v[26:27], v3 offset:34816\n\t"
    "s_waitcnt lgkmcnt(4)\n\t"
    "v_mfma_f32_16x16x32_bf16 v[56:59], v[16:19], v[176:179], v[56:59]\n\t"
    "ds_read_b64_tr_b16 v[28:29], v4 offset:32768\n\t"
    "v_mfma_f32_16x16x32_bf16 v[88:91], v[16:19], v[180:183], v[88:91]\n\t"
    "ds_read_b64_tr_b16 v[30:31], v4 offset:34816\n\t"
    "s_waitcnt lgkmcnt(4)\n\t"
    "v_mfma_f32_16x16x32_bf16 v[92:95], v[20:23], v[180:183], v[92:95]\n\t"
    "v_mfma_f32_16x16x32_bf16 v[60:63], v[20:23], v[176:179], v[60:63]\n\t"
    "s_waitcnt lgkmcnt(0)\n\t"
    "s_barrier\n\t"
    "v_add_u32 v1, s68, v1\n\t"
    "v_add_u32 v2, s68, v2\n\t"
    "v_add_u32 v3, s68, v3\n\t"
    "v_add_u32 v4, s68, v4\n\t"
    "v_mfma_f32_16x16x32_bf16 v[72:75], v[24:27], v[176:179], v[72:75]\n\t"
    "ds_read_b128 v[16:19], v5 offset:0\n\t"
    "v_mfma_f32_16x16x32_bf16 v[104:107], v[24:27], v[180:183], v[104:107]\n\t"
    "ds_read_b128 v[20:23], v5 offset:1024\n\t"
    "v_mfma_f32_16x16x32_bf16 v[108:111], v[28:31], v[180:183], v[108:111]\n\t"
    "v_mfma_f32_16x16x32_bf16 v[76:79], v[28:31], v[176:179], v[76:79]\n\t"
    "s_waitcnt lgkmcnt(1)\n\t"
    "v_mfma_f32_16x16x32_bf16 v[56:59], v[16:19], v[192:195], v[56:59]\n\t"
    "ds_read_b128 v[24:27], v5 offset:2048\n\t"
    "v_mfma_f32_16x16x32_bf16 v[88:91], v[16:19], v[196:199], v[88:91]\n\t"
    "ds_read_b128 v[28:31], v5 offset:3072\n\t"
    "s_waitcnt lgkmcnt(2)\n\t"
    "v_mfma_f32_16x16x32_bf16 v[92:95], v[20:23], v[196:199], v[92:95]\n\t"
    "ds_read_b128 v[16:19], v5 offset:4096\n\t"
    "v_mfma_f32_16x16x32_bf16 v[60:63], v[20:23], v[192:195], v[60:63]\n\t"
    "s_waitcnt lgkmcnt(2)\n\t"
    "v_mfma_f32_16x16x32_bf16 v[72:75], v[24:27], v[192:195], v[72:75]\n\t"
    "ds_read_b128 v[20:23], v5 offset:5120\n\t"
    "v_mfma_f32_16x16x32_bf16 v[104:107], v[24:27], v[196:199], v[104:107]\n\t"
    "s_waitcnt lgkmcnt(2)\n\t"
    "v_mfma_f32_16x16x32_bf16 v[108:111], v[28:31], v[196:199], v[108:111]\n\t"
    "ds_read_b128 v[24:27], v5 offset:6144\n\t"
    "v_mfma_f32_16x16x32_bf16 v[76:79], v[28:31], v[192:195], v[76:79]\n\t"
    "s_waitcnt lgkmcnt(2)\n\t"
    "v_mfma_f32_16x16x32_bf16 v[56:59], v[16:19], v[200:203], v[56:59]\n\t"
    "ds_read_b128 v[28:31], v5 offset:7168\n\t"
    "s_waitcnt vmcnt(0)\n\t"
    "s_mov_b64 exec, 1\n\t"
    "buffer_store_dword v6, v14, s[40:43], s85 offen sc1\n\t"
    "s_mov_b64 exec, -1\n\t"
    "s_add_u32 s85, s85, 256\n\t"
    "s_nop 0\n\t"
    "v_mfma_f32_16x16x32_bf16 v[88:91], v[16:19], v[204:207], v[88:91]\n\t"
    "s_waitcnt lgkmcnt(2)\n\t"
    "v_mfma_f32_16x16x32_bf16 v[92:95], v[20:23], v[204:207], v[92:95]\n\t"
    "ds_read_b128 v[16:19], v5 offset:8192\n\t"
    "v_mfma_f32_16x16x32_bf16 v[60:63], v[20:23], v[200:203], v[60:63]\n\t"
    "s_waitcnt lgkmcnt(2)\n\t"
    "v_mfma_f32_16x16x32_bf16 v[72:75], v[24:27], v[200:203], v[72:75]\n\t"
    "ds_read_b128 v[20:23], v5 offset:9216\n\t"
    "v_mfma_f32_16x16x32_bf16 v[104:107], v[24:27], v[204:207], v[104:107]\n\t"
    "s_waitcnt lgkmcnt(2)\n\t"
    "v_mfma_f32_16x16x32_bf16 v[108:111], v[28:31], v[204:207], v[108:111]\n\t"
    "ds_read_b128 v[24:27], v5 offset:10240\n\t"
    "v_mfma_f32_16x16x32_bf16 v[76:79], v[28:31], v[200:203], v[76:79]\n\t"
    "s_waitcnt lgkmcnt(2)\n\t"
    "v_mfma_f32_16x16x32_bf16 v[56:59], v[16:19], v[208:211], v[56:59]\n\t"
    "ds_read_b128 v[28:31], v5 offset:11264\n\t"
    "v_mfma_f32_16x16x32_bf16 v[88:91], v[16:19], v[212:215], v[88:91]\n\t"
    "s_waitcnt lgkmcnt(2)\n\t"
    "v_mfma_f32_16x16x32_bf16 v[92:95], v[20:23], v[212:215], v[92:95]\n\t"
    "ds_read_b128 v[16:19], v5 offset:12288\n\t"
    "v_mfma_f32_16x16x32_bf16 v[60:63], v[20:23], v[208:211], v[60:63]\n\t"
    "s_waitcnt lgkmcnt(2)\n\t"
    "v_mfma_f32_16x16x32_bf16 v[72:75], v[24:27], v[208:211], v[72:75]\n\t"
    "ds_read_b128 v[20:23], v5 offset:13312\n\t"
    "v_mfma_f32_16x16x32_bf16 v[104:107], v[24:27], v[212:215], v[104:107]\n\t"
    "s_waitcnt lgkmcnt(2)\n\t"
    "v_mfma_f32_16x16x32_bf16 v[108:111], v[28:31], v[212:215], v[108:111]\n\t"
    "ds_read_b128 v[24:27], v5 offset:14336\n\t"
    "v_mfma_f32_16x16x32_bf16 v[76:79], v[28:31], v[208:211], v[76:79]\n\t"
    "s_waitcnt lgkmcnt(2)\n\t"
    "v_mfma_f32_16x16x32_bf16 v[56:59], v[16:19], v[216:219], v[56:59]\n\t"
    "ds_read_b128 v[28:31], v5 offset:15360\n\t"
    "v_mfma_f32_16x16x32_bf16 v[88:91], v[16:19], v[220:223], v[88:91]\n\t"
    "s_waitcnt lgkmcnt(2)\n\t"
    "v_mfma_f32_16x16x32_bf16 v[92:95], v[20:23], v[220:223], v[92:95]\n\t"
    "ds_read_b128 v[16:19], v5 offset:16384\n\t"
    "v_mfma_f32_16x16x32_bf16 v[60:63], v[20:23], v[216:219], v[60:63]\n\t"
    "s_waitcnt lgkmcnt(2)\n\t"
    "v_mfma_f32_16x16x32_bf16 v[72:75], v[24:27], v[216:219], v[72:75]\n\t"
    "ds_read_b128 v[20:23], v5 offset:17408\n\t"
    "v_mfma_f32_16x16x32_bf16 v[104:107], v[24:27], v[220:223], v[104:107]\n\t"
    "s_waitcnt lgkmcnt(2)\n\t"
    "v_mfma_f32_16x16x32_bf16 v[108:111], v[28:31], v[220:223], v[108:111]\n\t"
    "ds_read_b128 v[24:27], v5 offset:18432\n\t"
    "v_mfma_f32_16x16x32_bf16 v[76:79], v[28:31], v[216:219], v[76:79]\n\t"
    "s_waitcnt lgkmcnt(2)\n\t"
    "v_mfma_f32_16x16x32_bf16 v[56:59], v[16:19], v[224:227], v[56:59]\n\t"
    "ds_read_b128 v[28:31], v5 offset:19456\n\t"
    "v_mfma_f32_16x16x32_bf16 v[88:91], v[16:19], v[228:231], v[88:91]\n\t"
    "s_waitcnt lgkmcnt(2)\n\t"
    "v_mfma_f32_16x16x32_bf16 v[92:95], v[20:23], v[228:231], v[92:95]\n\t"
    "ds_read_b128 v[16:19], v5 offset:20480\n\t"
    "v_mfma_f32_16x16x32_bf16 v[60:63], v[20:23], v[224:227], v[60:63]\n\t"
    "s_waitcnt lgkmcnt(2)\n\t"
    "v_mfma_f32_16x16x32_bf16 v[72:75], v[24:27], v[224:227], v[72:75]\n\t"
    "ds_read_b128 v[20:23], v5 offset:21504\n\t"
    "v_mfma_f32_16x16x32_bf16 v[104:107], v[24:27], v[228:231], v[104:107]\n\t"
    "s_waitcnt lgkmcnt(2)\n\t"
    "v_mfma_f32_16x16x32_bf16 v[108:111], v[28:31], v[228:231], v[108:111]\n\t"
    "ds_read_b128 v[24:27], v5 offset:22528\n\t"
    "v_mfma_f32_16x16x32_bf16 v[76:79], v[28:31], v[224:227], v[76:79]\n\t"
    "s_waitcnt lgkmcnt(2)\n\t"
    "v_mfma_f32_16x16x32_bf16 v[56:59], v[16:19], v[232:235], v[56:59]\n\t"
    "ds_read_b128 v[28:31], v5 offset:23552\n\t"
    "v_mfma_f32_16x16x32_bf16 v[88:91], v[16:19], v[236:239], v[88:91]\n\t"
    "s_waitcnt lgkmcnt(2)\n\t"
    "v_mfma_f32_16x16x32_bf16 v[92:95], v[20:23], v[236:239], v[92:95]\n\t"
    "ds_read_b128 v[16:19], v5 offset:24576\n\t"
    "v_mfma_f32_16x16x32_bf16 v[60:63], v[20:23], v[232:235], v[60:63]\n\t"
    "s_waitcnt lgkmcnt(2)\n\t"
    "v_mfma_f32_16x16x32_bf16 v[72:75], v[24:27], v[232:235], v[72:75]\n\t"
    "ds_read_b128 v[20:23], v5 offset:25600\n\t"
    "v_mfma_f32_16x16x32_bf16 v[104:107], v[24:27], v[236:239], v[104:107]\n\t"
    "s_waitcnt lgkmcnt(2)\n\t"
    "v_mfma_f32_16x16x32_bf16 v[108:111], v[28:31], v[236:239], v[108:111]\n\t"
    "ds_read_b128 v[24:27], v5 offset:26624\n\t"
    "v_mfma_f32_16x16x32_bf16 v[76:79], v[28:31], v[232:235], v[76:79]\n\t"
    "s_waitcnt lgkmcnt(2)\n\t"
    "v_mfma_f32_16x16x32_bf16 v[56:59], v[16:19], v[240:243], v[56:59]\n\t"
    "ds_read_b128 v[28:31], v5 offset:27648\n\t"
    "v_mfma_f32_16x16x32_bf16 v[88:91], v[16:19], v[244:247], v[88:91]\n\t"
    "s_waitcnt lgkmcnt(2)\n\t"
    "v_mfma_f32_16x16x32_bf16 v[92:95], v[20:23], v[244:247], v[92:95]\n\t"
    "ds_read_b128 v[16:19], v5 offset:28672\n\t"
    "v_mfma_f32_16x16x32_bf16 v[60:63], v[20:23], v[240:243], v[60:63]\n\t"
    "s_waitcnt lgkmcnt(2)\n\t"
    "v_mfma_f32_16x16x32_bf16 v[72:75], v[24:27], v[240:243], v[72:75]\n\t"
    "ds_read_b128 v[20:23], v5 offset:29696\n\t"
    "v_mfma_f32_16x16x32_bf16 v[104:107], v[24:27], v[244:247], v[104:107]\n\t"
    "s_waitcnt lgkmcnt(2)\n\t"
    "v_mfma_f32_16x16x32_bf16 v[108:111], v[28:31], v[244:247], v[108:111]\n\t"
    "ds_read_b128 v[24:27], v5 offset:30720\n\t"
    "v_mfma_f32_16x16x32_bf16 v[76:79], v[28:31], v[240:243], v[76:79]\n\t"
    "s_waitcnt lgkmcnt(2)\n\t"
    "v_mfma_f32_16x16x32_bf16 v[56:59], v[16:19], v[248:251], v[56:59]\n\t"
    "ds_read_b128 v[28:31], v5 offset:31744\n\t"
    "v_mfma_f32_16x16x32_bf16 v[88:91], v[16:19], v[252:255], v[88:91]\n\t"
    "s_waitcnt lgkmcnt(2)\n\t"
    "v_mfma_f32_16x16x32_bf16 v[92:95], v[20:23], v[252:255], v[92:95]\n\t"
    "v_mfma_f32_16x16x32_bf16 v[60:63], v[20:23], v[248:251], v[60:63]\n\t"
    "s_waitcnt lgkmcnt(0)\n\t"
    "v_mfma_f32_16x16x32_bf16 v[72:75], v[24:27], v[248:251], v[72:75]\n\t"
    "v_mfma_f32_16x16x32_bf16 v[104:107], v[24:27], v[252:255], v[104:107]\n\t"
    "v_mfma_f32_16x16x32_bf16 v[108:111], v[28:31], v[252:255], v[108:111]\n\t"
    "v_mfma_f32_16x16x32_bf16 v[76:79], v[28:31], v[248:251], v[76:79]\n\t"
    "s_nop 7\n\t"
    "v_cvt_pk_bf16_f32 v32, v56, v57\n\t"
    "v_cvt_pk_bf16_f32 v33, v58, v59\n\t"
    "v_cvt_pk_bf16_f32 v34, v60, v61\n\t"
    "v_cvt_pk_bf16_f32 v35, v62, v63\n\t"
    "buffer_store_dwordx4 v[32:35], v0, s[36:39], s72 offen offset:0 sc1\n\t"
    "v_cvt_pk_bf16_f32 v36, v72, v73\n\t"
    "v_cvt_pk_bf16_f32 v37, v74, v75\n\t"
    "v_cvt_pk_bf16_f32 v38, v76, v77\n\t"
    "v_cvt_pk_bf16_f32 v39, v78, v79\n\t"
    "buffer_store_dwordx4 v[36:39], v0, s[36:39], s72 offen offset:1024 sc1\n\t"
    "v_cvt_pk_bf16_f32 v40, v88, v89\n\t"
    "v_cvt_pk_bf16_f32 v41, v90, v91\n\t"
    "v_cvt_pk_bf16_f32 v42, v92, v93\n\t"
    "v_cvt_pk_bf16_f32 v43, v94, v95\n\t"
    "buffer_store_dwordx4 v[40:43], v0, s[36:39], s72 offen offset:2048 sc1\n\t"
    "v_cvt_pk_bf16_f32 v44, v104, v105\n\t"
    "v_cvt_pk_bf16_f32 v45, v106, v107\n\t"
    "v_cvt_pk_bf16_f32 v46, v108, v109\n\t"
    "v_cvt_pk_bf16_f32 v47, v110, v111\n\t"
    "buffer_store_dwordx4 v[44:47], v0, s[36:39], s72 offen offset:3072 sc1\n\t"
    "s_add_u32 s75, s75, 1\n\t"
    "s_cmp_eq_u32 s75, 9\n\t"
    "s_cselect_b32 s75, 0, s75\n\t"
    "s_lshl_b32 s72, s75, 18\n\t"
    "s_add_u32 s72, s72, s74\n\t"
    "v_readfirstlane_b32 s80, v6\n\t"
    "s_sub_u32 s81, 8, s64\n\t"
    "v_mbcnt_lo_u32_b32 v11, -1, 0\n\t"
    "v_mbcnt_hi_u32_b32 v11, -1, v11\n\t"
    "v_and_b32 v12, 15, v11\n\t"
    "v_lshrrev_b32 v13, 4, v11\n\t"
    "v_lshlrev_b32 v2, 2, v12\n\t"
    "s_mul_i32 s82, s48, 2304\n\t"
    "s_lshr_b32 s84, s49, 2\n\t"
    "s_lshl_b32 s83, s84, 6\n\t"
    "s_add_u32 s82, s82, s83\n\t"
    "s_add_u32 s82, s82, 4096\n\t"
    "v_add_u32 v5, s82, v2\n\t"
    "s_lshl_b32 s84, s84, 5\n\t"
    "s_and_b32 s82, s49, 2\n\t"
    "s_lshl_b32 s82, s82, 1\n\t"
    "s_add_u32 s82, s82, s84\n\t"
    "v_lshrrev_b32 v3, 2, v12\n\t"
    "v_and_b32 v4, 3, v12\n\t"
    "v_lshl_add_u32 v3, v3, 3, v4\n\t"
    "v_add_u32 v3, s82, v3\n\t"
    "v_mul_u32_u24 v3, 1152, v3\n\t"
    "v_and_b32 v4, 1, v13\n\t"
    "v_lshl_add_u32 v3, v4, 5, v3\n\t"
    "v_lshrrev_b32 v4, 1, v13\n\t"
    "v_lshl_add_u32 v3, v4, 4, v3\n\t"
    "s_and_b32 s82, s49, 1\n\t"
    "s_lshl_b32 s82, s82, 6\n\t"
    "v_add_u32 v3, s82, v3\n\t"
    "v_lshrrev_b32 v24, 2, v12\n\t"
    "v_cmp_eq_u32 vcc, v24, v13\n\t"
    "v_and_b32 v25, 1, v12\n\t"
    "v_lshlrev_b32 v25, 4, v25\n\t"
    "v_mov_b32 v26, 0x3f80\n\t"
    "v_lshlrev_b32 v25, v25, v26\n\t"
    "v_mov_b32 v26, 0\n\t"
    "v_cndmask_b32 v25, v26, v25, vcc\n\t"
    "v_and_b32 v24, 2, v12\n\t"
    "v_cmp_eq_u32 vcc, 0, v24\n\t"
    "v_mov_b32 v27, 0\n\t"
    "s_nop 0\n\t"
    "v_cndmask_b32 v24, v26, v25, vcc\n\t"
    "v_cndmask_b32 v25, v25, v26, vcc\n\t"
    "v_mov_b32 v28, v24\n\t"
    "v_mov_b32 v29, v25\n\t"
    "s_cmp_lt_u32 s64, 3\n\t"
    "s_cbranch_scc0 CEND_%=\n\t"
    "s_cmp_eq_u32 s64, 0\n\t"
    "s_cbranch_scc0 TAILQ_%=\n\t"
    "s_add_u32 s83, s81, s52\n\t"
    "s_cmp_ge_u32 s83, 9\n\t"
    "s_cselect_b32 s82, 9, 0\n\t"
    "s_sub_u32 s83, s83, s82\n\t"
    "s_lshl_b32 s89, s83, 18\n\t"
    "s_lshl_b32 s84, s49, 10\n\t"
    "s_add_u32 s89, s89, s84\n\t"
    "s_lshl_b32 s83, s83, 7\n\t"
    "v_lshrrev_b32 v22, 2, v11\n\t"
    "v_and_b32 v23, 3, v11\n\t"
    "v_lshlrev_b32 v22, 14, v22\n\t"
    "v_lshl_add_u32 v22, v23, 8, v22\n\t"
    "s_mov_b32 s66, 0\n\t"
    "TAILL_%=:\n\t"
    "buffer_load_dword v40, v22, s[36:39], s89 offen offset:252 sc1\n\t"
    "s_waitcnt vmcnt(0)\n\t"
    "v_cmp_eq_u32 vcc, -1, v40\n\t"
    "s_cmp_eq_u64 vcc, 0\n\t"
    "s_cbranch_scc1 TAILP_%=\n\t"
    "s_sleep 1\n\t"
    "s_add_u32 s66, s66, 1\n\t"
    "s_cmp_lt_u32 s66, 0x40000\n\t"
    "s_cbranch_scc1 TAILL_%=\n\t"
    "TAILP_%=:\n\t"
    "s_add_u32 s83, s81, s52\n\t"
    "s_cmp_ge_u32 s83, 9\n\t"
    "s_cselect_b32 s82, 9, 0\n\t"
    "s_sub_u32 s83, s83, s82\n\t"
    "s_lshl_b32 s89, s83, 18\n\t"
    "s_lshl_b32 s84, s49, 10\n\t"
    "s_add_u32 s89, s89, s84\n\t"
    "s_lshl_b32 s83, s83, 7\n\t"
    "buffer_load_dwordx4 v[64:67], v0, s[36:39], s89 offen sc1\n\t"
    "s_add_u32 s89, s89, 16384\n\t"
    "buffer_load_dwordx4 v[68:71], v0, s[36:39], s89 offen sc1\n\t"
    "s_add_u32 s89, s89, 16384\n\t"
    "buffer_load_dwordx4 v[72:75], v0, s[36:39], s89 offen sc1\n\t"
    "s_add_u32 s89, s89, 16384\n\t"
    "buffer_load_dwordx4 v[76:79], v0, s[36:39], s89 offen sc1\n\t"
    "s_add_u32 s89, s89, 16384\n\t"
    "buffer_load_dwordx4 v[80:83], v0, s[36:39], s89 offen sc1\n\t"
    "s_add_u32 s89, s89, 16384\n\t"
    "buffer_load_dwordx4 v[84:87], v0, s[36:39], s89 offen sc1\n\t"
    "s_add_u32 s89, s89, 16384\n\t"
    "buffer_load_dwordx4 v[88:91], v0, s[36:39], s89 offen sc1\n\t"
    "s_add_u32 s89, s89, 16384\n\t"
    "buffer_load_dwordx4 v[92:95], v0, s[36:39], s89 offen sc1\n\t"
    "s_add_u32 s89, s89, 16384\n\t"
    "buffer_load_dwordx4 v[96:99], v0, s[36:39], s89 offen sc1\n\t"
    "s_add_u32 s89, s89, 16384\n\t"
    "buffer_load_dwordx4 v[100:103], v0, s[36:39], s89 offen sc1\n\t"
    "s_add_u32 s89, s89, 16384\n\t"
    "buffer_load_dwordx4 v[104:107], v0, s[36:39], s89 offen sc1\n\t"
    "s_add_u32 s89, s89, 16384\n\t"
    "buffer_load_dwordx4 v[108:111], v0, s[36:39], s89 offen sc1\n\t"
    "s_add_u32 s89, s89, 16384\n\t"
    "buffer_load_dwordx4 v[112:115], v0, s[36:39], s89 offen sc1\n\t"
    "s_add_u32 s89, s89, 16384\n\t"
    "buffer_load_dwordx4 v[116:119], v0, s[36:39], s89 offen sc1\n\t"
    "s_add_u32 s89, s89, 16384\n\t"
    "buffer_load_dwordx4 v[120:123], v0, s[36:39], s89 offen sc1\n\t"
    "s_add_u32 s89, s89, 16384\n\t"
    "buffer_load_dwordx4 v[124:127], v0, s[36:39], s89 offen sc1\n\t"
    "s_add_u32 s89, s89, 16384\n\t"
    "v_mov_b32 v21, 0\n\t"
    "s_waitcnt vmcnt(15)\n\t"
    "v_mfma_f32_16x16x32_bf16 v[32:35], v[24:27], v[64:67], 0\n\t"
    "v_max3_u32 v21, v64, v65, v21\n\t"
    "v_mfma_f32_16x16x32_bf16 v[36:39], v[26:29], v[64:67], 0\n\t"
    "v_max3_u32 v21, v66, v67, v21\n\t"
    "s_waitcnt vmcnt(14)\n\t"
    "v_mfma_f32_16x16x32_bf16 v[32:35], v[24:27], v[68:71], v[32:35]\n\t"
    "v_max3_u32 v21, v68, v69, v21\n\t"
    "v_mfma_f32_16x16x32_bf16 v[36:39], v[26:29], v[68:71], v[36:39]\n\t"
    "v_max3_u32 v21, v70, v71, v21\n\t"
    "s_waitcnt vmcnt(13)\n\t"
    "v_mfma_f32_16x16x32_bf16 v[32:35], v[24:27], v[72:75], v[32:35]\n\t"
    "v_max3_u32 v21, v72, v73, v21\n\t"
    "v_mfma_f32_16x16x32_bf16 v[36:39], v[26:29], v[72:75], v[36:39]\n\t"
    "v_max3_u32 v21, v74, v75, v21\n\t"
    "s_waitcnt vmcnt(12)\n\t"
    "v_mfma_f32_16x16x32_bf16 v[32:35], v[24:27], v[76:79], v[32:35]\n\t"
    "v_max3_u32 v21, v76, v77, v21\n\t"
    "v_mfma_f32_16x16x32_bf16 v[36:39], v[26:29], v[76:79], v[36:39]\n\t"
    "v_max3_u32 v21, v78, v79, v21\n\t"
    "s_waitcnt vmcnt(11)\n\t"
    "v_mfma_f32_16x16x32_bf16 v[32:35], v[24:27], v[80:83], v[32:35]\n\t"
    "v_max3_u32 v21, v80, v81, v21\n\t"
    "v_mfma_f32_16x16x32_bf16 v[36:39], v[26:29], v[80:83], v[36:39]\n\t"
    "v_max3_u32 v21, v82, v83, v21\n\t"
    "s_waitcnt vmcnt(10)\n\t"
    "v_mfma_f32_16x16x32_bf16 v[32:35], v[24:27], v[84:87], v[32:35]\n\t"
    "v_max3_u32 v21, v84, v85, v21\n\t"
    "v_mfma_f32_16x16x32_bf16 v[36:39], v[26:29], v[84:87], v[36:39]\n\t"
    "v_max3_u32 v21, v86, v87, v21\n\t"
    "s_waitcnt vmcnt(9)\n\t"
    "v_mfma_f32_16x16x32_bf16 v[32:35], v[24:27], v[88:91], v[32:35]\n\t"
    "v_max3_u32 v21, v88, v89, v21\n\t"
    "v_mfma_f32_16x16x32_bf16 v[36:39], v[26:29], v[88:91], v[36:39]\n\t"
    "v_max3_u32 v21, v90, v91, v21\n\t"
    "s_waitcnt vmcnt(8)\n\t"
    "v_mfma_f32_16x16x32_bf16 v[32:35], v[24:27], v[92:95], v[32:35]\n\t"
    "v_max3_u32 v21, v92, v93, v21\n\t"
    "v_mfma_f32_16x16x32_bf16 v[36:39], v[26:29], v[92:95], v[36:39]\n\t"
    "v_max3_u32 v21, v94, v95, v21\n\t"
    "s_waitcnt vmcnt(7)\n\t"
    "v_mfma_f32_16x16x32_bf16 v[32:35], v[24:27], v[96:99], v[32:35]\n\t"
    "v_max3_u32 v21, v96, v97, v21\n\t"
    "v_mfma_f32_16x16x32_bf16 v[36:39], v[26:29], v[96:99], v[36:39]\n\t"
    "v_max3_u32 v21, v98, v99, v21\n\t"
    "s_waitcnt vmcnt(6)\n\t"
    "v_mfma_f32_16x16x32_bf16 v[32:35], v[24:27], v[100:103], v[32:35]\n\t"
    "v_max3_u32 v21, v100, v101, v21\n\t"
    "v_mfma_f32_16x16x32_bf16 v[36:39], v[26:29], v[100:103], v[36:39]\n\t"
    "v_max3_u32 v21, v102, v103, v21\n\t"
    "s_waitcnt vmcnt(5)\n\t"
    "v_mfma_f32_16x16x32_bf16 v[32:35], v[24:27], v[104:107], v[32:35]\n\t"
    "v_max3_u32 v21, v104, v105, v21\n\t"
    "v_mfma_f32_16x16x32_bf16 v[36:39], v[26:29], v[104:107], v[36:39]\n\t"
    "v_max3_u32 v21, v106, v107, v21\n\t"
    "s_waitcnt vmcnt(4)\n\t"
    "v_mfma_f32_16x16x32_bf16 v[32:35], v[24:27], v[108:111], v[32:35]\n\t"
    "v_max3_u32 v21, v108, v109, v21\n\t"
    "v_mfma_f32_16x16x32_bf16 v[36:39], v[26:29], v[108:111], v[36:39]\n\t"
    "v_max3_u32 v21, v110, v111, v21\n\t"
    "s_waitcnt vmcnt(3)\n\t"
    "v_mfma_f32_16x16x32_bf16 v[32:35], v[24:27], v[112:115], v[32:35]\n\t"
    "v_max3_u32 v21, v112, v113, v21\n\t"
    "v_mfma_f32_16x16x32_bf16 v[36:39], v[26:29], v[112:115], v[36:39]\n\t"
    "v_max3_u32 v21, v114, v115, v21\n\t"
    "s_waitcnt vmcnt(2)\n\t"
    "v_mfma_f32_16x16x32_bf16 v[32:35], v[24:27], v[116:119], v[32:35]\n\t"
    "v_max3_u32 v21, v116, v117, v21\n\t"
    "v_mfma_f32_16x16x32_bf16 v[36:39], v[26:29], v[116:119], v[36:39]\n\t"
    "v_max3_u32 v21, v118, v119, v21\n\t"
    "s_waitcnt vmcnt(1)\n\t"
    "v_mfma_f32_16x16x32_bf16 v[32:35], v[24:27], v[120:123], v[32:35]\n\t"
    "v_max3_u32 v21, v120, v121, v21\n\t"
    "v_mfma_f32_16x16x32_bf16 v[36:39], v[26:29], v[120:123], v[36:39]\n\t"
    "v_max3_u32 v21, v122, v123, v21\n\t"
    "s_waitcnt vmcnt(0)\n\t"
    "v_mfma_f32_16x16x32_bf16 v[32:35], v[24:27], v[124:127], v[32:35]\n\t"
    "v_max3_u32 v21, v124, v125, v21\n\t"
    "v_mfma_f32_16x16x32_bf16 v[36:39], v[26:29], v[124:127], v[36:39]\n\t"
    "v_max3_u32 v21, v126, v127, v21\n\t"
    "v_cmp_eq_u32 vcc, -1, v21\n\t"
    "s_add_u32 s66, s66, 1\n\t"
    "s_cmp_eq_u64 vcc, 0\n\t"
    "s_cbranch_scc1 TAILD_%=\n\t"
    "s_cmp_lt_u32 s66, 0x40000\n\t"
    "s_cbranch_scc1 TAILP_%=\n\t"
    "TAILD_%=:\n\t"
    "s_nop 7\n\t"
    "v_cvt_pk_bf16_f32 v48, v32, v33\n\t"
    "v_cvt_pk_bf16_f32 v49, v34, v35\n\t"
    "v_cvt_pk_bf16_f32 v50, v36, v37\n\t"
    "v_cvt_pk_bf16_f32 v51, v38, v39\n\t"
    "s_nop 1\n\t"
    "v_permlane16_swap_b32 v48, v50\n\t"
    "v_permlane16_swap_b32 v49, v51\n\t"
    "buffer_store_dwordx4 v[48:51], v3, s[44:47], s83 offen\n\t"
    "s_branch CEND_%=\n\t"
    "TAILQ_%=:\n\t"
    "s_lshl_b32 s82, s81, 8\n\t"
    "v_add_u32 v2, s82, v5\n\t"
    "buffer_load_dword v20, v2, s[40:43], 0 offen sc1\n\t"
    "TAILW_%=:\n\t"
    "s_mov_b32 s66, 0\n\t"
    "s_branch PWt_%=\n\t"
    "PLt_%=:\n\t"
    "buffer_load_dword v20, v2, s[40:43], 0 offen sc1\n\t"
    "PWt_%=:\n\t"
    "s_waitcnt vmcnt(0)\n\t"
    "v_cmp_ne_u32 vcc, s80, v20\n\t"
    "s_add_u32 s66, s66, 1\n\t"
    "s_cmp_eq_u64 vcc, 0\n\t"
    "s_cbranch_scc1 PDt_%=\n\t"
    "s_sleep 1\n\t"
    "s_cmp_lt_u32 s66, 0x40000\n\t"
    "s_cbranch_scc1 PLt_%=\n\t"
    "PDt_%=:\n\t"
    "s_add_u32 s83, s81, s52\n\t"
    "s_cmp_ge_u32 s83, 9\n\t"
    "s_cselect_b32 s82, 9, 0\n\t"
    "s_sub_u32 s83, s83, s82\n\t"
    "s_lshl_b32 s89, s83, 18\n\t"
    "s_lshl_b32 s84, s49, 10\n\t"
    "s_add_u32 s89, s89, s84\n\t"
    "s_lshl_b32 s83, s83, 7\n\t"
    "buffer_load_dwordx4 v[64:67], v0, s[36:39], s89 offen sc1\n\t"
    "s_add_u32 s89, s89, 16384\n\t"
    "buffer_load_dwordx4 v[68:71], v0, s[36:39], s89 offen sc1\n\t"
    "s_add_u32 s89, s89, 16384\n\t"
    "buffer_load_dwordx4 v[72:75], v0, s[36:39], s89 offen sc1\n\t"
    "s_add_u32 s89, s89, 16384\n\t"
    "buffer_load_dwordx4 v[76:79], v0, s[36:39], s89 offen sc1\n\t"
    "s_add_u32 s89, s89, 16384\n\t"
    "buffer_load_dwordx4 v[80:83], v0, s[36:39], s89 offen sc1\n\t"
    "s_add_u32 s89, s89, 16384\n\t"
    "buffer_load_dwordx4 v[84:87], v0, s[36:39], s89 offen sc1\n\t"
    "s_add_u32 s89, s89, 16384\n\t"
    "buffer_load_dwordx4 v[88:91], v0, s[36:39], s89 offen sc1\n\t"
    "s_add_u32 s89, s89, 16384\n\t"
    "buffer_load_dwordx4 v[92:95], v0, s[36:39], s89 offen sc1\n\t"
    "s_add_u32 s89, s89, 16384\n\t"
    "buffer_load_dwordx4 v[96:99], v0, s[36:39], s89 offen sc1\n\t"
    "s_add_u32 s89, s89, 16384\n\t"
    "buffer_load_dwordx4 v[100:103], v0, s[36:39], s89 offen sc1\n\t"
    "s_add_u32 s89, s89, 16384\n\t"
    "buffer_load_dwordx4 v[104:107], v0, s[36:39], s89 offen sc1\n\t"
    "s_add_u32 s89, s89, 16384\n\t"
    "buffer_load_dwordx4 v[108:111], v0, s[36:39], s89 offen sc1\n\t"
    "s_add_u32 s89, s89, 16384\n\t"
    "buffer_load_dwordx4 v[112:115], v0, s[36:39], s89 offen sc1\n\t"
    "s_add_u32 s89, s89, 16384\n\t"
    "buffer_load_dwordx4 v[116:119], v0, s[36:39], s89 offen sc1\n\t"
    "s_add_u32 s89, s89, 16384\n\t"
    "buffer_load_dwordx4 v[120:123], v0, s[36:39], s89 offen sc1\n\t"
    "s_add_u32 s89, s89, 16384\n\t"
    "buffer_load_dwordx4 v[124:127], v0, s[36:39], s89 offen sc1\n\t"
    "s_add_u32 s89, s89, 16384\n\t"
    "s_waitcnt vmcnt(15)\n\t"
    "v_mfma_f32_16x16x32_bf16 v[32:35], v[24:27], v[64:67], 0\n\t"
    "v_mfma_f32_16x16x32_bf16 v[36:39], v[26:29], v[64:67], 0\n\t"
    "s_waitcnt vmcnt(14)\n\t"
    "v_mfma_f32_16x16x32_bf16 v[32:35], v[24:27], v[68:71], v[32:35]\n\t"
    "v_mfma_f32_16x16x32_bf16 v[36:39], v[26:29], v[68:71], v[36:39]\n\t"
    "s_waitcnt vmcnt(13)\n\t"
    "v_mfma_f32_16x16x32_bf16 v[32:35], v[24:27], v[72:75], v[32:35]\n\t"
    "v_mfma_f32_16x16x32_bf16 v[36:39], v[26:29], v[72:75], v[36:39]\n\t"
    "s_waitcnt vmcnt(12)\n\t"
    "v_mfma_f32_16x16x32_bf16 v[32:35], v[24:27], v[76:79], v[32:35]\n\t"
    "v_mfma_f32_16x16x32_bf16 v[36:39], v[26:29], v[76:79], v[36:39]\n\t"
    "s_waitcnt vmcnt(11)\n\t"
    "v_mfma_f32_16x16x32_bf16 v[32:35], v[24:27], v[80:83], v[32:35]\n\t"
    "v_mfma_f32_16x16x32_bf16 v[36:39], v[26:29], v[80:83], v[36:39]\n\t"
    "s_waitcnt vmcnt(10)\n\t"
    "v_mfma_f32_16x16x32_bf16 v[32:35], v[24:27], v[84:87], v[32:35]\n\t"
    "v_mfma_f32_16x16x32_bf16 v[36:39], v[26:29], v[84:87], v[36:39]\n\t"
    "s_waitcnt vmcnt(9)\n\t"
    "v_mfma_f32_16x16x32_bf16 v[32:35], v[24:27], v[88:91], v[32:35]\n\t"
    "v_mfma_f32_16x16x32_bf16 v[36:39], v[26:29], v[88:91], v[36:39]\n\t"
    "s_waitcnt vmcnt(8)\n\t"
    "v_mfma_f32_16x16x32_bf16 v[32:35], v[24:27], v[92:95], v[32:35]\n\t"
    "v_mfma_f32_16x16x32_bf16 v[36:39], v[26:29], v[92:95], v[36:39]\n\t"
    "s_waitcnt vmcnt(7)\n\t"
    "v_mfma_f32_16x16x32_bf16 v[32:35], v[24:27], v[96:99], v[32:35]\n\t"
    "v_mfma_f32_16x16x32_bf16 v[36:39], v[26:29], v[96:99], v[36:39]\n\t"
    "s_waitcnt vmcnt(6)\n\t"
    "v_mfma_f32_16x16x32_bf16 v[32:35], v[24:27], v[100:103], v[32:35]\n\t"
    "v_mfma_f32_16x16x32_bf16 v[36:39], v[26:29], v[100:103], v[36:39]\n\t"
    "s_waitcnt vmcnt(5)\n\t"
    "v_mfma_f32_16x16x32_bf16 v[32:35], v[24:27], v[104:107], v[32:35]\n\t"
    "v_mfma_f32_16x16x32_bf16 v[36:39], v[26:29], v[104:107], v[36:39]\n\t"
    "s_waitcnt vmcnt(4)\n\t"
    "v_mfma_f32_16x16x32_bf16 v[32:35], v[24:27], v[108:111], v[32:35]\n\t"
    "v_mfma_f32_16x16x32_bf16 v[36:39], v[26:29], v[108:111], v[36:39]\n\t"
    "s_waitcnt vmcnt(3)\n\t"
    "v_mfma_f32_16x16x32_bf16 v[32:35], v[24:27], v[112:115], v[32:35]\n\t"
    "v_mfma_f32_16x16x32_bf16 v[36:39], v[26:29], v[112:115], v[36:39]\n\t"
    "s_waitcnt vmcnt(2)\n\t"
    "v_mfma_f32_16x16x32_bf16 v[32:35], v[24:27], v[116:119], v[32:35]\n\t"
    "v_mfma_f32_16x16x32_bf16 v[36:39], v[26:29], v[116:119], v[36:39]\n\t"
    "s_waitcnt vmcnt(1)\n\t"
    "v_mfma_f32_16x16x32_bf16 v[32:35], v[24:27], v[120:123], v[32:35]\n\t"
    "v_mfma_f32_16x16x32_bf16 v[36:39], v[26:29], v[120:123], v[36:39]\n\t"
    "s_waitcnt vmcnt(0)\n\t"
    "v_mfma_f32_16x16x32_bf16 v[32:35], v[24:27], v[124:127], v[32:35]\n\t"
    "v_mfma_f32_16x16x32_bf16 v[36:39], v[26:29], v[124:127], v[36:39]\n\t"
    "s_nop 7\n\t"
    "v_cvt_pk_bf16_f32 v48, v32, v33\n\t"
    "v_cvt_pk_bf16_f32 v49, v34, v35\n\t"
    "v_cvt_pk_bf16_f32 v50, v36, v37\n\t"
    "v_cvt_pk_bf16_f32 v51, v38, v39\n\t"
    "s_nop 1\n\t"
    "v_permlane16_swap_b32 v48, v50\n\t"
    "v_permlane16_swap_b32 v49, v51\n\t"
    "buffer_store_dwordx4 v[48:51], v3, s[44:47], s83 offen\n\t"
    "CEND_%=:\n\t"
    "s_waitcnt vmcnt(0)\n\t"
    "s_branch END_%=\n\t"
    "HELP_%=:\n\t"
    "s_cmp_lt_u32 s64, 6\n\t"
    "s_cbranch_scc0 AUX_%=\n\t"
    "s_sub_u32 s65, s64, 4\n\t"
    "v_lshrrev_b32 v12, 3, v11\n\t"
    "v_and_b32 v13, 7, v11\n\t"
    "v_mul_u32_u24 v14, 1152, v12\n\t"
    "v_bfe_u32 v15, v12, 1, 1\n\t"
    "v_add_u32 v15, 0, v15\n\t"
    "v_xor_b32 v15, v15, v13\n\t"
    "v_lshl_add_u32 v1, v15, 4, v14\n\t"
    "v_bfe_u32 v15, v12, 1, 1\n\t"
    "v_add_u32 v15, 2, v15\n\t"
    "v_xor_b32 v15, v15, v13\n\t"
    "v_lshl_add_u32 v2, v15, 4, v14\n\t"
    "v_bfe_u32 v15, v12, 1, 1\n\t"
    "v_add_u32 v15, 4, v15\n\t"
    "v_xor_b32 v15, v15, v13\n\t"
    "v_lshl_add_u32 v3, v15, 4, v14\n\t"
    "v_bfe_u32 v15, v12, 1, 1\n\t"
    "v_add_u32 v15, 6, v15\n\t"
    "v_xor_b32 v15, v15, v13\n\t"
    "v_lshl_add_u32 v4, v15, 4, v14\n\t"
    "v_lshrrev_b32 v15, 1, v12\n\t"
    "v_add_u32 v15, 0, v15\n\t"
    "v_xor_b32 v15, v15, v13\n\t"
    "v_lshl_add_u32 v5, v15, 4, v14\n\t"
    "v_lshrrev_b32 v15, 1, v12\n\t"
    "v_add_u32 v15, 4, v15\n\t"
    "v_xor_b32 v15, v15, v13\n\t"
    "v_lshl_add_u32 v6, v15, 4, v14\n\t"
    "v_lshlrev_b32 v7, 4, v15\n\t"
    "s_mov_b32 s69, 0\n\t"
    "s_mov_b32 s68, 0\n\t"
    "s_mov_b32 s76, s51\n\t"
    "s_lshl_b32 s72, s65, 13\n\t"
    "s_mul_i32 s77, s65, 73728\n\t"
    "s_mul_i32 s74, s65, 5120\n\t"
    "s_mul_i32 s78, s65, 46080\n\t"
    "LLA_%=:\n\t"
    "s_cmp_lt_u32 s68, 4\n\t"
    "s_cbranch_scc1 LLA0_%=\n\t"
    "s_waitcnt vmcnt(26)\n\t"
    "s_barrier\n\t"
    "s_branch LLA1_%=\n\t"
    "LLA0_%=:\n\t"
    "s_cmp_eq_u32 s68, 2\n\t"
    "s_cbranch_scc0 LLA1_%=\n\t"
    "s_waitcnt vmcnt(13)\n\t"
    "s_barrier\n\t"
    "LLA1_%=:\n\t"
    "s_lshl_b32 s79, s76, 7\n\t"
    "s_add_u32 s73, s77, s79\n\t"
    "s_add_u32 s75, s78, s79\n\t"
    "s_add_u32 s70, s69, s72\n\t"
    "s_add_u32 m0, s70, 0\n\t"
    "s_add_u32 s71, s73, 0\n\t"
    "buffer_load_dwordx4 v1, s[8:11], s71 offen lds\n\t"
    "s_add_u32 m0, s70, 1024\n\t"
    "s_add_u32 s71, s73, 9216\n\t"
    "buffer_load_dwordx4 v2, s[8:11], s71 offen lds\n\t"
    "s_add_u32 m0, s70, 2048\n\t"
    "s_add_u32 s71, s73, 18432\n\t"
    "buffer_load_dwordx4 v3, s[8:11], s71 offen lds\n\t"
    "s_add_u32 m0, s70, 3072\n\t"
    "s_add_u32 s71, s73, 27648\n\t"
    "buffer_load_dwordx4 v4, s[8:11], s71 offen lds\n\t"
    "s_add_u32 m0, s70, 4096\n\t"
    "s_add_u32 s71, s73, 36864\n\t"
    "buffer_load_dwordx4 v1, s[8:11], s71 offen lds\n\t"
    "s_add_u32 m0, s70, 5120\n\t"
    "s_add_u32 s71, s73, 46080\n\t"
    "buffer_load_dwordx4 v2, s[8:11], s71 offen lds\n\t"
    "s_add_u32 m0, s70, 6144\n\t"
    "s_add_u32 s71, s73, 55296\n\t"
    "buffer_load_dwordx4 v3, s[8:11], s71 offen lds\n\t"
    "s_add_u32 m0, s70, 7168\n\t"
    "s_add_u32 s71, s73, 64512\n\t"
    "buffer_load_dwordx4 v4, s[8:11], s71 offen lds\n\t"
    "s_add_u32 s70, s69, s74\n\t"
    "s_cmp_eq_u32 s65, 0\n\t"
    "s_cbranch_scc0 LLA2_%=\n\t"
    "s_cmp_lt_u32 s68, 1\n\t"
    "s_cbranch_scc0 NTha_%=\n\t"
    "s_add_u32 m0, s70, 16384\n\t"
    "s_add_u32 s71, s75, 0\n\t"
    "buffer_load_dwordx4 v5, s[20:23], s71 offen nt lds\n\t"
    "s_add_u32 m0, s70, 17408\n\t"
    "s_add_u32 s71, s75, 9216\n\t"
    "buffer_load_dwordx4 v6, s[20:23], s71 offen nt lds\n\t"
    "s_add_u32 m0, s70, 18432\n\t"
    "s_add_u32 s71, s75, 18432\n\t"
    "buffer_load_dwordx4 v5, s[20:23], s71 offen nt lds\n\t"
    "s_add_u32 m0, s70, 19456\n\t"
    "s_add_u32 s71, s75, 27648\n\t"
    "buffer_load_dwordx4 v6, s[20:23], s71 offen nt lds\n\t"
    "s_add_u32 m0, s70, 20480\n\t"
    "s_add_u32 s71, s75, 36864\n\t"
    "buffer_load_dwordx4 v5, s[20:23], s71 offen nt lds\n\t"
    "s_branch NThb_%=\n\t"
    "NTha_%=:\n\t"
    "s_add_u32 m0, s70, 16384\n\t"
    "s_add_u32 s71, s75, 0\n\t"
    "buffer_load_dwordx4 v5, s[20:23], s71 offen lds\n\t"
    "s_add_u32 m0, s70, 17408\n\t"
    "s_add_u32 s71, s75, 9216\n\t"
    "buffer_load_dwordx4 v6, s[20:23], s71 offen lds\n\t"
    "s_add_u32 m0, s70, 18432\n\t"
    "s_add_u32 s71, s75, 18432\n\t"
    "buffer_load_dwordx4 v5, s[20:23], s71 offen lds\n\t"
    "s_add_u32 m0, s70, 19456\n\t"
    "s_add_u32 s71, s75, 27648\n\t"
    "buffer_load_dwordx4 v6, s[20:23], s71 offen lds\n\t"
    "s_add_u32 m0, s70, 20480\n\t"
    "s_add_u32 s71, s75, 36864\n\t"
    "buffer_load_dwordx4 v5, s[20:23], s71 offen lds\n\t"
    "NThb_%=:\n\t"
    "s_branch LLA3_%=\n\t"
    "LLA2_%=:\n\t"
    "s_cmp_lt_u32 s68, 1\n\t"
    "s_cbranch_scc0 NTia_%=\n\t"
    "s_add_u32 m0, s70, 16384\n\t"
    "s_add_u32 s71, s75, 0\n\t"
    "buffer_load_dwordx4 v6, s[20:23], s71 offen nt lds\n\t"
    "s_add_u32 m0, s70, 17408\n\t"
    "s_add_u32 s71, s75, 9216\n\t"
    "buffer_load_dwordx4 v5, s[20:23], s71 offen nt lds\n\t"
    "s_add_u32 m0, s70, 18432\n\t"
    "s_add_u32 s71, s75, 18432\n\t"
    "buffer_load_dwordx4 v6, s[20:23], s71 offen nt lds\n\t"
    "s_add_u32 m0, s70, 19456\n\t"
    "s_add_u32 s71, s75, 27648\n\t"
    "buffer_load_dwordx4 v5, s[20:23], s71 offen nt lds\n\t"
    "s_branch NTib_%=\n\t"
    "NTia_%=:\n\t"
    "s_add_u32 m0, s70, 16384\n\t"
    "s_add_u32 s71, s75, 0\n\t"
    "buffer_load_dwordx4 v6, s[20:23], s71 offen lds\n\t"
    "s_add_u32 m0, s70, 17408\n\t"
    "s_add_u32 s71, s75, 9216\n\t"
    "buffer_load_dwordx4 v5, s[20:23], s71 offen lds\n\t"
    "s_add_u32 m0, s70, 18432\n\t"
    "s_add_u32 s71, s75, 18432\n\t"
    "buffer_load_dwordx4 v6, s[20:23], s71 offen lds\n\t"
    "s_add_u32 m0, s70, 19456\n\t"
    "s_add_u32 s71, s75, 27648\n\t"
    "buffer_load_dwordx4 v5, s[20:23], s71 offen lds\n\t"
    "NTib_%=:\n\t"
    "s_add_u32 m0, s70, 20480\n\t"
    "s_mov_b32 s71, s79\n\t"
    "buffer_load_dwordx4 v7, s[24:27], s71 offen lds\n\t"
    "LLA3_%=:\n\t"
    "s_cmp_eq_u32 s68, 3\n\t"
    "s_cbranch_scc0 LLA4_%=\n\t"
    "v_bfe_u32 v15, v12, 1, 2\n\t"
    "v_lshlrev_b32 v15, 1, v15\n\t"
    "v_xor_b32 v15, v15, v13\n\t"
    "v_lshl_add_u32 v8, v15, 4, v14\n\t"
    "v_lshlrev_b32 v9, 12, v12\n\t"
    "v_bfe_u32 v15, v12, 1, 1\n\t"
    "v_add_u32 v15, 0, v15\n\t"
    "v_xor_b32 v15, v15, v13\n\t"
    "v_lshl_add_u32 v26, v15, 4, v9\n\t"
    "v_bfe_u32 v15, v12, 1, 1\n\t"
    "v_add_u32 v15, 2, v15\n\t"
    "v_xor_b32 v15, v15, v13\n\t"
    "v_lshl_add_u32 v27, v15, 4, v9\n\t"
    "v_bfe_u32 v15, v12, 1, 1\n\t"
    "v_add_u32 v15, 4, v15\n\t"
    "v_xor_b32 v15, v15, v13\n\t"
    "v_lshl_add_u32 v28, v15, 4, v9\n\t"
    "v_bfe_u32 v15, v12, 1, 1\n\t"
    "v_add_u32 v15, 6, v15\n\t"
    "v_xor_b32 v15, v15, v13\n\t"
    "v_lshl_add_u32 v29, v15, 4, v9\n\t"
    "s_mul_i32 s66, s65, 576\n\t"
    "s_add_u32 s67, s66, 0\n\t"
    "v_add_u32 v12, s67, v11\n\t"
    "v_mul_u32_u24 v13, 1821, v12\n\t"
    "v_lshrrev_b32 v13, 16, v13\n\t"
    "v_mul_u32_u24 v14, 36, v13\n\t"
    "v_sub_u32 v12, v12, v14\n\t"
    "v_bfe_u32 v14, v13, 3, 1\n\t"
    "v_lshlrev_b32 v14, 1, v14\n\t"
    "v_xor_b32 v12, v12, v14\n\t"
    "v_mul_u32_u24 v13, 9216, v13\n\t"
    "v_lshl_add_u32 v16, v12, 4, v13\n\t"
    "s_add_u32 s67, s66, 64\n\t"
    "v_add_u32 v12, s67, v11\n\t"
    "v_mul_u32_u24 v13, 1821, v12\n\t"
    "v_lshrrev_b32 v13, 16, v13\n\t"
    "v_mul_u32_u24 v14, 36, v13\n\t"
    "v_sub_u32 v12, v12, v14\n\t"
    "v_bfe_u32 v14, v13, 3, 1\n\t"
    "v_lshlrev_b32 v14, 1, v14\n\t"
    "v_xor_b32 v12, v12, v14\n\t"
    "v_mul_u32_u24 v13, 9216, v13\n\t"
    "v_lshl_add_u32 v17, v12, 4, v13\n\t"
    "s_add_u32 s67, s66, 128\n\t"
    "v_add_u32 v12, s67, v11\n\t"
    "v_mul_u32_u24 v13, 1821, v12\n\t"
    "v_lshrrev_b32 v13, 16, v13\n\t"
    "v_mul_u32_u24 v14, 36, v13\n\t"
    "v_sub_u32 v12, v12, v14\n\t"
    "v_bfe_u32 v14, v13, 3, 1\n\t"
    "v_lshlrev_b32 v14, 1, v14\n\t"
    "v_xor_b32 v12, v12, v14\n\t"
    "v_mul_u32_u24 v13, 9216, v13\n\t"
    "v_lshl_add_u32 v18, v12, 4, v13\n\t"
    "s_add_u32 s67, s66, 192\n\t"
    "v_add_u32 v12, s67, v11\n\t"
    "v_mul_u32_u24 v13, 1821, v12\n\t"
    "v_lshrrev_b32 v13, 16, v13\n\t"
    "v_mul_u32_u24 v14, 36, v13\n\t"
    "v_sub_u32 v12, v12, v14\n\t"
    "v_bfe_u32 v14, v13, 3, 1\n\t"
    "v_lshlrev_b32 v14, 1, v14\n\t"
    "v_xor_b32 v12, v12, v14\n\t"
    "v_mul_u32_u24 v13, 9216, v13\n\t"
    "v_lshl_add_u32 v19, v12, 4, v13\n\t"
    "s_add_u32 s67, s66, 256\n\t"
    "v_add_u32 v12, s67, v11\n\t"
    "v_mul_u32_u24 v13, 1821, v12\n\t"
    "v_lshrrev_b32 v13, 16, v13\n\t"
    "v_mul_u32_u24 v14, 36, v13\n\t"
    "v_sub_u32 v12, v12, v14\n\t"
    "v_bfe_u32 v14, v13, 3, 1\n\t"
    "v_lshlrev_b32 v14, 1, v14\n\t"
    "v_xor_b32 v12, v12, v14\n\t"
    "v_mul_u32_u24 v13, 9216, v13\n\t"
    "v_lshl_add_u32 v20, v12, 4, v13\n\t"
    "s_add_u32 s67, s66, 320\n\t"
    "v_add_u32 v12, s67, v11\n\t"
    "v_mul_u32_u24 v13, 1821, v12\n\t"
    "v_lshrrev_b32 v13, 16, v13\n\t"
    "v_mul_u32_u24 v14, 36, v13\n\t"
    "v_sub_u32 v12, v12, v14\n\t"
    "v_bfe_u32 v14, v13, 3, 1\n\t"
    "v_lshlrev_b32 v14, 1, v14\n\t"
    "v_xor_b32 v12, v12, v14\n\t"
    "v_mul_u32_u24 v13, 9216, v13\n\t"
    "v_lshl_add_u32 v21, v12, 4, v13\n\t"
    "s_add_u32 s67, s66, 384\n\t"
    "v_add_u32 v12, s67, v11\n\t"
    "v_mul_u32_u24 v13, 1821, v12\n\t"
    "v_lshrrev_b32 v13, 16, v13\n\t"
    "v_mul_u32_u24 v14, 36, v13\n\t"
    "v_sub_u32 v12, v12, v14\n\t"
    "v_bfe_u32 v14, v13, 3, 1\n\t"
    "v_lshlrev_b32 v14, 1, v14\n\t"
    "v_xor_b32 v12, v12, v14\n\t"
    "v_mul_u32_u24 v13, 9216, v13\n\t"
    "v_lshl_add_u32 v22, v12, 4, v13\n\t"
    "s_add_u32 s67, s66, 448\n\t"
    "v_add_u32 v12, s67, v11\n\t"
    "v_mul_u32_u24 v13, 1821, v12\n\t"
    "v_lshrrev_b32 v13, 16, v13\n\t"
    "v_mul_u32_u24 v14, 36, v13\n\t"
    "v_sub_u32 v12, v12, v14\n\t"
    "v_bfe_u32 v14, v13, 3, 1\n\t"
    "v_lshlrev_b32 v14, 1, v14\n\t"
    "v_xor_b32 v12, v12, v14\n\t"
    "v_mul_u32_u24 v13, 9216, v13\n\t"
    "v_lshl_add_u32 v23, v12, 4, v13\n\t"
    "s_add_u32 s67, s66, 512\n\t"
    "v_add_u32 v12, s67, v11\n\t"
    "v_mul_u32_u24 v13, 1821, v12\n\t"
    "v_lshrrev_b32 v13, 16, v13\n\t"
    "v_mul_u32_u24 v14, 36, v13\n\t"
    "v_sub_u32 v12, v12, v14\n\t"
    "v_bfe_u32 v14, v13, 3, 1\n\t"
    "v_lshlrev_b32 v14, 1, v14\n\t"
    "v_xor_b32 v12, v12, v14\n\t"
    "v_mul_u32_u24 v13, 9216, v13\n\t"
    "v_lshl_add_u32 v24, v12, 4, v13\n\t"
    "LLA4_%=:\n\t"
    "s_add_u32 s76, s76, 1\n\t"
    "s_cmp_eq_u32 s76, 9\n\t"
    "s_cselect_b32 s76, 0, s76\n\t"
    "s_add_u32 s69, s69, 36864\n\t"
    "s_cmp_eq_u32 s69, 147456\n\t"
    "s_cselect_b32 s69, 0, s69\n\t"
    "s_add_u32 s68, s68, 1\n\t"
    "s_cmp_lt_u32 s68, 9\n\t"
    "s_cbranch_scc1 LLA_%=\n\t"
    "s_mul_i32 s74, s65, 9216\n\t"
    "s_lshl_b32 s78, s65, 18\n\t"
    "s_mul_i32 s80, s55, 294912\n\t"
    "s_lshl_b32 s81, s80, 1\n\t"
    "s_sub_u32 s81, 294912, s81\n\t"
    "s_sub_u32 s76, s76, 1\n\t"
    "s_cselect_b32 s76, 8, s76\n\t"
    "s_mul_i32 s75, s76, 589824\n\t"
    "s_add_u32 s75, s75, s80\n\t"
    "s_lshl_b32 s79, s76, 7\n\t"
    "s_add_u32 s73, s77, s79\n\t"
    "s_waitcnt vmcnt(26)\n\t"
    "s_barrier\n\t"
    "s_add_u32 s70, s69, s74\n\t"
    "s_mov_b32 s71, s75\n\t"
    "s_add_u32 m0, s70, 0\n\t"
    "s_nop 0\n\t"
    "buffer_load_dwordx4 v16, s[12:15], s71 offen nt lds\n\t"
    "s_add_u32 m0, s70, 1024\n\t"
    "s_nop 0\n\t"
    "buffer_load_dwordx4 v17, s[12:15], s71 offen nt lds\n\t"
    "s_add_u32 m0, s70, 2048\n\t"
    "s_nop 0\n\t"
    "buffer_load_dwordx4 v18, s[12:15], s71 offen nt lds\n\t"
    "s_add_u32 m0, s70, 3072\n\t"
    "s_nop 0\n\t"
    "buffer_load_dwordx4 v19, s[12:15], s71 offen nt lds\n\t"
    "s_add_u32 m0, s70, 4096\n\t"
    "s_nop 0\n\t"
    "buffer_load_dwordx4 v20, s[12:15], s71 offen nt lds\n\t"
    "s_add_u32 m0, s70, 5120\n\t"
    "s_nop 0\n\t"
    "buffer_load_dwordx4 v21, s[12:15], s71 offen nt lds\n\t"
    "s_add_u32 m0, s70, 6144\n\t"
    "s_nop 0\n\t"
    "buffer_load_dwordx4 v22, s[12:15], s71 offen nt lds\n\t"
    "s_add_u32 m0, s70, 7168\n\t"
    "s_nop 0\n\t"
    "buffer_load_dwordx4 v23, s[12:15], s71 offen nt lds\n\t"
    "s_add_u32 m0, s70, 8192\n\t"
    "s_nop 0\n\t"
    "buffer_load_dwordx4 v24, s[12:15], s71 offen nt lds\n\t"
    "s_add_u32 s70, s69, s72\n\t"
    "s_add_u32 s69, s69, 36864\n\t"
    "s_cmp_eq_u32 s69, 147456\n\t"
    "s_cselect_b32 s69, 0, s69\n\t"
    "s_add_u32 s75, s75, s81\n\t"
    "s_waitcnt vmcnt(22)\n\t"
    "s_barrier\n\t"
    "s_add_u32 s70, s69, s74\n\t"
    "s_mov_b32 s71, s75\n\t"
    "s_add_u32 m0, s70, 0\n\t"
    "s_nop 0\n\t"
    "buffer_load_dwordx4 v16, s[12:15], s71 offen nt lds\n\t"
    "s_add_u32 m0, s70, 1024\n\t"
    "s_nop 0\n\t"
    "buffer_load_dwordx4 v17, s[12:15], s71 offen nt lds\n\t"
    "s_add_u32 m0, s70, 2048\n\t"
    "s_nop 0\n\t"
    "buffer_load_dwordx4 v18, s[12:15], s71 offen nt lds\n\t"
    "s_add_u32 m0, s70, 3072\n\t"
    "s_nop 0\n\t"
    "buffer_load_dwordx4 v19, s[12:15], s71 offen nt lds\n\t"
    "s_add_u32 m0, s70, 4096\n\t"
    "s_nop 0\n\t"
    "buffer_load_dwordx4 v20, s[12:15], s71 offen nt lds\n\t"
    "s_add_u32 m0, s70, 5120\n\t"
    "s_nop 0\n\t"
    "buffer_load_dwordx4 v21, s[12:15], s71 offen nt lds\n\t"
    "s_add_u32 m0, s70, 6144\n\t"
    "s_nop 0\n\t"
    "buffer_load_dwordx4 v22, s[12:15], s71 offen nt lds\n\t"
    "s_add_u32 m0, s70, 7168\n\t"
    "s_nop 0\n\t"
    "buffer_load_dwordx4 v23, s[12:15], s71 offen nt lds\n\t"
    "s_add_u32 m0, s70, 8192\n\t"
    "s_nop 0\n\t"
    "buffer_load_dwordx4 v24, s[12:15], s71 offen nt lds\n\t"
    "s_add_u32 s70, s69, s72\n\t"
    "s_add_u32 s69, s69, 36864\n\t"
    "s_cmp_eq_u32 s69, 147456\n\t"
    "s_cselect_b32 s69, 0, s69\n\t"
    "s_sub_u32 s76, s76, 1\n\t"
    "s_cselect_b32 s76, 8, s76\n\t"
    "s_mul_i32 s75, s76, 589824\n\t"
    "s_add_u32 s75, s75, s80\n\t"
    "s_lshl_b32 s79, s76, 7\n\t"
    "s_add_u32 s73, s77, s79\n\t"
    "s_waitcnt vmcnt(18)\n\t"
    "s_barrier\n\t"
    "s_add_u32 s70, s69, s74\n\t"
    "s_mov_b32 s71, s75\n\t"
    "s_add_u32 m0, s70, 0\n\t"
    "s_nop 0\n\t"
    "buffer_load_dwordx4 v16, s[12:15], s71 offen lds\n\t"
    "s_add_u32 m0, s70, 1024\n\t"
    "s_nop 0\n\t"
    "buffer_load_dwordx4 v17, s[12:15], s71 offen lds\n\t"
    "s_add_u32 m0, s70, 2048\n\t"
    "s_nop 0\n\t"
    "buffer_load_dwordx4 v18, s[12:15], s71 offen lds\n\t"
    "s_add_u32 m0, s70, 3072\n\t"
    "s_nop 0\n\t"
    "buffer_load_dwordx4 v19, s[12:15], s71 offen lds\n\t"
    "s_add_u32 m0, s70, 4096\n\t"
    "s_nop 0\n\t"
    "buffer_load_dwordx4 v20, s[12:15], s71 offen lds\n\t"
    "s_add_u32 m0, s70, 5120\n\t"
    "s_nop 0\n\t"
    "buffer_load_dwordx4 v21, s[12:15], s71 offen lds\n\t"
    "s_add_u32 m0, s70, 6144\n\t"
    "s_nop 0\n\t"
    "buffer_load_dwordx4 v22, s[12:15], s71 offen lds\n\t"
    "s_add_u32 m0, s70, 7168\n\t"
    "s_nop 0\n\t"
    "buffer_load_dwordx4 v23, s[12:15], s71 offen lds\n\t"
    "s_add_u32 m0, s70, 8192\n\t"
    "s_nop 0\n\t"
    "buffer_load_dwordx4 v24, s[12:15], s71 offen lds\n\t"
    "s_add_u32 s70, s69, s72\n\t"
    "s_add_u32 s69, s69, 36864\n\t"
    "s_cmp_eq_u32 s69, 147456\n\t"
    "s_cselect_b32 s69, 0, s69\n\t"
    "s_add_u32 s75, s75, s81\n\t"
    "s_waitcnt vmcnt(18)\n\t"
    "s_barrier\n\t"
    "s_add_u32 s70, s69, s74\n\t"
    "s_mov_b32 s71, s75\n\t"
    "s_add_u32 m0, s70, 0\n\t"
    "s_nop 0\n\t"
    "buffer_load_dwordx4 v16, s[12:15], s71 offen lds\n\t"
    "s_add_u32 m0, s70, 1024\n\t"
    "s_nop 0\n\t"
    "buffer_load_dwordx4 v17, s[12:15], s71 offen lds\n\t"
    "s_add_u32 m0, s70, 2048\n\t"
    "s_nop 0\n\t"
    "buffer_load_dwordx4 v18, s[12:15], s71 offen lds\n\t"
    "s_add_u32 m0, s70, 3072\n\t"
    "s_nop 0\n\t"
    "buffer_load_dwordx4 v19, s[12:15], s71 offen lds\n\t"
    "s_add_u32 m0, s70, 4096\n\t"
    "s_nop 0\n\t"
    "buffer_load_dwordx4 v20, s[12:15], s71 offen lds\n\t"
    "s_add_u32 m0, s70, 5120\n\t"
    "s_nop 0\n\t"
    "buffer_load_dwordx4 v21, s[12:15], s71 offen lds\n\t"
    "s_add_u32 m0, s70, 6144\n\t"
    "s_nop 0\n\t"
    "buffer_load_dwordx4 v22, s[12:15], s71 offen lds\n\t"
    "s_add_u32 m0, s70, 7168\n\t"
    "s_nop 0\n\t"
    "buffer_load_dwordx4 v23, s[12:15], s71 offen lds\n\t"
    "s_add_u32 m0, s70, 8192\n\t"
    "s_nop 0\n\t"
    "buffer_load_dwordx4 v24, s[12:15], s71 offen lds\n\t"
    "s_add_u32 s70, s69, s72\n\t"
    "s_add_u32 s69, s69, 36864\n\t"
    "s_cmp_eq_u32 s69, 147456\n\t"
    "s_cselect_b32 s69, 0, s69\n\t"
    "s_sub_u32 s76, s76, 1\n\t"
    "s_cselect_b32 s76, 8, s76\n\t"
    "s_mul_i32 s75, s76, 589824\n\t"
    "s_add_u32 s75, s75, s80\n\t"
    "s_lshl_b32 s79, s76, 7\n\t"
    "s_add_u32 s73, s77, s79\n\t"
    "s_waitcnt vmcnt(18)\n\t"
    "s_barrier\n\t"
    "s_add_u32 s70, s69, s74\n\t"
    "s_mov_b32 s71, s75\n\t"
    "s_add_u32 m0, s70, 0\n\t"
    "s_nop 0\n\t"
    "buffer_load_dwordx4 v16, s[12:15], s71 offen lds\n\t"
    "s_add_u32 m0, s70, 1024\n\t"
    "s_nop 0\n\t"
    "buffer_load_dwordx4 v17, s[12:15], s71 offen lds\n\t"
    "s_add_u32 m0, s70, 2048\n\t"
    "s_nop 0\n\t"
    "buffer_load_dwordx4 v18, s[12:15], s71 offen lds\n\t"
    "s_add_u32 m0, s70, 3072\n\t"
    "s_nop 0\n\t"
    "buffer_load_dwordx4 v19, s[12:15], s71 offen lds\n\t"
    "s_add_u32 m0, s70, 4096\n\t"
    "s_nop 0\n\t"
    "buffer_load_dwordx4 v20, s[12:15], s71 offen lds\n\t"
    "s_add_u32 m0, s70, 5120\n\t"
    "s_nop 0\n\t"
    "buffer_load_dwordx4 v21, s[12:15], s71 offen lds\n\t"
    "s_add_u32 m0, s70, 6144\n\t"
    "s_nop 0\n\t"
    "buffer_load_dwordx4 v22, s[12:15], s71 offen lds\n\t"
    "s_add_u32 m0, s70, 7168\n\t"
    "s_nop 0\n\t"
    "buffer_load_dwordx4 v23, s[12:15], s71 offen lds\n\t"
    "s_add_u32 m0, s70, 8192\n\t"
    "s_nop 0\n\t"
    "buffer_load_dwordx4 v24, s[12:15], s71 offen lds\n\t"
    "s_add_u32 s70, s69, s72\n\t"
    "s_add_u32 s69, s69, 36864\n\t"
    "s_cmp_eq_u32 s69, 147456\n\t"
    "s_cselect_b32 s69, 0, s69\n\t"
    "s_add_u32 s75, s75, s81\n\t"
    "s_waitcnt vmcnt(18)\n\t"
    "s_barrier\n\t"
    "s_add_u32 s70, s69, s74\n\t"
    "s_mov_b32 s71, s75\n\t"
    "s_add_u32 m0, s70, 0\n\t"
    "s_nop 0\n\t"
    "buffer_load_dwordx4 v16, s[12:15], s71 offen lds\n\t"
    "s_add_u32 m0, s70, 1024\n\t"
    "s_nop 0\n\t"
    "buffer_load_dwordx4 v17, s[12:15], s71 offen lds\n\t"
    "s_add_u32 m0, s70, 2048\n\t"
    "s_nop 0\n\t"
    "buffer_load_dwordx4 v18, s[12:15], s71 offen lds\n\t"
    "s_add_u32 m0, s70, 3072\n\t"
    "s_nop 0\n\t"
    "buffer_load_dwordx4 v19, s[12:15], s71 offen lds\n\t"
    "s_add_u32 m0, s70, 4096\n\t"
    "s_nop 0\n\t"
    "buffer_load_dwordx4 v20, s[12:15], s71 offen lds\n\t"
    "s_add_u32 m0, s70, 5120\n\t"
    "s_nop 0\n\t"
    "buffer_load_dwordx4 v21, s[12:15], s71 offen lds\n\t"
    "s_add_u32 m0, s70, 6144\n\t"
    "s_nop 0\n\t"
    "buffer_load_dwordx4 v22, s[12:15], s71 offen lds\n\t"
    "s_add_u32 m0, s70, 7168\n\t"
    "s_nop 0\n\t"
    "buffer_load_dwordx4 v23, s[12:15], s71 offen lds\n\t"
    "s_add_u32 m0, s70, 8192\n\t"
    "s_nop 0\n\t"
    "buffer_load_dwordx4 v24, s[12:15], s71 offen lds\n\t"
    "s_add_u32 s70, s69, s72\n\t"
    "s_add_u32 m0, s70, 18432\n\t"
    "s_add_u32 s71, s78, 0\n\t"
    "buffer_load_dwordx4 v26, s[28:31], s71 offen nt lds\n\t"
    "s_add_u32 m0, s70, 19456\n\t"
    "s_add_u32 s71, s78, 32768\n\t"
    "buffer_load_dwordx4 v27, s[28:31], s71 offen nt lds\n\t"
    "s_add_u32 m0, s70, 20480\n\t"
    "s_add_u32 s71, s78, 65536\n\t"
    "buffer_load_dwordx4 v28, s[28:31], s71 offen nt lds\n\t"
    "s_add_u32 m0, s70, 21504\n\t"
    "s_add_u32 s71, s78, 98304\n\t"
    "buffer_load_dwordx4 v29, s[28:31], s71 offen nt lds\n\t"
    "s_add_u32 m0, s70, 22528\n\t"
    "s_add_u32 s71, s78, 131072\n\t"
    "buffer_load_dwordx4 v26, s[28:31], s71 offen nt lds\n\t"
    "s_add_u32 m0, s70, 23552\n\t"
    "s_add_u32 s71, s78, 163840\n\t"
    "buffer_load_dwordx4 v27, s[28:31], s71 offen nt lds\n\t"
    "s_add_u32 m0, s70, 24576\n\t"
    "s_add_u32 s71, s78, 196608\n\t"
    "buffer_load_dwordx4 v28, s[28:31], s71 offen nt lds\n\t"
    "s_add_u32 m0, s70, 25600\n\t"
    "s_add_u32 s71, s78, 229376\n\t"
    "buffer_load_dwordx4 v29, s[28:31], s71 offen nt lds\n\t"
    "s_add_u32 s69, s69, 36864\n\t"
    "s_cmp_eq_u32 s69, 147456\n\t"
    "s_cselect_b32 s69, 0, s69\n\t"
    "s_sub_u32 s76, s76, 1\n\t"
    "s_cselect_b32 s76, 8, s76\n\t"
    "s_mul_i32 s75, s76, 589824\n\t"
    "s_add_u32 s75, s75, s80\n\t"
    "s_lshl_b32 s79, s76, 7\n\t"
    "s_add_u32 s73, s77, s79\n\t"
    "s_waitcnt vmcnt(26)\n\t"
    "s_barrier\n\t"
    "s_add_u32 s70, s69, s74\n\t"
    "s_mov_b32 s71, s75\n\t"
    "s_add_u32 m0, s70, 0\n\t"
    "s_nop 0\n\t"
    "buffer_load_dwordx4 v16, s[12:15], s71 offen lds\n\t"
    "s_add_u32 m0, s70, 1024\n\t"
    "s_nop 0\n\t"
    "buffer_load_dwordx4 v17, s[12:15], s71 offen lds\n\t"
    "s_add_u32 m0, s70, 2048\n\t"
    "s_nop 0\n\t"
    "buffer_load_dwordx4 v18, s[12:15], s71 offen lds\n\t"
    "s_add_u32 m0, s70, 3072\n\t"
    "s_nop 0\n\t"
    "buffer_load_dwordx4 v19, s[12:15], s71 offen lds\n\t"
    "s_add_u32 m0, s70, 4096\n\t"
    "s_nop 0\n\t"
    "buffer_load_dwordx4 v20, s[12:15], s71 offen lds\n\t"
    "s_add_u32 m0, s70, 5120\n\t"
    "s_nop 0\n\t"
    "buffer_load_dwordx4 v21, s[12:15], s71 offen lds\n\t"
    "s_add_u32 m0, s70, 6144\n\t"
    "s_nop 0\n\t"
    "buffer_load_dwordx4 v22, s[12:15], s71 offen lds\n\t"
    "s_add_u32 m0, s70, 7168\n\t"
    "s_nop 0\n\t"
    "buffer_load_dwordx4 v23, s[12:15], s71 offen lds\n\t"
    "s_add_u32 m0, s70, 8192\n\t"
    "s_nop 0\n\t"
    "buffer_load_dwordx4 v24, s[12:15], s71 offen lds\n\t"
    "s_add_u32 s70, s69, s72\n\t"
    "s_add_u32 s69, s69, 36864\n\t"
    "s_cmp_eq_u32 s69, 147456\n\t"
    "s_cselect_b32 s69, 0, s69\n\t"
    "s_add_u32 s75, s75, s81\n\t"
    "s_waitcnt vmcnt(26)\n\t"
    "s_barrier\n\t"
    "s_add_u32 s70, s69, s74\n\t"
    "s_mov_b32 s71, s75\n\t"
    "s_add_u32 m0, s70, 0\n\t"
    "s_nop 0\n\t"
    "buffer_load_dwordx4 v16, s[12:15], s71 offen lds\n\t"
    "s_add_u32 m0, s70, 1024\n\t"
    "s_nop 0\n\t"
    "buffer_load_dwordx4 v17, s[12:15], s71 offen lds\n\t"
    "s_add_u32 m0, s70, 2048\n\t"
    "s_nop 0\n\t"
    "buffer_load_dwordx4 v18, s[12:15], s71 offen lds\n\t"
    "s_add_u32 m0, s70, 3072\n\t"
    "s_nop 0\n\t"
    "buffer_load_dwordx4 v19, s[12:15], s71 offen lds\n\t"
    "s_add_u32 m0, s70, 4096\n\t"
    "s_nop 0\n\t"
    "buffer_load_dwordx4 v20, s[12:15], s71 offen lds\n\t"
    "s_add_u32 m0, s70, 5120\n\t"
    "s_nop 0\n\t"
    "buffer_load_dwordx4 v21, s[12:15], s71 offen lds\n\t"
    "s_add_u32 m0, s70, 6144\n\t"
    "s_nop 0\n\t"
    "buffer_load_dwordx4 v22, s[12:15], s71 offen lds\n\t"
    "s_add_u32 m0, s70, 7168\n\t"
    "s_nop 0\n\t"
    "buffer_load_dwordx4 v23, s[12:15], s71 offen lds\n\t"
    "s_add_u32 m0, s70, 8192\n\t"
    "s_nop 0\n\t"
    "buffer_load_dwordx4 v24, s[12:15], s71 offen lds\n\t"
    "s_add_u32 s70, s69, s72\n\t"
    "s_add_u32 m0, s70, 18432\n\t"
    "s_add_u32 s71, s78, 128\n\t"
    "buffer_load_dwordx4 v26, s[28:31], s71 offen nt lds\n\t"
    "s_add_u32 m0, s70, 19456\n\t"
    "s_add_u32 s71, s78, 32896\n\t"
    "buffer_load_dwordx4 v27, s[28:31], s71 offen nt lds\n\t"
    "s_add_u32 m0, s70, 20480\n\t"
    "s_add_u32 s71, s78, 65664\n\t"
    "buffer_load_dwordx4 v28, s[28:31], s71 offen nt lds\n\t"
    "s_add_u32 m0, s70, 21504\n\t"
    "s_add_u32 s71, s78, 98432\n\t"
    "buffer_load_dwordx4 v29, s[28:31], s71 offen nt lds\n\t"
    "s_add_u32 m0, s70, 22528\n\t"
    "s_add_u32 s71, s78, 131200\n\t"
    "buffer_load_dwordx4 v26, s[28:31], s71 offen nt lds\n\t"
    "s_add_u32 m0, s70, 23552\n\t"
    "s_add_u32 s71, s78, 163968\n\t"
    "buffer_load_dwordx4 v27, s[28:31], s71 offen nt lds\n\t"
    "s_add_u32 m0, s70, 24576\n\t"
    "s_add_u32 s71, s78, 196736\n\t"
    "buffer_load_dwordx4 v28, s[28:31], s71 offen nt lds\n\t"
    "s_add_u32 m0, s70, 25600\n\t"
    "s_add_u32 s71, s78, 229504\n\t"
    "buffer_load_dwordx4 v29, s[28:31], s71 offen nt lds\n\t"
    "s_add_u32 s69, s69, 36864\n\t"
    "s_cmp_eq_u32 s69, 147456\n\t"
    "s_cselect_b32 s69, 0, s69\n\t"
    "s_sub_u32 s76, s76, 1\n\t"
    "s_cselect_b32 s76, 8, s76\n\t"
    "s_mul_i32 s75, s76, 589824\n\t"
    "s_add_u32 s75, s75, s80\n\t"
    "s_lshl_b32 s79, s76, 7\n\t"
    "s_add_u32 s73, s77, s79\n\t"
    "s_waitcnt vmcnt(26)\n\t"
    "s_barrier\n\t"
    "s_add_u32 s70, s69, s74\n\t"
    "s_mov_b32 s71, s75\n\t"
    "s_add_u32 m0, s70, 0\n\t"
    "s_nop 0\n\t"
    "buffer_load_dwordx4 v16, s[12:15], s71 offen lds\n\t"
    "s_add_u32 m0, s70, 1024\n\t"
    "s_nop 0\n\t"
    "buffer_load_dwordx4 v17, s[12:15], s71 offen lds\n\t"
    "s_add_u32 m0, s70, 2048\n\t"
    "s_nop 0\n\t"
    "buffer_load_dwordx4 v18, s[12:15], s71 offen lds\n\t"
    "s_add_u32 m0, s70, 3072\n\t"
    "s_nop 0\n\t"
    "buffer_load_dwordx4 v19, s[12:15], s71 offen lds\n\t"
    "s_add_u32 m0, s70, 4096\n\t"
    "s_nop 0\n\t"
    "buffer_load_dwordx4 v20, s[12:15], s71 offen lds\n\t"
    "s_add_u32 m0, s70, 5120\n\t"
    "s_nop 0\n\t"
    "buffer_load_dwordx4 v21, s[12:15], s71 offen lds\n\t"
    "s_add_u32 m0, s70, 6144\n\t"
    "s_nop 0\n\t"
    "buffer_load_dwordx4 v22, s[12:15], s71 offen lds\n\t"
    "s_add_u32 m0, s70, 7168\n\t"
    "s_nop 0\n\t"
    "buffer_load_dwordx4 v23, s[12:15], s71 offen lds\n\t"
    "s_add_u32 m0, s70, 8192\n\t"
    "s_nop 0\n\t"
    "buffer_load_dwordx4 v24, s[12:15], s71 offen lds\n\t"
    "s_add_u32 s70, s69, s72\n\t"
    "s_add_u32 s69, s69, 36864\n\t"
    "s_cmp_eq_u32 s69, 147456\n\t"
    "s_cselect_b32 s69, 0, s69\n\t"
    "s_add_u32 s75, s75, s81\n\t"
    "s_waitcnt vmcnt(26)\n\t"
    "s_barrier\n\t"
    "s_add_u32 s70, s69, s74\n\t"
    "s_mov_b32 s71, s75\n\t"
    "s_add_u32 m0, s70, 0\n\t"
    "s_nop 0\n\t"
    "buffer_load_dwordx4 v16, s[12:15], s71 offen lds\n\t"
    "s_add_u32 m0, s70, 1024\n\t"
    "s_nop 0\n\t"
    "buffer_load_dwordx4 v17, s[12:15], s71 offen lds\n\t"
    "s_add_u32 m0, s70, 2048\n\t"
    "s_nop 0\n\t"
    "buffer_load_dwordx4 v18, s[12:15], s71 offen lds\n\t"
    "s_add_u32 m0, s70, 3072\n\t"
    "s_nop 0\n\t"
    "buffer_load_dwordx4 v19, s[12:15], s71 offen lds\n\t"
    "s_add_u32 m0, s70, 4096\n\t"
    "s_nop 0\n\t"
    "buffer_load_dwordx4 v20, s[12:15], s71 offen lds\n\t"
    "s_add_u32 m0, s70, 5120\n\t"
    "s_nop 0\n\t"
    "buffer_load_dwordx4 v21, s[12:15], s71 offen lds\n\t"
    "s_add_u32 m0, s70, 6144\n\t"
    "s_nop 0\n\t"
    "buffer_load_dwordx4 v22, s[12:15], s71 offen lds\n\t"
    "s_add_u32 m0, s70, 7168\n\t"
    "s_nop 0\n\t"
    "buffer_load_dwordx4 v23, s[12:15], s71 offen lds\n\t"
    "s_add_u32 m0, s70, 8192\n\t"
    "s_nop 0\n\t"
    "buffer_load_dwordx4 v24, s[12:15], s71 offen lds\n\t"
    "s_add_u32 s70, s69, s72\n\t"
    "s_add_u32 m0, s70, 18432\n\t"
    "s_add_u32 s71, s78, 256\n\t"
    "buffer_load_dwordx4 v26, s[28:31], s71 offen nt lds\n\t"
    "s_add_u32 m0, s70, 19456\n\t"
    "s_add_u32 s71, s78, 33024\n\t"
    "buffer_load_dwordx4 v27, s[28:31], s71 offen nt lds\n\t"
    "s_add_u32 m0, s70, 20480\n\t"
    "s_add_u32 s71, s78, 65792\n\t"
    "buffer_load_dwordx4 v28, s[28:31], s71 offen nt lds\n\t"
    "s_add_u32 m0, s70, 21504\n\t"
    "s_add_u32 s71, s78, 98560\n\t"
    "buffer_load_dwordx4 v29, s[28:31], s71 offen nt lds\n\t"
    "s_add_u32 m0, s70, 22528\n\t"
    "s_add_u32 s71, s78, 131328\n\t"
    "buffer_load_dwordx4 v26, s[28:31], s71 offen nt lds\n\t"
    "s_add_u32 m0, s70, 23552\n\t"
    "s_add_u32 s71, s78, 164096\n\t"
    "buffer_load_dwordx4 v27, s[28:31], s71 offen nt lds\n\t"
    "s_add_u32 m0, s70, 24576\n\t"
    "s_add_u32 s71, s78, 196864\n\t"
    "buffer_load_dwordx4 v28, s[28:31], s71 offen nt lds\n\t"
    "s_add_u32 m0, s70, 25600\n\t"
    "s_add_u32 s71, s78, 229632\n\t"
    "buffer_load_dwordx4 v29, s[28:31], s71 offen nt lds\n\t"
    "s_add_u32 s69, s69, 36864\n\t"
    "s_cmp_eq_u32 s69, 147456\n\t"
    "s_cselect_b32 s69, 0, s69\n\t"
    "s_sub_u32 s76, s76, 1\n\t"
    "s_cselect_b32 s76, 8, s76\n\t"
    "s_mul_i32 s75, s76, 589824\n\t"
    "s_add_u32 s75, s75, s80\n\t"
    "s_lshl_b32 s79, s76, 7\n\t"
    "s_add_u32 s73, s77, s79\n\t"
    "s_waitcnt vmcnt(26)\n\t"
    "s_barrier\n\t"
    "s_add_u32 s70, s69, s74\n\t"
    "s_mov_b32 s71, s75\n\t"
    "s_add_u32 m0, s70, 0\n\t"
    "s_nop 0\n\t"
    "buffer_load_dwordx4 v16, s[12:15], s71 offen lds\n\t"
    "s_add_u32 m0, s70, 1024\n\t"
    "s_nop 0\n\t"
    "buffer_load_dwordx4 v17, s[12:15], s71 offen lds\n\t"
    "s_add_u32 m0, s70, 2048\n\t"
    "s_nop 0\n\t"
    "buffer_load_dwordx4 v18, s[12:15], s71 offen lds\n\t"
    "s_add_u32 m0, s70, 3072\n\t"
    "s_nop 0\n\t"
    "buffer_load_dwordx4 v19, s[12:15], s71 offen lds\n\t"
    "s_add_u32 m0, s70, 4096\n\t"
    "s_nop 0\n\t"
    "buffer_load_dwordx4 v20, s[12:15], s71 offen lds\n\t"
    "s_add_u32 m0, s70, 5120\n\t"
    "s_nop 0\n\t"
    "buffer_load_dwordx4 v21, s[12:15], s71 offen lds\n\t"
    "s_add_u32 m0, s70, 6144\n\t"
    "s_nop 0\n\t"
    "buffer_load_dwordx4 v22, s[12:15], s71 offen lds\n\t"
    "s_add_u32 m0, s70, 7168\n\t"
    "s_nop 0\n\t"
    "buffer_load_dwordx4 v23, s[12:15], s71 offen lds\n\t"
    "s_add_u32 m0, s70, 8192\n\t"
    "s_nop 0\n\t"
    "buffer_load_dwordx4 v24, s[12:15], s71 offen lds\n\t"
    "s_add_u32 s70, s69, s72\n\t"
    "s_add_u32 m0, s70, 18432\n\t"
    "s_add_u32 s71, s73, 0\n\t"
    "buffer_load_dwordx4 v1, s[8:11], s71 offen lds\n\t"
    "s_add_u32 m0, s70, 19456\n\t"
    "s_add_u32 s71, s73, 9216\n\t"
    "buffer_load_dwordx4 v2, s[8:11], s71 offen lds\n\t"
    "s_add_u32 m0, s70, 20480\n\t"
    "s_add_u32 s71, s73, 18432\n\t"
    "buffer_load_dwordx4 v3, s[8:11], s71 offen lds\n\t"
    "s_add_u32 m0, s70, 21504\n\t"
    "s_add_u32 s71, s73, 27648\n\t"
    "buffer_load_dwordx4 v4, s[8:11], s71 offen lds\n\t"
    "s_add_u32 m0, s70, 22528\n\t"
    "s_add_u32 s71, s73, 36864\n\t"
    "buffer_load_dwordx4 v1, s[8:11], s71 offen lds\n\t"
    "s_add_u32 m0, s70, 23552\n\t"
    "s_add_u32 s71, s73, 46080\n\t"
    "buffer_load_dwordx4 v2, s[8:11], s71 offen lds\n\t"
    "s_add_u32 m0, s70, 24576\n\t"
    "s_add_u32 s71, s73, 55296\n\t"
    "buffer_load_dwordx4 v3, s[8:11], s71 offen lds\n\t"
    "s_add_u32 m0, s70, 25600\n\t"
    "s_add_u32 s71, s73, 64512\n\t"
    "buffer_load_dwordx4 v4, s[8:11], s71 offen lds\n\t"
    "s_add_u32 s69, s69, 36864\n\t"
    "s_cmp_eq_u32 s69, 147456\n\t"
    "s_cselect_b32 s69, 0, s69\n\t"
    "s_add_u32 s75, s75, s81\n\t"
    "s_waitcnt vmcnt(34)\n\t"
    "s_barrier\n\t"
    "s_add_u32 s70, s69, s74\n\t"
    "s_mov_b32 s71, s75\n\t"
    "s_add_u32 m0, s70, 0\n\t"
    "s_nop 0\n\t"
    "buffer_load_dwordx4 v16, s[12:15], s71 offen lds\n\t"
    "s_add_u32 m0, s70, 1024\n\t"
    "s_nop 0\n\t"
    "buffer_load_dwordx4 v17, s[12:15], s71 offen lds\n\t"
    "s_add_u32 m0, s70, 2048\n\t"
    "s_nop 0\n\t"
    "buffer_load_dwordx4 v18, s[12:15], s71 offen lds\n\t"
    "s_add_u32 m0, s70, 3072\n\t"
    "s_nop 0\n\t"
    "buffer_load_dwordx4 v19, s[12:15], s71 offen lds\n\t"
    "s_add_u32 m0, s70, 4096\n\t"
    "s_nop 0\n\t"
    "buffer_load_dwordx4 v20, s[12:15], s71 offen lds\n\t"
    "s_add_u32 m0, s70, 5120\n\t"
    "s_nop 0\n\t"
    "buffer_load_dwordx4 v21, s[12:15], s71 offen lds\n\t"
    "s_add_u32 m0, s70, 6144\n\t"
    "s_nop 0\n\t"
    "buffer_load_dwordx4 v22, s[12:15], s71 offen lds\n\t"
    "s_add_u32 m0, s70, 7168\n\t"
    "s_nop 0\n\t"
    "buffer_load_dwordx4 v23, s[12:15], s71 offen lds\n\t"
    "s_add_u32 m0, s70, 8192\n\t"
    "s_nop 0\n\t"
    "buffer_load_dwordx4 v24, s[12:15], s71 offen lds\n\t"
    "s_add_u32 s70, s69, s72\n\t"
    "s_add_u32 m0, s70, 18432\n\t"
    "s_add_u32 s71, s78, 384\n\t"
    "buffer_load_dwordx4 v26, s[28:31], s71 offen nt lds\n\t"
    "s_add_u32 m0, s70, 19456\n\t"
    "s_add_u32 s71, s78, 33152\n\t"
    "buffer_load_dwordx4 v27, s[28:31], s71 offen nt lds\n\t"
    "s_add_u32 m0, s70, 20480\n\t"
    "s_add_u32 s71, s78, 65920\n\t"
    "buffer_load_dwordx4 v28, s[28:31], s71 offen nt lds\n\t"
    "s_add_u32 m0, s70, 21504\n\t"
    "s_add_u32 s71, s78, 98688\n\t"
    "buffer_load_dwordx4 v29, s[28:31], s71 offen nt lds\n\t"
    "s_add_u32 m0, s70, 22528\n\t"
    "s_add_u32 s71, s78, 131456\n\t"
    "buffer_load_dwordx4 v26, s[28:31], s71 offen nt lds\n\t"
    "s_add_u32 m0, s70, 23552\n\t"
    "s_add_u32 s71, s78, 164224\n\t"
    "buffer_load_dwordx4 v27, s[28:31], s71 offen nt lds\n\t"
    "s_add_u32 m0, s70, 24576\n\t"
    "s_add_u32 s71, s78, 196992\n\t"
    "buffer_load_dwordx4 v28, s[28:31], s71 offen nt lds\n\t"
    "s_add_u32 m0, s70, 25600\n\t"
    "s_add_u32 s71, s78, 229760\n\t"
    "buffer_load_dwordx4 v29, s[28:31], s71 offen nt lds\n\t"
    "s_add_u32 s69, s69, 36864\n\t"
    "s_cmp_eq_u32 s69, 147456\n\t"
    "s_cselect_b32 s69, 0, s69\n\t"
    "s_sub_u32 s76, s76, 1\n\t"
    "s_cselect_b32 s76, 8, s76\n\t"
    "s_mul_i32 s75, s76, 589824\n\t"
    "s_add_u32 s75, s75, s80\n\t"
    "s_lshl_b32 s79, s76, 7\n\t"
    "s_add_u32 s73, s77, s79\n\t"
    "s_waitcnt vmcnt(34)\n\t"
    "s_barrier\n\t"
    "s_add_u32 s70, s69, s74\n\t"
    "s_mov_b32 s71, s75\n\t"
    "s_add_u32 m0, s70, 0\n\t"
    "s_nop 0\n\t"
    "buffer_load_dwordx4 v16, s[12:15], s71 offen lds\n\t"
    "s_add_u32 m0, s70, 1024\n\t"
    "s_nop 0\n\t"
    "buffer_load_dwordx4 v17, s[12:15], s71 offen lds\n\t"
    "s_add_u32 m0, s70, 2048\n\t"
    "s_nop 0\n\t"
    "buffer_load_dwordx4 v18, s[12:15], s71 offen lds\n\t"
    "s_add_u32 m0, s70, 3072\n\t"
    "s_nop 0\n\t"
    "buffer_load_dwordx4 v19, s[12:15], s71 offen lds\n\t"
    "s_add_u32 m0, s70, 4096\n\t"
    "s_nop 0\n\t"
    "buffer_load_dwordx4 v20, s[12:15], s71 offen lds\n\t"
    "s_add_u32 m0, s70, 5120\n\t"
    "s_nop 0\n\t"
    "buffer_load_dwordx4 v21, s[12:15], s71 offen lds\n\t"
    "s_add_u32 m0, s70, 6144\n\t"
    "s_nop 0\n\t"
    "buffer_load_dwordx4 v22, s[12:15], s71 offen lds\n\t"
    "s_add_u32 m0, s70, 7168\n\t"
    "s_nop 0\n\t"
    "buffer_load_dwordx4 v23, s[12:15], s71 offen lds\n\t"
    "s_add_u32 m0, s70, 8192\n\t"
    "s_nop 0\n\t"
    "buffer_load_dwordx4 v24, s[12:15], s71 offen lds\n\t"
    "s_add_u32 s70, s69, s72\n\t"
    "s_add_u32 m0, s70, 18432\n\t"
    "s_add_u32 s71, s73, 0\n\t"
    "buffer_load_dwordx4 v1, s[8:11], s71 offen lds\n\t"
    "s_add_u32 m0, s70, 19456\n\t"
    "s_add_u32 s71, s73, 9216\n\t"
    "buffer_load_dwordx4 v2, s[8:11], s71 offen lds\n\t"
    "s_add_u32 m0, s70, 20480\n\t"
    "s_add_u32 s71, s73, 18432\n\t"
    "buffer_load_dwordx4 v3, s[8:11], s71 offen lds\n\t"
    "s_add_u32 m0, s70, 21504\n\t"
    "s_add_u32 s71, s73, 27648\n\t"
    "buffer_load_dwordx4 v4, s[8:11], s71 offen lds\n\t"
    "s_add_u32 m0, s70, 22528\n\t"
    "s_add_u32 s71, s73, 36864\n\t"
    "buffer_load_dwordx4 v1, s[8:11], s71 offen lds\n\t"
    "s_add_u32 m0, s70, 23552\n\t"
    "s_add_u32 s71, s73, 46080\n\t"
    "buffer_load_dwordx4 v2, s[8:11], s71 offen lds\n\t"
    "s_add_u32 m0, s70, 24576\n\t"
    "s_add_u32 s71, s73, 55296\n\t"
    "buffer_load_dwordx4 v3, s[8:11], s71 offen lds\n\t"
    "s_add_u32 m0, s70, 25600\n\t"
    "s_add_u32 s71, s73, 64512\n\t"
    "buffer_load_dwordx4 v4, s[8:11], s71 offen lds\n\t"
    "s_add_u32 s69, s69, 36864\n\t"
    "s_cmp_eq_u32 s69, 147456\n\t"
    "s_cselect_b32 s69, 0, s69\n\t"
    "s_add_u32 s75, s75, s81\n\t"
    "s_waitcnt vmcnt(34)\n\t"
    "s_barrier\n\t"
    "s_add_u32 s70, s69, s74\n\t"
    "s_mov_b32 s71, s75\n\t"
    "s_add_u32 m0, s70, 0\n\t"
    "s_nop 0\n\t"
    "buffer_load_dwordx4 v16, s[12:15], s71 offen lds\n\t"
    "s_add_u32 m0, s70, 1024\n\t"
    "s_nop 0\n\t"
    "buffer_load_dwordx4 v17, s[12:15], s71 offen lds\n\t"
    "s_add_u32 m0, s70, 2048\n\t"
    "s_nop 0\n\t"
    "buffer_load_dwordx4 v18, s[12:15], s71 offen lds\n\t"
    "s_add_u32 m0, s70, 3072\n\t"
    "s_nop 0\n\t"
    "buffer_load_dwordx4 v19, s[12:15], s71 offen lds\n\t"
    "s_add_u32 m0, s70, 4096\n\t"
    "s_nop 0\n\t"
    "buffer_load_dwordx4 v20, s[12:15], s71 offen lds\n\t"
    "s_add_u32 m0, s70, 5120\n\t"
    "s_nop 0\n\t"
    "buffer_load_dwordx4 v21, s[12:15], s71 offen lds\n\t"
    "s_add_u32 m0, s70, 6144\n\t"
    "s_nop 0\n\t"
    "buffer_load_dwordx4 v22, s[12:15], s71 offen lds\n\t"
    "s_add_u32 m0, s70, 7168\n\t"
    "s_nop 0\n\t"
    "buffer_load_dwordx4 v23, s[12:15], s71 offen lds\n\t"
    "s_add_u32 m0, s70, 8192\n\t"
    "s_nop 0\n\t"
    "buffer_load_dwordx4 v24, s[12:15], s71 offen lds\n\t"
    "s_add_u32 s70, s69, s72\n\t"
    "s_add_u32 s69, s69, 36864\n\t"
    "s_cmp_eq_u32 s69, 147456\n\t"
    "s_cselect_b32 s69, 0, s69\n\t"
    "s_sub_u32 s76, s76, 1\n\t"
    "s_cselect_b32 s76, 8, s76\n\t"
    "s_mul_i32 s75, s76, 589824\n\t"
    "s_add_u32 s75, s75, s80\n\t"
    "s_lshl_b32 s79, s76, 7\n\t"
    "s_add_u32 s73, s77, s79\n\t"
    "s_waitcnt vmcnt(26)\n\t"
    "s_barrier\n\t"
    "s_add_u32 s70, s69, s74\n\t"
    "s_mov_b32 s71, s75\n\t"
    "s_add_u32 m0, s70, 0\n\t"
    "s_nop 0\n\t"
    "buffer_load_dwordx4 v16, s[12:15], s71 offen lds\n\t"
    "s_add_u32 m0, s70, 1024\n\t"
    "s_nop 0\n\t"
    "buffer_load_dwordx4 v17, s[12:15], s71 offen lds\n\t"
    "s_add_u32 m0, s70, 2048\n\t"
    "s_nop 0\n\t"
    "buffer_load_dwordx4 v18, s[12:15], s71 offen lds\n\t"
    "s_add_u32 m0, s70, 3072\n\t"
    "s_nop 0\n\t"
    "buffer_load_dwordx4 v19, s[12:15], s71 offen lds\n\t"
    "s_add_u32 m0, s70, 4096\n\t"
    "s_nop 0\n\t"
    "buffer_load_dwordx4 v20, s[12:15], s71 offen lds\n\t"
    "s_add_u32 m0, s70, 5120\n\t"
    "s_nop 0\n\t"
    "buffer_load_dwordx4 v21, s[12:15], s71 offen lds\n\t"
    "s_add_u32 m0, s70, 6144\n\t"
    "s_nop 0\n\t"
    "buffer_load_dwordx4 v22, s[12:15], s71 offen lds\n\t"
    "s_add_u32 m0, s70, 7168\n\t"
    "s_nop 0\n\t"
    "buffer_load_dwordx4 v23, s[12:15], s71 offen lds\n\t"
    "s_add_u32 m0, s70, 8192\n\t"
    "s_nop 0\n\t"
    "buffer_load_dwordx4 v24, s[12:15], s71 offen lds\n\t"
    "s_add_u32 s70, s69, s72\n\t"
    "s_add_u32 m0, s70, 18432\n\t"
    "s_add_u32 s71, s73, 0\n\t"
    "buffer_load_dwordx4 v1, s[8:11], s71 offen lds\n\t"
    "s_add_u32 m0, s70, 19456\n\t"
    "s_add_u32 s71, s73, 9216\n\t"
    "buffer_load_dwordx4 v2, s[8:11], s71 offen lds\n\t"
    "s_add_u32 m0, s70, 20480\n\t"
    "s_add_u32 s71, s73, 18432\n\t"
    "buffer_load_dwordx4 v3, s[8:11], s71 offen lds\n\t"
    "s_add_u32 m0, s70, 21504\n\t"
    "s_add_u32 s71, s73, 27648\n\t"
    "buffer_load_dwordx4 v4, s[8:11], s71 offen lds\n\t"
    "s_add_u32 m0, s70, 22528\n\t"
    "s_add_u32 s71, s73, 36864\n\t"
    "buffer_load_dwordx4 v1, s[8:11], s71 offen lds\n\t"
    "s_add_u32 m0, s70, 23552\n\t"
    "s_add_u32 s71, s73, 46080\n\t"
    "buffer_load_dwordx4 v2, s[8:11], s71 offen lds\n\t"
    "s_add_u32 m0, s70, 24576\n\t"
    "s_add_u32 s71, s73, 55296\n\t"
    "buffer_load_dwordx4 v3, s[8:11], s71 offen lds\n\t"
    "s_add_u32 m0, s70, 25600\n\t"
    "s_add_u32 s71, s73, 64512\n\t"
    "buffer_load_dwordx4 v4, s[8:11], s71 offen lds\n\t"
    "s_add_u32 s69, s69, 36864\n\t"
    "s_cmp_eq_u32 s69, 147456\n\t"
    "s_cselect_b32 s69, 0, s69\n\t"
    "s_add_u32 s75, s75, s81\n\t"
    "s_waitcnt vmcnt(26)\n\t"
    "s_barrier\n\t"
    "s_add_u32 s70, s69, s74\n\t"
    "s_mov_b32 s71, s75\n\t"
    "s_add_u32 m0, s70, 0\n\t"
    "s_nop 0\n\t"
    "buffer_load_dwordx4 v16, s[12:15], s71 offen lds\n\t"
    "s_add_u32 m0, s70, 1024\n\t"
    "s_nop 0\n\t"
    "buffer_load_dwordx4 v17, s[12:15], s71 offen lds\n\t"
    "s_add_u32 m0, s70, 2048\n\t"
    "s_nop 0\n\t"
    "buffer_load_dwordx4 v18, s[12:15], s71 offen lds\n\t"
    "s_add_u32 m0, s70, 3072\n\t"
    "s_nop 0\n\t"
    "buffer_load_dwordx4 v19, s[12:15], s71 offen lds\n\t"
    "s_add_u32 m0, s70, 4096\n\t"
    "s_nop 0\n\t"
    "buffer_load_dwordx4 v20, s[12:15], s71 offen lds\n\t"
    "s_add_u32 m0, s70, 5120\n\t"
    "s_nop 0\n\t"
    "buffer_load_dwordx4 v21, s[12:15], s71 offen lds\n\t"
    "s_add_u32 m0, s70, 6144\n\t"
    "s_nop 0\n\t"
    "buffer_load_dwordx4 v22, s[12:15], s71 offen lds\n\t"
    "s_add_u32 m0, s70, 7168\n\t"
    "s_nop 0\n\t"
    "buffer_load_dwordx4 v23, s[12:15], s71 offen lds\n\t"
    "s_add_u32 m0, s70, 8192\n\t"
    "s_nop 0\n\t"
    "buffer_load_dwordx4 v24, s[12:15], s71 offen lds\n\t"
    "s_add_u32 s70, s69, s72\n\t"
    "s_add_u32 s69, s69, 36864\n\t"
    "s_cmp_eq_u32 s69, 147456\n\t"
    "s_cselect_b32 s69, 0, s69\n\t"
    "s_sub_u32 s76, s76, 1\n\t"
    "s_cselect_b32 s76, 8, s76\n\t"
    "s_mul_i32 s75, s76, 589824\n\t"
    "s_add_u32 s75, s75, s80\n\t"
    "s_lshl_b32 s79, s76, 7\n\t"
    "s_add_u32 s73, s77, s79\n\t"
    "s_waitcnt vmcnt(26)\n\t"
    "s_barrier\n\t"
    "s_add_u32 s70, s69, s74\n\t"
    "s_mov_b32 s71, s75\n\t"
    "s_add_u32 m0, s70, 0\n\t"
    "s_nop 0\n\t"
    "buffer_load_dwordx4 v16, s[12:15], s71 offen lds\n\t"
    "s_add_u32 m0, s70, 1024\n\t"
    "s_nop 0\n\t"
    "buffer_load_dwordx4 v17, s[12:15], s71 offen lds\n\t"
    "s_add_u32 m0, s70, 2048\n\t"
    "s_nop 0\n\t"
    "buffer_load_dwordx4 v18, s[12:15], s71 offen lds\n\t"
    "s_add_u32 m0, s70, 3072\n\t"
    "s_nop 0\n\t"
    "buffer_load_dwordx4 v19, s[12:15], s71 offen lds\n\t"
    "s_add_u32 m0, s70, 4096\n\t"
    "s_nop 0\n\t"
    "buffer_load_dwordx4 v20, s[12:15], s71 offen lds\n\t"
    "s_add_u32 m0, s70, 5120\n\t"
    "s_nop 0\n\t"
    "buffer_load_dwordx4 v21, s[12:15], s71 offen lds\n\t"
    "s_add_u32 m0, s70, 6144\n\t"
    "s_nop 0\n\t"
    "buffer_load_dwordx4 v22, s[12:15], s71 offen lds\n\t"
    "s_add_u32 m0, s70, 7168\n\t"
    "s_nop 0\n\t"
    "buffer_load_dwordx4 v23, s[12:15], s71 offen lds\n\t"
    "s_add_u32 m0, s70, 8192\n\t"
    "s_nop 0\n\t"
    "buffer_load_dwordx4 v24, s[12:15], s71 offen lds\n\t"
    "s_add_u32 s70, s69, s72\n\t"
    "s_add_u32 m0, s70, 18432\n\t"
    "s_add_u32 s71, s73, 0\n\t"
    "buffer_load_dwordx4 v1, s[8:11], s71 offen lds\n\t"
    "s_add_u32 m0, s70, 19456\n\t"
    "s_add_u32 s71, s73, 9216\n\t"
    "buffer_load_dwordx4 v2, s[8:11], s71 offen lds\n\t"
    "s_add_u32 m0, s70, 20480\n\t"
    "s_add_u32 s71, s73, 18432\n\t"
    "buffer_load_dwordx4 v3, s[8:11], s71 offen lds\n\t"
    "s_add_u32 m0, s70, 21504\n\t"
    "s_add_u32 s71, s73, 27648\n\t"
    "buffer_load_dwordx4 v4, s[8:11], s71 offen lds\n\t"
    "s_add_u32 m0, s70, 22528\n\t"
    "s_add_u32 s71, s73, 36864\n\t"
    "buffer_load_dwordx4 v1, s[8:11], s71 offen lds\n\t"
    "s_add_u32 m0, s70, 23552\n\t"
    "s_add_u32 s71, s73, 46080\n\t"
    "buffer_load_dwordx4 v2, s[8:11], s71 offen lds\n\t"
    "s_add_u32 m0, s70, 24576\n\t"
    "s_add_u32 s71, s73, 55296\n\t"
    "buffer_load_dwordx4 v3, s[8:11], s71 offen lds\n\t"
    "s_add_u32 m0, s70, 25600\n\t"
    "s_add_u32 s71, s73, 64512\n\t"
    "buffer_load_dwordx4 v4, s[8:11], s71 offen lds\n\t"
    "s_add_u32 s69, s69, 36864\n\t"
    "s_cmp_eq_u32 s69, 147456\n\t"
    "s_cselect_b32 s69, 0, s69\n\t"
    "s_add_u32 s75, s75, s81\n\t"
    "s_waitcnt vmcnt(26)\n\t"
    "s_barrier\n\t"
    "s_add_u32 s70, s69, s74\n\t"
    "s_mov_b32 s71, s75\n\t"
    "s_add_u32 m0, s70, 0\n\t"
    "s_nop 0\n\t"
    "buffer_load_dwordx4 v16, s[12:15], s71 offen lds\n\t"
    "s_add_u32 m0, s70, 1024\n\t"
    "s_nop 0\n\t"
    "buffer_load_dwordx4 v17, s[12:15], s71 offen lds\n\t"
    "s_add_u32 m0, s70, 2048\n\t"
    "s_nop 0\n\t"
    "buffer_load_dwordx4 v18, s[12:15], s71 offen lds\n\t"
    "s_add_u32 m0, s70, 3072\n\t"
    "s_nop 0\n\t"
    "buffer_load_dwordx4 v19, s[12:15], s71 offen lds\n\t"
    "s_add_u32 m0, s70, 4096\n\t"
    "s_nop 0\n\t"
    "buffer_load_dwordx4 v20, s[12:15], s71 offen lds\n\t"
    "s_add_u32 m0, s70, 5120\n\t"
    "s_nop 0\n\t"
    "buffer_load_dwordx4 v21, s[12:15], s71 offen lds\n\t"
    "s_add_u32 m0, s70, 6144\n\t"
    "s_nop 0\n\t"
    "buffer_load_dwordx4 v22, s[12:15], s71 offen lds\n\t"
    "s_add_u32 m0, s70, 7168\n\t"
    "s_nop 0\n\t"
    "buffer_load_dwordx4 v23, s[12:15], s71 offen lds\n\t"
    "s_add_u32 m0, s70, 8192\n\t"
    "s_nop 0\n\t"
    "buffer_load_dwordx4 v24, s[12:15], s71 offen lds\n\t"
    "s_add_u32 s70, s69, s72\n\t"
    "s_add_u32 s69, s69, 36864\n\t"
    "s_cmp_eq_u32 s69, 147456\n\t"
    "s_cselect_b32 s69, 0, s69\n\t"
    "s_mul_i32 s72, s65, 18432\n\t"
    "s_mul_i32 s77, s65, 165888\n\t"
    "s_lshl_b32 s74, s65, 14\n\t"
    "s_lshl_b32 s78, s50, 1\n\t"
    "s_add_u32 s78, s78, s65\n\t"
    "s_mul_i32 s78, s78, 147456\n\t"
    "s_mov_b32 s68, 0\n\t"
    "s_mov_b32 s76, s52\n\t"
    "s_mul_i32 s80, s55, 9216\n\t"
    "s_mul_i32 s81, s55, 82944\n\t"
    "s_lshl_b32 s66, s80, 1\n\t"
    "s_sub_u32 s66, 9216, s66\n\t"
    "s_lshl_b32 s67, s81, 1\n\t"
    "s_sub_u32 s67, 82944, s67\n\t"
    "s_mul_i32 s85, s55, 36864\n\t"
    "LLC_%=:\n\t"
    "s_lshl_b32 s79, s76, 7\n\t"
    "s_add_u32 s73, s77, s79\n\t"
    "s_lshl_b32 s79, s76, 12\n\t"
    "s_add_u32 s75, s78, s79\n\t"
    "s_cmp_eq_u32 s68, 0\n\t"
    "s_cbranch_scc1 SC0a_%=\n\t"
    "s_waitcnt vmcnt(34)\n\t"
    "s_branch SC0b_%=\n\t"
    "SC0a_%=:\n\t"
    "s_waitcnt vmcnt(26)\n\t"
    "SC0b_%=:\n\t"
    "s_barrier\n\t"
    "s_add_u32 s70, s69, s72\n\t"
    "s_add_u32 s70, s70, s80\n\t"
    "s_add_u32 s73, s73, s81\n\t"
    "s_add_u32 m0, s70, 0\n\t"
    "s_add_u32 s71, s73, 0\n\t"
    "buffer_load_dwordx4 v8, s[16:19], s71 offen lds\n\t"
    "s_add_u32 m0, s70, 1024\n\t"
    "s_add_u32 s71, s73, 9216\n\t"
    "buffer_load_dwordx4 v8, s[16:19], s71 offen lds\n\t"
    "s_add_u32 m0, s70, 2048\n\t"
    "s_add_u32 s71, s73, 18432\n\t"
    "buffer_load_dwordx4 v8, s[16:19], s71 offen lds\n\t"
    "s_add_u32 m0, s70, 3072\n\t"
    "s_add_u32 s71, s73, 27648\n\t"
    "buffer_load_dwordx4 v8, s[16:19], s71 offen lds\n\t"
    "s_add_u32 m0, s70, 4096\n\t"
    "s_add_u32 s71, s73, 36864\n\t"
    "buffer_load_dwordx4 v8, s[16:19], s71 offen lds\n\t"
    "s_add_u32 m0, s70, 5120\n\t"
    "s_add_u32 s71, s73, 46080\n\t"
    "buffer_load_dwordx4 v8, s[16:19], s71 offen lds\n\t"
    "s_add_u32 m0, s70, 6144\n\t"
    "s_add_u32 s71, s73, 55296\n\t"
    "buffer_load_dwordx4 v8, s[16:19], s71 offen lds\n\t"
    "s_add_u32 m0, s70, 7168\n\t"
    "s_add_u32 s71, s73, 64512\n\t"
    "buffer_load_dwordx4 v8, s[16:19], s71 offen lds\n\t"
    "s_add_u32 m0, s70, 8192\n\t"
    "s_add_u32 s71, s73, 73728\n\t"
    "buffer_load_dwordx4 v8, s[16:19], s71 offen lds\n\t"
    "s_add_u32 s70, s70, s66\n\t"
    "s_add_u32 s73, s73, s67\n\t"
    "s_add_u32 m0, s70, 0\n\t"
    "s_add_u32 s71, s73, 0\n\t"
    "buffer_load_dwordx4 v8, s[16:19], s71 offen lds\n\t"
    "s_add_u32 m0, s70, 1024\n\t"
    "s_add_u32 s71, s73, 9216\n\t"
    "buffer_load_dwordx4 v8, s[16:19], s71 offen lds\n\t"
    "s_add_u32 m0, s70, 2048\n\t"
    "s_add_u32 s71, s73, 18432\n\t"
    "buffer_load_dwordx4 v8, s[16:19], s71 offen lds\n\t"
    "s_add_u32 m0, s70, 3072\n\t"
    "s_add_u32 s71, s73, 27648\n\t"
    "buffer_load_dwordx4 v8, s[16:19], s71 offen lds\n\t"
    "s_add_u32 m0, s70, 4096\n\t"
    "s_add_u32 s71, s73, 36864\n\t"
    "buffer_load_dwordx4 v8, s[16:19], s71 offen lds\n\t"
    "s_add_u32 m0, s70, 5120\n\t"
    "s_add_u32 s71, s73, 46080\n\t"
    "buffer_load_dwordx4 v8, s[16:19], s71 offen lds\n\t"
    "s_add_u32 m0, s70, 6144\n\t"
    "s_add_u32 s71, s73, 55296\n\t"
    "buffer_load_dwordx4 v8, s[16:19], s71 offen lds\n\t"
    "s_add_u32 m0, s70, 7168\n\t"
    "s_add_u32 s71, s73, 64512\n\t"
    "buffer_load_dwordx4 v8, s[16:19], s71 offen lds\n\t"
    "s_add_u32 m0, s70, 8192\n\t"
    "s_add_u32 s71, s73, 73728\n\t"
    "buffer_load_dwordx4 v8, s[16:19], s71 offen lds\n\t"
    "s_add_u32 s69, s69, 36864\n\t"
    "s_cmp_eq_u32 s69, 147456\n\t"
    "s_cselect_b32 s69, 0, s69\n\t"
    "s_cmp_eq_u32 s68, 0\n\t"
    "s_cbranch_scc1 SC1a_%=\n\t"
    "s_waitcnt vmcnt(34)\n\t"
    "s_branch SC1b_%=\n\t"
    "SC1a_%=:\n\t"
    "s_waitcnt vmcnt(27)\n\t"
    "SC1b_%=:\n\t"
    "s_barrier\n\t"
    "s_add_u32 s70, s69, s74\n\t"
    "s_add_u32 s86, s75, s85\n\t"
    "s_add_u32 m0, s70, 0\n\t"
    "s_add_u32 s71, s86, 0\n\t"
    "buffer_load_dwordx4 v0, s[56:59], s71 offen sc1 lds\n\t"
    "buffer_load_dwordx4 v0, s[56:59], s71 offen offset:1024 sc1 lds\n\t"
    "buffer_load_dwordx4 v0, s[56:59], s71 offen offset:2048 sc1 lds\n\t"
    "buffer_load_dwordx4 v0, s[56:59], s71 offen offset:3072 sc1 lds\n\t"
    "s_sub_u32 s86, s75, s85\n\t"
    "s_add_u32 m0, s70, 4096\n\t"
    "s_add_u32 s71, s86, 36864\n\t"
    "buffer_load_dwordx4 v0, s[56:59], s71 offen sc1 lds\n\t"
    "buffer_load_dwordx4 v0, s[56:59], s71 offen offset:1024 sc1 lds\n\t"
    "buffer_load_dwordx4 v0, s[56:59], s71 offen offset:2048 sc1 lds\n\t"
    "buffer_load_dwordx4 v0, s[56:59], s71 offen offset:3072 sc1 lds\n\t"
    "s_add_u32 s86, s75, s85\n\t"
    "s_add_u32 m0, s70, 8192\n\t"
    "s_add_u32 s71, s86, 73728\n\t"
    "buffer_load_dwordx4 v0, s[56:59], s71 offen sc1 lds\n\t"
    "buffer_load_dwordx4 v0, s[56:59], s71 offen offset:1024 sc1 lds\n\t"
    "buffer_load_dwordx4 v0, s[56:59], s71 offen offset:2048 sc1 lds\n\t"
    "buffer_load_dwordx4 v0, s[56:59], s71 offen offset:3072 sc1 lds\n\t"
    "s_sub_u32 s86, s75, s85\n\t"
    "s_add_u32 m0, s70, 12288\n\t"
    "s_add_u32 s71, s86, 110592\n\t"
    "buffer_load_dwordx4 v0, s[56:59], s71 offen sc1 lds\n\t"
    "buffer_load_dwordx4 v0, s[56:59], s71 offen offset:1024 sc1 lds\n\t"
    "buffer_load_dwordx4 v0, s[56:59], s71 offen offset:2048 sc1 lds\n\t"
    "buffer_load_dwordx4 v0, s[56:59], s71 offen offset:3072 sc1 lds\n\t"
    "s_add_u32 s69, s69, 36864\n\t"
    "s_cmp_eq_u32 s69, 147456\n\t"
    "s_cselect_b32 s69, 0, s69\n\t"
    "s_add_u32 s76, s76, 1\n\t"
    "s_cmp_eq_u32 s76, 9\n\t"
    "s_cselect_b32 s76, 0, s76\n\t"
    "s_add_u32 s68, s68, 1\n\t"
    "s_cmp_lt_u32 s68, 9\n\t"
    "s_cbranch_scc1 LLC_%=\n\t"
    "s_waitcnt vmcnt(34)\n\t"
    "s_barrier\n\t"
    "s_waitcnt vmcnt(16)\n\t"
    "s_barrier\n\t"
    "s_waitcnt vmcnt(0)\n\t"
    "s_barrier\n\t"
    "s_branch END_%=\n\t"
    "AUX_%=:\n\t"
    "s_cmp_eq_u32 s64, 6\n\t"
    "s_cbranch_scc0 IDLE_%=\n\t"
    "v_mov_b32 v6, 0\n\t"
    "buffer_load_dword v7, v6, s[40:43], 0 offen sc1\n\t"
    "v_mbcnt_lo_u32_b32 v11, -1, 0\n\t"
    "v_mbcnt_hi_u32_b32 v11, -1, v11\n\t"
    "v_and_b32 v12, 15, v11\n\t"
    "v_lshrrev_b32 v13, 4, v11\n\t"
    "v_lshlrev_b32 v2, 2, v12\n\t"
    "s_mul_i32 s82, s48, 2304\n\t"
    "s_lshr_b32 s84, s49, 2\n\t"
    "s_lshl_b32 s83, s84, 6\n\t"
    "s_add_u32 s82, s82, s83\n\t"
    "s_add_u32 s82, s82, 4096\n\t"
    "v_add_u32 v5, s82, v2\n\t"
    "s_lshl_b32 s84, s84, 5\n\t"
    "s_and_b32 s82, s49, 2\n\t"
    "s_lshl_b32 s82, s82, 1\n\t"
    "s_add_u32 s82, s82, s84\n\t"
    "v_lshrrev_b32 v3, 2, v12\n\t"
    "v_and_b32 v4, 3, v12\n\t"
    "v_lshl_add_u32 v3, v3, 3, v4\n\t"
    "v_add_u32 v3, s82, v3\n\t"
    "v_mul_u32_u24 v3, 1152, v3\n\t"
    "v_and_b32 v4, 1, v13\n\t"
    "v_lshl_add_u32 v3, v4, 5, v3\n\t"
    "v_lshrrev_b32 v4, 1, v13\n\t"
    "v_lshl_add_u32 v3, v4, 4, v3\n\t"
    "s_and_b32 s82, s49, 1\n\t"
    "s_lshl_b32 s82, s82, 6\n\t"
    "v_add_u32 v3, s82, v3\n\t"
    "s_mov_b32 s65, 0\n\t"
    "BRp0_%=:\n\t"
    "s_barrier\n\t"
    "s_add_u32 s65, s65, 1\n\t"
    "s_cmp_lt_u32 s65, 13\n\t"
    "s_cbranch_scc1 BRp0_%=\n\t"
    "s_waitcnt vmcnt(0)\n\t"
    "v_readfirstlane_b32 s80, v7\n\t"
    "s_mul_i32 s80, s80, 0x9e3779b1\n\t"
    "s_add_u32 s80, s80, 0x7f4a7c15\n\t"
    "s_lshl_b32 s66, s48, 4\n\t"
    "s_add_u32 s66, s66, s49\n\t"
    "s_lshl_b32 s66, s66, 2\n\t"
    "s_add_u32 s66, s66, 1024\n\t"
    "v_mov_b32 v8, s80\n\t"
    "s_mov_b64 exec, 1\n\t"
    "buffer_store_dword v8, v6, s[40:43], s66 offen sc1\n\t"
    "s_mov_b64 exec, -1\n\t"
    "v_mov_b32 v1, 147968\n\t"
    "ds_write_b32 v1, v8\n\t"
    "v_and_b32 v2, 7, v11\n\t"
    "v_bfe_u32 v4, v11, 3, 1\n\t"
    "v_lshl_add_u32 v2, v4, 4, v2\n\t"
    "s_lshl_b32 s66, s50, 5\n\t"
    "s_and_b32 s67, s49, 8\n\t"
    "s_add_u32 s66, s66, s67\n\t"
    "s_add_u32 s66, s66, 256\n\t"
    "v_add_u32 v2, s66, v2\n\t"
    "v_lshlrev_b32 v2, 2, v2\n\t"
    "s_waitcnt lgkmcnt(0)\n\t"
    "s_mov_b32 s65, 0\n\t"
    "BRp1_%=:\n\t"
    "s_barrier\n\t"
    "s_add_u32 s65, s65, 1\n\t"
    "s_cmp_lt_u32 s65, 9\n\t"
    "s_cbranch_scc1 BRp1_%=\n\t"
    "buffer_load_dword v20, v2, s[40:43], 0 offen sc1\n\t"
    "s_mov_b32 s65, 0\n\t"
    "BRp2_%=:\n\t"
    "s_barrier\n\t"
    "s_add_u32 s65, s65, 1\n\t"
    "s_cmp_lt_u32 s65, 3\n\t"
    "s_cbranch_scc1 BRp2_%=\n\t"
    "s_mov_b32 s66, 0\n\t"
    "s_branch PWa_%=\n\t"
    "PLa_%=:\n\t"
    "buffer_load_dword v20, v2, s[40:43], 0 offen sc1\n\t"
    "PWa_%=:\n\t"
    "s_waitcnt vmcnt(0)\n\t"
    "v_cmp_ne_u32 vcc, s80, v20\n\t"
    "s_add_u32 s66, s66, 1\n\t"
    "s_cmp_eq_u64 vcc, 0\n\t"
    "s_cbranch_scc1 PDa_%=\n\t"
    "s_sleep 2\n\t"
    "s_cmp_lt_u32 s66, 0x40000\n\t"
    "s_cbranch_scc1 PLa_%=\n\t"
    "PDa_%=:\n\t"
    "s_mov_b32 s65, 0\n\t"
    "BRp3_%=:\n\t"
    "s_barrier\n\t"
    "s_add_u32 s65, s65, 1\n\t"
    "s_cmp_lt_u32 s65, 11\n\t"
    "s_cbranch_scc1 BRp3_%=\n\t"
    "s_mov_b32 s81, 1\n\t"
    "s_mov_b32 s86, 36\n\t"
    "s_branch REDS_%=\n\t"
    "SYT_%=:\n\t"
    "s_or_b32 s66, s48, s49\n\t"
    "s_cmp_eq_u32 s66, 0\n\t"
    "s_cbranch_scc0 SYE_%=\n\t"
    "v_add_u32 v2, 1024, v0\n\t"
    "s_mov_b32 s66, 0\n\t"
    "PLb_%=:\n\t"
    "buffer_load_dwordx4 v[20:23], v2, s[40:43], 0 offen sc1\n\t"
    "PWb_%=:\n\t"
    "s_waitcnt vmcnt(0)\n\t"
    "v_cmp_ne_u32 vcc, s80, v20\n\t"
    "v_cmp_ne_u32 s[92:93], s80, v21\n\t"
    "s_or_b64 vcc, vcc, s[92:93]\n\t"
    "v_cmp_ne_u32 s[92:93], s80, v22\n\t"
    "s_or_b64 vcc, vcc, s[92:93]\n\t"
    "v_cmp_ne_u32 s[92:93], s80, v23\n\t"
    "s_or_b64 vcc, vcc, s[92:93]\n\t"
    "s_add_u32 s66, s66, 1\n\t"
    "s_cmp_eq_u64 vcc, 0\n\t"
    "s_cbranch_scc1 PDb_%=\n\t"
    "s_sleep 2\n\t"
    "s_cmp_lt_u32 s66, 0x40000\n\t"
    "s_cbranch_scc1 PLb_%=\n\t"
    "PDb_%=:\n\t"
    "s_mov_b64 exec, 1\n\t"
    "buffer_store_dword v8, v6, s[40:43], 0 offen sc1\n\t"
    "s_mov_b64 exec, -1\n\t"
    "SYE_%=:\n\t"
    "s_waitcnt vmcnt(0)\n\t"
    "s_branch END_%=\n\t"
    "IDLE_%=:\n\t"
    "v_mbcnt_lo_u32_b32 v11, -1, 0\n\t"
    "v_mbcnt_hi_u32_b32 v11, -1, v11\n\t"
    "v_and_b32 v12, 15, v11\n\t"
    "v_lshrrev_b32 v13, 4, v11\n\t"
    "v_lshlrev_b32 v2, 2, v12\n\t"
    "s_mul_i32 s82, s48, 2304\n\t"
    "s_lshr_b32 s84, s49, 2\n\t"
    "s_lshl_b32 s83, s84, 6\n\t"
    "s_add_u32 s82, s82, s83\n\t"
    "s_add_u32 s82, s82, 4096\n\t"
    "v_add_u32 v5, s82, v2\n\t"
    "s_lshl_b32 s84, s84, 5\n\t"
    "s_and_b32 s82, s49, 2\n\t"
    "s_lshl_b32 s82, s82, 1\n\t"
    "s_add_u32 s82, s82, s84\n\t"
    "v_lshrrev_b32 v3, 2, v12\n\t"
    "v_and_b32 v4, 3, v12\n\t"
    "v_lshl_add_u32 v3, v3, 3, v4\n\t"
    "v_add_u32 v3, s82, v3\n\t"
    "v_mul_u32_u24 v3, 1152, v3\n\t"
    "v_and_b32 v4, 1, v13\n\t"
    "v_lshl_add_u32 v3, v4, 5, v3\n\t"
    "v_lshrrev_b32 v4, 1, v13\n\t"
    "v_lshl_add_u32 v3, v4, 4, v3\n\t"
    "s_and_b32 s82, s49, 1\n\t"
    "s_lshl_b32 s82, s82, 6\n\t"
    "v_add_u32 v3, s82, v3\n\t"
    "s_mov_b32 s65, 0\n\t"
    "BRq0_%=:\n\t"
    "s_barrier\n\t"
    "s_add_u32 s65, s65, 1\n\t"
    "s_cmp_lt_u32 s65, 21\n\t"
    "s_cbranch_scc1 BRq0_%=\n\t"
    "v_mov_b32 v1, 147968\n\t"
    "ds_read_b32 v1, v1\n\t"
    "s_waitcnt vmcnt(0) lgkmcnt(0)\n\t"
    "v_readfirstlane_b32 s80, v1\n\t"
    "s_mov_b32 s65, 0\n\t"
    "BRq1_%=:\n\t"
    "s_barrier\n\t"
    "s_add_u32 s65, s65, 1\n\t"
    "s_cmp_lt_u32 s65, 6\n\t"
    "s_cbranch_scc1 BRq1_%=\n\t"
    "s_add_u32 s66, s52, 8\n\t"
    "s_cmp_ge_u32 s66, 9\n\t"
    "s_cselect_b32 s67, 9, 0\n\t"
    "s_sub_u32 s66, s66, s67\n\t"
    "s_lshl_b32 s66, s66, 18\n\t"
    "s_lshl_b32 s67, s49, 10\n\t"
    "s_add_u32 s90, s66, s67\n\t"
    "v_mov_b32 v16, -1\n\t"
    "v_mov_b32 v17, -1\n\t"
    "v_mov_b32 v18, -1\n\t"
    "v_mov_b32 v19, -1\n\t"
    "buffer_store_dwordx4 v[16:19], v0, s[36:39], s90 offen sc1\n\t"
    "s_add_u32 s90, s90, 16384\n\t"
    "buffer_store_dwordx4 v[16:19], v0, s[36:39], s90 offen sc1\n\t"
    "s_add_u32 s90, s90, 16384\n\t"
    "buffer_store_dwordx4 v[16:19], v0, s[36:39], s90 offen sc1\n\t"
    "s_add_u32 s90, s90, 16384\n\t"
    "buffer_store_dwordx4 v[16:19], v0, s[36:39], s90 offen sc1\n\t"
    "s_add_u32 s90, s90, 16384\n\t"
    "buffer_store_dwordx4 v[16:19], v0, s[36:39], s90 offen sc1\n\t"
    "s_add_u32 s90, s90, 16384\n\t"
    "buffer_store_dwordx4 v[16:19], v0, s[36:39], s90 offen sc1\n\t"
    "s_add_u32 s90, s90, 16384\n\t"
    "buffer_store_dwordx4 v[16:19], v0, s[36:39], s90 offen sc1\n\t"
    "s_add_u32 s90, s90, 16384\n\t"
    "buffer_store_dwordx4 v[16:19], v0, s[36:39], s90 offen sc1\n\t"
    "s_add_u32 s90, s90, 16384\n\t"
    "buffer_store_dwordx4 v[16:19], v0, s[36:39], s90 offen sc1\n\t"
    "s_add_u32 s90, s90, 16384\n\t"
    "buffer_store_dwordx4 v[16:19], v0, s[36:39], s90 offen sc1\n\t"
    "s_add_u32 s90, s90, 16384\n\t"
    "buffer_store_dwordx4 v[16:19], v0, s[36:39], s90 offen sc1\n\t"
    "s_add_u32 s90, s90, 16384\n\t"
    "buffer_store_dwordx4 v[16:19], v0, s[36:39], s90 offen sc1\n\t"
    "s_add_u32 s90, s90, 16384\n\t"
    "buffer_store_dwordx4 v[16:19], v0, s[36:39], s90 offen sc1\n\t"
    "s_add_u32 s90, s90, 16384\n\t"
    "buffer_store_dwordx4 v[16:19], v0, s[36:39], s90 offen sc1\n\t"
    "s_add_u32 s90, s90, 16384\n\t"
    "buffer_store_dwordx4 v[16:19], v0, s[36:39], s90 offen sc1\n\t"
    "s_add_u32 s90, s90, 16384\n\t"
    "buffer_store_dwordx4 v[16:19], v0, s[36:39], s90 offen sc1\n\t"
    "s_add_u32 s90, s90, 16384\n\t"
    "s_mov_b32 s65, 0\n\t"
    "BRq2_%=:\n\t"
    "s_barrier\n\t"
    "s_add_u32 s65, s65, 1\n\t"
    "s_cmp_lt_u32 s65, 3\n\t"
    "s_cbranch_scc1 BRq2_%=\n\t"
    "s_waitcnt vmcnt(0)\n\t"
    "s_mov_b32 s65, 0\n\t"
    "BRq3_%=:\n\t"
    "s_barrier\n\t"
    "s_add_u32 s65, s65, 1\n\t"
    "s_cmp_lt_u32 s65, 4\n\t"
    "s_cbranch_scc1 BRq3_%=\n\t"
    "s_mov_b32 s81, 0\n\t"
    "s_mov_b32 s86, 34\n\t"
    "REDS_%=:\n\t"
    "s_lshl_b32 s82, s81, 8\n\t"
    "v_add_u32 v2, s82, v5\n\t"
    "buffer_load_dword v20, v2, s[40:43], 0 offen sc1\n\t"
    "REDL_%=:\n\t"
    "s_cmp_lt_u32 s86, 45\n\t"
    "s_cbranch_scc0 RB1_%=\n\t"
    "s_barrier\n\t"
    "RB1_%=:\n\t"
    "s_add_u32 s86, s86, 1\n\t"
    "s_mov_b32 s66, 0\n\t"
    "s_branch PWr_%=\n\t"
    "PLr_%=:\n\t"
    "buffer_load_dword v20, v2, s[40:43], 0 offen sc1\n\t"
    "PWr_%=:\n\t"
    "s_waitcnt vmcnt(0)\n\t"
    "v_cmp_ne_u32 vcc, s80, v20\n\t"
    "s_add_u32 s66, s66, 1\n\t"
    "s_cmp_eq_u64 vcc, 0\n\t"
    "s_cbranch_scc1 PDr_%=\n\t"
    "s_sleep 2\n\t"
    "s_cmp_lt_u32 s66, 0x40000\n\t"
    "s_cbranch_scc1 PLr_%=\n\t"
    "PDr_%=:\n\t"
    "s_mov_b32 s87, s83\n\t"
    "s_add_u32 s83, s81, s52\n\t"
    "s_cmp_ge_u32 s83, 9\n\t"
    "s_cselect_b32 s82, 9, 0\n\t"
    "s_sub_u32 s83, s83, s82\n\t"
    "s_lshl_b32 s89, s83, 18\n\t"
    "s_lshl_b32 s84, s49, 10\n\t"
    "s_add_u32 s89, s89, s84\n\t"
    "s_lshl_b32 s83, s83, 7\n\t"
    "buffer_load_dwordx4 v[64:67], v0, s[36:39], s89 offen sc1\n\t"
    "s_add_u32 s89, s89, 16384\n\t"
    "buffer_load_dwordx4 v[68:71], v0, s[36:39], s89 offen sc1\n\t"
    "s_add_u32 s89, s89, 16384\n\t"
    "buffer_load_dwordx4 v[72:75], v0, s[36:39], s89 offen sc1\n\t"
    "s_add_u32 s89, s89, 16384\n\t"
    "buffer_load_dwordx4 v[76:79], v0, s[36:39], s89 offen sc1\n\t"
    "s_add_u32 s89, s89, 16384\n\t"
    "buffer_load_dwordx4 v[80:83], v0, s[36:39], s89 offen sc1\n\t"
    "s_add_u32 s89, s89, 16384\n\t"
    "buffer_load_dwordx4 v[84:87], v0, s[36:39], s89 offen sc1\n\t"
    "s_add_u32 s89, s89, 16384\n\t"
    "buffer_load_dwordx4 v[88:91], v0, s[36:39], s89 offen sc1\n\t"
    "s_add_u32 s89, s89, 16384\n\t"
    "buffer_load_dwordx4 v[92:95], v0, s[36:39], s89 offen sc1\n\t"
    "s_add_u32 s89, s89, 16384\n\t"
    "s_mov_b32 s88, s83\n\t"
    "s_mov_b32 s83, s87\n\t"
    "s_cmp_lt_u32 s86, 45\n\t"
    "s_cbranch_scc0 RB2_%=\n\t"
    "s_barrier\n\t"
    "RB2_%=:\n\t"
    "s_add_u32 s86, s86, 1\n\t"
    "s_cmp_lt_u32 s81, 2\n\t"
    "s_cbranch_scc1 REDF_%=\n\t"
    "REDT_%=:\n\t"
    "s_waitcnt vmcnt(8)\n\t"
    "v_lshlrev_b32 v40, 16, v96\n\t"
    "v_and_b32 v41, 0xffff0000, v96\n\t"
    "v_lshlrev_b32 v42, 16, v97\n\t"
    "v_and_b32 v43, 0xffff0000, v97\n\t"
    "v_lshlrev_b32 v44, 16, v98\n\t"
    "v_and_b32 v45, 0xffff0000, v98\n\t"
    "v_lshlrev_b32 v46, 16, v99\n\t"
    "v_and_b32 v47, 0xffff0000, v99\n\t"
    "v_add_f32 v32, v32, v40\n\t"
    "v_add_f32 v33, v33, v41\n\t"
    "v_add_f32 v34, v34, v42\n\t"
    "v_add_f32 v35, v35, v43\n\t"
    "v_add_f32 v36, v36, v44\n\t"
    "v_add_f32 v37, v37, v45\n\t"
    "v_add_f32 v38, v38, v46\n\t"
    "v_add_f32 v39, v39, v47\n\t"
    "v_lshlrev_b32 v40, 16, v100\n\t"
    "v_and_b32 v41, 0xffff0000, v100\n\t"
    "v_lshlrev_b32 v42, 16, v101\n\t"
    "v_and_b32 v43, 0xffff0000, v101\n\t"
    "v_lshlrev_b32 v44, 16, v102\n\t"
    "v_and_b32 v45, 0xffff0000, v102\n\t"
    "v_lshlrev_b32 v46, 16, v103\n\t"
    "v_and_b32 v47, 0xffff0000, v103\n\t"
    "v_add_f32 v32, v32, v40\n\t"
    "v_add_f32 v33, v33, v41\n\t"
    "v_add_f32 v34, v34, v42\n\t"
    "v_add_f32 v35, v35, v43\n\t"
    "v_add_f32 v36, v36, v44\n\t"
    "v_add_f32 v37, v37, v45\n\t"
    "v_add_f32 v38, v38, v46\n\t"
    "v_add_f32 v39, v39, v47\n\t"
    "v_lshlrev_b32 v40, 16, v104\n\t"
    "v_and_b32 v41, 0xffff0000, v104\n\t"
    "v_lshlrev_b32 v42, 16, v105\n\t"
    "v_and_b32 v43, 0xffff0000, v105\n\t"
    "v_lshlrev_b32 v44, 16, v106\n\t"
    "v_and_b32 v45, 0xffff0000, v106\n\t"
    "v_lshlrev_b32 v46, 16, v107\n\t"
    "v_and_b32 v47, 0xffff0000, v107\n\t"
    "v_add_f32 v32, v32, v40\n\t"
    "v_add_f32 v33, v33, v41\n\t"
    "v_add_f32 v34, v34, v42\n\t"
    "v_add_f32 v35, v35, v43\n\t"
    "v_add_f32 v36, v36, v44\n\t"
    "v_add_f32 v37, v37, v45\n\t"
    "v_add_f32 v38, v38, v46\n\t"
    "v_add_f32 v39, v39, v47\n\t"
    "v_lshlrev_b32 v40, 16, v108\n\t"
    "v_and_b32 v41, 0xffff0000, v108\n\t"
    "v_lshlrev_b32 v42, 16, v109\n\t"
    "v_and_b32 v43, 0xffff0000, v109\n\t"
    "v_lshlrev_b32 v44, 16, v110\n\t"
    "v_and_b32 v45, 0xffff0000, v110\n\t"
    "v_lshlrev_b32 v46, 16, v111\n\t"
    "v_and_b32 v47, 0xffff0000, v111\n\t"
    "v_add_f32 v32, v32, v40\n\t"
    "v_add_f32 v33, v33, v41\n\t"
    "v_add_f32 v34, v34, v42\n\t"
    "v_add_f32 v35, v35, v43\n\t"
    "v_add_f32 v36, v36, v44\n\t"
    "v_add_f32 v37, v37, v45\n\t"
    "v_add_f32 v38, v38, v46\n\t"
    "v_add_f32 v39, v39, v47\n\t"
    "v_lshlrev_b32 v40, 16, v112\n\t"
    "v_and_b32 v41, 0xffff0000, v112\n\t"
    "v_lshlrev_b32 v42, 16, v113\n\t"
    "v_and_b32 v43, 0xffff0000, v113\n\t"
    "v_lshlrev_b32 v44, 16, v114\n\t"
    "v_and_b32 v45, 0xffff0000, v114\n\t"
    "v_lshlrev_b32 v46, 16, v115\n\t"
    "v_and_b32 v47, 0xffff0000, v115\n\t"
    "v_add_f32 v32, v32, v40\n\t"
    "v_add_f32 v33, v33, v41\n\t"
    "v_add_f32 v34, v34, v42\n\t"
    "v_add_f32 v35, v35, v43\n\t"
    "v_add_f32 v36, v36, v44\n\t"
    "v_add_f32 v37, v37, v45\n\t"
    "v_add_f32 v38, v38, v46\n\t"
    "v_add_f32 v39, v39, v47\n\t"
    "v_lshlrev_b32 v40, 16, v116\n\t"
    "v_and_b32 v41, 0xffff0000, v116\n\t"
    "v_lshlrev_b32 v42, 16, v117\n\t"
    "v_and_b32 v43, 0xffff0000, v117\n\t"
    "v_lshlrev_b32 v44, 16, v118\n\t"
    "v_and_b32 v45, 0xffff0000, v118\n\t"
    "v_lshlrev_b32 v46, 16, v119\n\t"
    "v_and_b32 v47, 0xffff0000, v119\n\t"
    "v_add_f32 v32, v32, v40\n\t"
    "v_add_f32 v33, v33, v41\n\t"
    "v_add_f32 v34, v34, v42\n\t"
    "v_add_f32 v35, v35, v43\n\t"
    "v_add_f32 v36, v36, v44\n\t"
    "v_add_f32 v37, v37, v45\n\t"
    "v_add_f32 v38, v38, v46\n\t"
    "v_add_f32 v39, v39, v47\n\t"
    "v_lshlrev_b32 v40, 16, v120\n\t"
    "v_and_b32 v41, 0xffff0000, v120\n\t"
    "v_lshlrev_b32 v42, 16, v121\n\t"
    "v_and_b32 v43, 0xffff0000, v121\n\t"
    "v_lshlrev_b32 v44, 16, v122\n\t"
    "v_and_b32 v45, 0xffff0000, v122\n\t"
    "v_lshlrev_b32 v46, 16, v123\n\t"
    "v_and_b32 v47, 0xffff0000, v123\n\t"
    "v_add_f32 v32, v32, v40\n\t"
    "v_add_f32 v33, v33, v41\n\t"
    "v_add_f32 v34, v34, v42\n\t"
    "v_add_f32 v35, v35, v43\n\t"
    "v_add_f32 v36, v36, v44\n\t"
    "v_add_f32 v37, v37, v45\n\t"
    "v_add_f32 v38, v38, v46\n\t"
    "v_add_f32 v39, v39, v47\n\t"
    "v_lshlrev_b32 v40, 16, v124\n\t"
    "v_and_b32 v41, 0xffff0000, v124\n\t"
    "v_lshlrev_b32 v42, 16, v125\n\t"
    "v_and_b32 v43, 0xffff0000, v125\n\t"
    "v_lshlrev_b32 v44, 16, v126\n\t"
    "v_and_b32 v45, 0xffff0000, v126\n\t"
    "v_lshlrev_b32 v46, 16, v127\n\t"
    "v_and_b32 v47, 0xffff0000, v127\n\t"
    "v_add_f32 v32, v32, v40\n\t"
    "v_add_f32 v33, v33, v41\n\t"
    "v_add_f32 v34, v34, v42\n\t"
    "v_add_f32 v35, v35, v43\n\t"
    "v_add_f32 v36, v36, v44\n\t"
    "v_add_f32 v37, v37, v45\n\t"
    "v_add_f32 v38, v38, v46\n\t"
    "v_add_f32 v39, v39, v47\n\t"
    "v_cvt_pk_bf16_f32 v48, v32, v33\n\t"
    "v_cvt_pk_bf16_f32 v49, v34, v35\n\t"
    "v_cvt_pk_bf16_f32 v50, v36, v37\n\t"
    "v_cvt_pk_bf16_f32 v51, v38, v39\n\t"
    "s_nop 1\n\t"
    "v_permlane16_swap_b32 v48, v50\n\t"
    "v_permlane16_swap_b32 v49, v51\n\t"
    "buffer_store_dwordx4 v[48:51], v3, s[44:47], s83 offen\n\t"
    "s_cmp_ge_u32 s81, 6\n\t"
    "s_cbranch_scc1 REDE_%=\n\t"
    "REDF_%=:\n\t"
    "s_mov_b32 s83, s88\n\t"
    "s_cmp_lt_u32 s86, 45\n\t"
    "s_cbranch_scc0 RB3_%=\n\t"
    "s_barrier\n\t"
    "RB3_%=:\n\t"
    "s_add_u32 s86, s86, 1\n\t"
    "buffer_load_dwordx4 v[96:99], v0, s[36:39], s89 offen sc1\n\t"
    "s_add_u32 s89, s89, 16384\n\t"
    "buffer_load_dwordx4 v[100:103], v0, s[36:39], s89 offen sc1\n\t"
    "s_add_u32 s89, s89, 16384\n\t"
    "buffer_load_dwordx4 v[104:107], v0, s[36:39], s89 offen sc1\n\t"
    "s_add_u32 s89, s89, 16384\n\t"
    "buffer_load_dwordx4 v[108:111], v0, s[36:39], s89 offen sc1\n\t"
    "s_add_u32 s89, s89, 16384\n\t"
    "buffer_load_dwordx4 v[112:115], v0, s[36:39], s89 offen sc1\n\t"
    "s_add_u32 s89, s89, 16384\n\t"
    "buffer_load_dwordx4 v[116:119], v0, s[36:39], s89 offen sc1\n\t"
    "s_add_u32 s89, s89, 16384\n\t"
    "buffer_load_dwordx4 v[120:123], v0, s[36:39], s89 offen sc1\n\t"
    "s_add_u32 s89, s89, 16384\n\t"
    "buffer_load_dwordx4 v[124:127], v0, s[36:39], s89 offen sc1\n\t"
    "s_add_u32 s89, s89, 16384\n\t"
    "s_cmp_lt_u32 s86, 45\n\t"
    "s_cbranch_scc0 RB4_%=\n\t"
    "s_barrier\n\t"
    "RB4_%=:\n\t"
    "s_add_u32 s86, s86, 1\n\t"
    "s_waitcnt vmcnt(8)\n\t"
    "v_lshlrev_b32 v32, 16, v64\n\t"
    "v_and_b32 v33, 0xffff0000, v64\n\t"
    "v_lshlrev_b32 v34, 16, v65\n\t"
    "v_and_b32 v35, 0xffff0000, v65\n\t"
    "v_lshlrev_b32 v36, 16, v66\n\t"
    "v_and_b32 v37, 0xffff0000, v66\n\t"
    "v_lshlrev_b32 v38, 16, v67\n\t"
    "v_and_b32 v39, 0xffff0000, v67\n\t"
    "v_lshlrev_b32 v40, 16, v68\n\t"
    "v_and_b32 v41, 0xffff0000, v68\n\t"
    "v_lshlrev_b32 v42, 16, v69\n\t"
    "v_and_b32 v43, 0xffff0000, v69\n\t"
    "v_lshlrev_b32 v44, 16, v70\n\t"
    "v_and_b32 v45, 0xffff0000, v70\n\t"
    "v_lshlrev_b32 v46, 16, v71\n\t"
    "v_and_b32 v47, 0xffff0000, v71\n\t"
    "v_add_f32 v32, v32, v40\n\t"
    "v_add_f32 v33, v33, v41\n\t"
    "v_add_f32 v34, v34, v42\n\t"
    "v_add_f32 v35, v35, v43\n\t"
    "v_add_f32 v36, v36, v44\n\t"
    "v_add_f32 v37, v37, v45\n\t"
    "v_add_f32 v38, v38, v46\n\t"
    "v_add_f32 v39, v39, v47\n\t"
    "v_lshlrev_b32 v40, 16, v72\n\t"
    "v_and_b32 v41, 0xffff0000, v72\n\t"
    "v_lshlrev_b32 v42, 16, v73\n\t"
    "v_and_b32 v43, 0xffff0000, v73\n\t"
    "v_lshlrev_b32 v44, 16, v74\n\t"
    "v_and_b32 v45, 0xffff0000, v74\n\t"
    "v_lshlrev_b32 v46, 16, v75\n\t"
    "v_and_b32 v47, 0xffff0000, v75\n\t"
    "v_add_f32 v32, v32, v40\n\t"
    "v_add_f32 v33, v33, v41\n\t"
    "v_add_f32 v34, v34, v42\n\t"
    "v_add_f32 v35, v35, v43\n\t"
    "v_add_f32 v36, v36, v44\n\t"
    "v_add_f32 v37, v37, v45\n\t"
    "v_add_f32 v38, v38, v46\n\t"
    "v_add_f32 v39, v39, v47\n\t"
    "v_lshlrev_b32 v40, 16, v76\n\t"
    "v_and_b32 v41, 0xffff0000, v76\n\t"
    "v_lshlrev_b32 v42, 16, v77\n\t"
    "v_and_b32 v43, 0xffff0000, v77\n\t"
    "v_lshlrev_b32 v44, 16, v78\n\t"
    "v_and_b32 v45, 0xffff0000, v78\n\t"
    "v_lshlrev_b32 v46, 16, v79\n\t"
    "v_and_b32 v47, 0xffff0000, v79\n\t"
    "v_add_f32 v32, v32, v40\n\t"
    "v_add_f32 v33, v33, v41\n\t"
    "v_add_f32 v34, v34, v42\n\t"
    "v_add_f32 v35, v35, v43\n\t"
    "v_add_f32 v36, v36, v44\n\t"
    "v_add_f32 v37, v37, v45\n\t"
    "v_add_f32 v38, v38, v46\n\t"
    "v_add_f32 v39, v39, v47\n\t"
    "v_lshlrev_b32 v40, 16, v80\n\t"
    "v_and_b32 v41, 0xffff0000, v80\n\t"
    "v_lshlrev_b32 v42, 16, v81\n\t"
    "v_and_b32 v43, 0xffff0000, v81\n\t"
    "v_lshlrev_b32 v44, 16, v82\n\t"
    "v_and_b32 v45, 0xffff0000, v82\n\t"
    "v_lshlrev_b32 v46, 16, v83\n\t"
    "v_and_b32 v47, 0xffff0000, v83\n\t"
    "v_add_f32 v32, v32, v40\n\t"
    "v_add_f32 v33, v33, v41\n\t"
    "v_add_f32 v34, v34, v42\n\t"
    "v_add_f32 v35, v35, v43\n\t"
    "v_add_f32 v36, v36, v44\n\t"
    "v_add_f32 v37, v37, v45\n\t"
    "v_add_f32 v38, v38, v46\n\t"
    "v_add_f32 v39, v39, v47\n\t"
    "v_lshlrev_b32 v40, 16, v84\n\t"
    "v_and_b32 v41, 0xffff0000, v84\n\t"
    "v_lshlrev_b32 v42, 16, v85\n\t"
    "v_and_b32 v43, 0xffff0000, v85\n\t"
    "v_lshlrev_b32 v44, 16, v86\n\t"
    "v_and_b32 v45, 0xffff0000, v86\n\t"
    "v_lshlrev_b32 v46, 16, v87\n\t"
    "v_and_b32 v47, 0xffff0000, v87\n\t"
    "v_add_f32 v32, v32, v40\n\t"
    "v_add_f32 v33, v33, v41\n\t"
    "v_add_f32 v34, v34, v42\n\t"
    "v_add_f32 v35, v35, v43\n\t"
    "v_add_f32 v36, v36, v44\n\t"
    "v_add_f32 v37, v37, v45\n\t"
    "v_add_f32 v38, v38, v46\n\t"
    "v_add_f32 v39, v39, v47\n\t"
    "v_lshlrev_b32 v40, 16, v88\n\t"
    "v_and_b32 v41, 0xffff0000, v88\n\t"
    "v_lshlrev_b32 v42, 16, v89\n\t"
    "v_and_b32 v43, 0xffff0000, v89\n\t"
    "v_lshlrev_b32 v44, 16, v90\n\t"
    "v_and_b32 v45, 0xffff0000, v90\n\t"
    "v_lshlrev_b32 v46, 16, v91\n\t"
    "v_and_b32 v47, 0xffff0000, v91\n\t"
    "v_add_f32 v32, v32, v40\n\t"
    "v_add_f32 v33, v33, v41\n\t"
    "v_add_f32 v34, v34, v42\n\t"
    "v_add_f32 v35, v35, v43\n\t"
    "v_add_f32 v36, v36, v44\n\t"
    "v_add_f32 v37, v37, v45\n\t"
    "v_add_f32 v38, v38, v46\n\t"
    "v_add_f32 v39, v39, v47\n\t"
    "v_lshlrev_b32 v40, 16, v92\n\t"
    "v_and_b32 v41, 0xffff0000, v92\n\t"
    "v_lshlrev_b32 v42, 16, v93\n\t"
    "v_and_b32 v43, 0xffff0000, v93\n\t"
    "v_lshlrev_b32 v44, 16, v94\n\t"
    "v_and_b32 v45, 0xffff0000, v94\n\t"
    "v_lshlrev_b32 v46, 16, v95\n\t"
    "v_and_b32 v47, 0xffff0000, v95\n\t"
    "v_add_f32 v32, v32, v40\n\t"
    "v_add_f32 v33, v33, v41\n\t"
    "v_add_f32 v34, v34, v42\n\t"
    "v_add_f32 v35, v35, v43\n\t"
    "v_add_f32 v36, v36, v44\n\t"
    "v_add_f32 v37, v37, v45\n\t"
    "v_add_f32 v38, v38, v46\n\t"
    "v_add_f32 v39, v39, v47\n\t"
    "s_add_u32 s81, s81, 2\n\t"
    "s_cmp_lt_u32 s81, 6\n\t"
    "s_cbranch_scc0 REDU_%=\n\t"
    "s_lshl_b32 s82, s81, 8\n\t"
    "v_add_u32 v2, s82, v5\n\t"
    "buffer_load_dword v20, v2, s[40:43], 0 offen sc1\n\t"
    "s_branch REDL_%=\n\t"
    "REDU_%=:\n\t"
    "s_cmp_lt_u32 s86, 45\n\t"
    "s_cbranch_scc0 RB5_%=\n\t"
    "s_barrier\n\t"
    "RB5_%=:\n\t"
    "s_add_u32 s86, s86, 1\n\t"
    "s_cmp_lt_u32 s86, 45\n\t"
    "s_cbranch_scc0 RB6_%=\n\t"
    "s_barrier\n\t"
    "RB6_%=:\n\t"
    "s_add_u32 s86, s86, 1\n\t"
    "s_waitcnt vmcnt(0)\n\t"
    "s_branch REDT_%=\n\t"
    "REDE_%=:\n\t"
    "s_cmp_lt_u32 s86, 45\n\t"
    "s_cbranch_scc0 REDZ_%=\n\t"
    "s_cmp_lt_u32 s86, 45\n\t"
    "s_cbranch_scc0 RB7_%=\n\t"
    "s_barrier\n\t"
    "RB7_%=:\n\t"
    "s_add_u32 s86, s86, 1\n\t"
    "s_branch REDE_%=\n\t"
    "REDZ_%=:\n\t"
    "s_cmp_eq_u32 s64, 6\n\t"
    "s_cbranch_scc1 SYT_%=\n\t"
    "s_waitcnt vmcnt(0)\n\t"
    "END_%=:\n\t"
    :
    : [wid] "s"(wid), [bid] "s"(bid), [ka] "s"(ka)
    : "memory", "scc", "vcc", "v0","v1","v2","v3","v4","v5","v6","v7","v8","v9","v10","v11","v12","v13","v14","v15","v16","v17","v18","v19","v20","v21","v22","v23","v24","v25","v26","v27","v28","v29","v30","v31","v32","v33","v34","v35","v36","v37","v38","v39","v40","v41","v42","v43","v44","v45","v46","v47","v48","v49","v50","v51","v52","v53","v54","v55","v56","v57","v58","v59","v60","v61","v62","v63","v64","v65","v66","v67","v68","v69","v70","v71","v72","v73","v74","v75","v76","v77","v78","v79","v80","v81","v82","v83","v84","v85","v86","v87","v88","v89","v90","v91","v92","v93","v94","v95","v96","v97","v98","v99","v100","v101","v102","v103","v104","v105","v106","v107","v108","v109","v110","v111","v112","v113","v114","v115","v116","v117","v118","v119","v120","v121","v122","v123","v124","v125","v126","v127","v128","v129","v130","v131","v132","v133","v134","v135","v136","v137","v138","v139","v140","v141","v142","v143","v144","v145","v146","v147","v148","v149","v150","v151","v152","v153","v154","v155","v156","v157","v158","v159","v160","v161","v162","v163","v164","v165","v166","v167","v168","v169","v170","v171","v172","v173","v174","v175","v176","v177","v178","v179","v180","v181","v182","v183","v184","v185","v186","v187","v188","v189","v190","v191","v192","v193","v194","v195","v196","v197","v198","v199","v200","v201","v202","v203","v204","v205","v206","v207","v208","v209","v210","v211","v212","v213","v214","v215","v216","v217","v218","v219","v220","v221","v222","v223","v224","v225","v226","v227","v228","v229","v230","v231","v232","v233","v234","v235","v236","v237","v238","v239","v240","v241","v242","v243","v244","v245","v246","v247","v248","v249","v250","v251","v252","v253","v254","v255","s8","s9","s10","s11","s12","s13","s14","s15","s16","s17","s18","s19","s20","s21","s22","s23","s24","s25","s26","s27","s28","s29","s30","s31","s36","s37","s38","s39","s40","s41","s42","s43","s44","s45","s46","s47","s48","s49","s50","s51","s52","s53","s54","s55","s56","s57","s58","s59","s60","s61","s62","s63","s64","s65","s66","s67","s68","s69","s70","s71","s72","s73","s74","s75","s76","s77","s78","s79","s80","s81","s82","s83","s84","s85","s86","s87","s88","s89","s90","s91","s92","s93","s94","s95","s96","s97","s98","s99");
}

extern "C" void kernel_launch(void* const* d_in, const int* in_sizes, int n_in, void* d_out, int out_size, void* d_ws, size_t ws_size,
                              hipStream_t stream) {
  (void)hipFuncSetAttribute((const void*)fused_main, hipFuncAttributeMaxDynamicSharedMemorySize, LDS_BYTES);
  fused_main<<<256, 512, LDS_BYTES, stream>>>(d_in[0], d_in[1], d_in[2], d_in[3], d_in[4], d_in[6], d_in[7], d_in[8], d_out, (unsigned*)d_ws);
}
```
